```python
import torch

def _softmax(x, axis=-1):
    return torch.softmax(x, dim=axis)

def _gelu(x):
    return torch.nn.functional.gelu(x, approximate='tanh')

def _silu(x):
    return torch.nn.functional.silu(x)

def _sigmoid(x):
    return torch.sigmoid(x)

def _layernorm(x, axis=-1, eps=1e-05):
    m = x.mean(dim=axis, keepdim=True)
    v = x.var(dim=axis, keepdim=True, unbiased=False)
    return (x - m) / torch.sqrt(v + eps)

def _scatter_add(tgt, idx, src, axis=0):
    return tgt.clone().index_add_(axis, idx, src)

def _segment_sum(src, seg, n):
    out = torch.zeros((n,) + src.shape[1:], dtype=src.dtype)
    return out.index_add_(0, seg, src)

def _pad(x, pw):
    flat = tuple((v for pair in reversed(pw) for v in pair))
    return torch.nn.functional.pad(x, flat)
kernel_name = 'adaLN_cond__mha_block_cached__geglu_ffn__prompt'

def setup_inputs():

    _w = lambda a: (lambda t: t.to(torch.bfloat16) if t.is_floating_point() else t)(torch.as_tensor(a))

    def _A_setup():
        B, Sq, D = (1, 2048, 512)
        H, Sk, Dh = (32, 2048, 128)
        return {'x': _w((lambda *s: torch.randn(*s, dtype=torch.bfloat16) * 0.1)(B, Sq, D)), 'k_cache': _w((lambda *s: torch.randn(*s, dtype=torch.bfloat16) * 0.8)(B, H, Sk, Dh)), 'v_cache': _w((lambda *s: torch.randn(*s, dtype=torch.bfloat16) * 1.6)(B, H, Sk, Dh)), 'wq': _w((lambda *s: torch.randn(*s, dtype=torch.bfloat16) * 0.8)(D, H * Dh) * D ** (-0.5)), 'wo': _w((lambda *s: torch.randn(*s, dtype=torch.bfloat16) * 1.6)(H * Dh, D) * (H * Dh) ** (-0.5))}
    _A_in = _A_setup()

    def _B_setup():
        B, S, D, F = (1, 2048, 512, 1792)
        return {'x': _w((lambda *s: torch.randn(*s, dtype=torch.bfloat16) * 0.1)(B, S, D)), 'w_gate': _w((lambda *s: torch.randn(*s, dtype=torch.bfloat16) * 0.1)(D, F)), 'w_up': _w((lambda *s: torch.randn(*s, dtype=torch.bfloat16) * 0.1)(D, F)), 'w_down': _w((lambda *s: torch.randn(*s, dtype=torch.bfloat16) * 0.1)(F, D))}
    _B_in = _B_setup()
    return {'A_x': _A_in['x'], 'A_k_cache': _A_in['k_cache'], 'A_v_cache': _A_in['v_cache'], 'A_wq': _A_in['wq'], 'A_wo': _A_in['wo'], 'B_x': _B_in['x'], 'B_w_gate': _B_in['w_gate'], 'B_w_up': _B_in['w_up'], 'B_w_down': _B_in['w_down'], 'c': _w((lambda *s: torch.randn(*s, dtype=torch.bfloat16) * 0.8)(1, 512) * 8.0), 'w_mod': _w((lambda *s: torch.randn(*s, dtype=torch.bfloat16) * 0.8)(512, 6 * 512) * 512 ** (-0.5) * 8.0)}

def reference(A_x, A_k_cache, A_v_cache, A_wq, A_wo, B_x, B_w_gate, B_w_up, B_w_down, c, w_mod):

    def _A_ref(x, k_cache, v_cache, wq, wo):
        B, Sq, D = x.shape
        H, Dh = (32, 128)
        q = (lambda x, *d: x.permute(*d))((x @ wq).reshape(B, Sq, H, Dh), 0, 2, 1, 3)
        scores = torch.einsum('bhqd,bhkd->bhqk', q, k_cache) * Dh ** (-0.5)
        p = _softmax(scores, axis=-1)
        o = torch.einsum('bhqk,bhkd->bhqd', p, v_cache)
        return (lambda x, *d: x.permute(*d))(o, 0, 2, 1, 3).reshape(B, Sq, H * Dh) @ wo

    def _B_ref(x, w_gate, w_up, w_down):
        return _gelu(x @ w_gate) * (x @ w_up) @ w_down
    x = A_x
    mod = (_silu(c) @ w_mod).reshape(c.shape[0], 6, 1, -1)
    sa, ca, ga, sb, cb, gb = (mod[:, 0].clone(), mod[:, 1].clone(), mod[:, 2].clone(), mod[:, 3].clone(), mod[:, 4].clone(), mod[:, 5].clone())
    h = _layernorm(x, axis=-1) * (1 + ca) + sa
    x = x + ga * _A_ref(h, A_k_cache, A_v_cache, A_wq, A_wo)
    h = _layernorm(x, axis=-1) * (1 + cb) + sb
    x = x + gb * _B_ref(h, B_w_gate, B_w_up, B_w_down)
    return x
```

```cpp
#include <hip/hip_runtime.h>
#include <hip/hip_bf16.h>
typedef unsigned short u16;
__device__ __forceinline__ float b2f(u16 v){ return __uint_as_float(((unsigned)v)<<16); }
__device__ __forceinline__ u16 f2b(float f){ __hip_bfloat16 b = __float2bfloat16(f); return *reinterpret_cast<u16*>(&b); }

__global__ void k_mod(const u16* c, const u16* wmod, float* mod){
  int j = blockIdx.x*blockDim.x+threadIdx.x; if(j>=3072) return;
  float acc=0;
  for(int k=0;k<512;++k){ float cv=b2f(c[k]); float s=cv/(1.f+__expf(-cv)); acc+= s*b2f(wmod[k*3072+j]); }
  mod[j]=acc;
}
template<bool XF32>
__global__ void k_ln(const void* xin, const float* sh, const float* sc, u16* h){
  int row=blockIdx.x; int t=threadIdx.x;
  float v[8]; float s=0;
  for(int i=0;i<8;++i){ int cidx=t*8+i; v[i]= XF32? ((const float*)xin)[row*512+cidx] : b2f(((const u16*)xin)[row*512+cidx]); s+=v[i]; }
  for(int o=32;o;o>>=1) s+=__shfl_xor(s,o);
  float m=s/512.f; float q=0; for(int i=0;i<8;++i){ float d=v[i]-m; q+=d*d; }
  for(int o=32;o;o>>=1) q+=__shfl_xor(q,o);
  float r=1.f/sqrtf(q/512.f+1e-5f);
  for(int i=0;i<8;++i){ int cidx=t*8+i; h[row*512+cidx]=f2b((v[i]-m)*r*(1.f+sc[cidx])+sh[cidx]); }
}
__global__ void k_gemm(const u16* A, const u16* B, float* C, int M, int N, int K){
  __shared__ float As[16][17], Bs[16][17];
  int tx=threadIdx.x&15, ty=threadIdx.x>>4; int r=blockIdx.y*16+ty, c=blockIdx.x*16+tx; float acc=0;
  for(int k0=0;k0<K;k0+=16){
    As[ty][tx]=b2f(A[(long)r*K+k0+tx]); Bs[ty][tx]=b2f(B[(long)(k0+ty)*N+c]); __syncthreads();
    for(int k=0;k<16;++k) acc+=As[ty][k]*Bs[k][tx]; __syncthreads();
  }
  C[(long)r*N+c]=acc;
}
__global__ void k_attn(const float* Q, const u16* Kc, const u16* Vc, u16* O){
  int q=blockIdx.x, h=blockIdx.y, t=threadIdx.x;
  __shared__ float qs[128]; __shared__ float p[2048]; __shared__ float red[256];
  if(t<128) qs[t]=b2f(f2b(Q[(long)q*4096+h*128+t])); __syncthreads();
  float mx=-1e30f;
  for(int k=t;k<2048;k+=256){ const u16* kr=Kc+((long)h*2048+k)*128; float a=0; for(int d=0;d<128;++d) a+=qs[d]*b2f(kr[d]); a*=0.08838834764831845f; p[k]=a; mx=fmaxf(mx,a);}
  red[t]=mx; __syncthreads(); for(int o=128;o;o>>=1){ if(t<o) red[t]=fmaxf(red[t],red[t+o]); __syncthreads(); } mx=red[0]; __syncthreads();
  float s=0; for(int k=t;k<2048;k+=256){ float e=__expf(p[k]-mx); p[k]=e; s+=e; }
  red[t]=s; __syncthreads(); for(int o=128;o;o>>=1){ if(t<o) red[t]+=red[t+o]; __syncthreads(); } s=red[0]; __syncthreads();
  if(t<128){ float a=0; for(int k=0;k<2048;++k) a+=p[k]*b2f(Vc[((long)h*2048+k)*128+t]); O[(long)q*4096+h*128+t]=f2b(a/s); }
}
__global__ void k_res1(const u16* x, const float* ga, const float* y, float* x1){
  int i=blockIdx.x*256+threadIdx.x; x1[i]=b2f(x[i])+ga[i&511]*y[i];
}
__global__ void k_geglu(const float* g, const float* u, u16* a){
  int i=blockIdx.x*256+threadIdx.x; float x=g[i]; float t=tanhf(0.7978845608028654f*(x+0.044715f*x*x*x)); a[i]=f2b(0.5f*x*(1.f+t)*u[i]);
}
__global__ void k_res2(const float* x1, const float* gb, const float* y, u16* out){
  int i=blockIdx.x*256+threadIdx.x; out[i]=f2b(x1[i]+gb[i&511]*y[i]);
}
extern "C" void kernel_launch(void* const* d_in, const int* in_sizes, int n_in, void* d_out, int out_size, void* d_ws, size_t ws_size, hipStream_t stream){
  const u16* x=(const u16*)d_in[0]; const u16* kc=(const u16*)d_in[1]; const u16* vc=(const u16*)d_in[2];
  const u16* wq=(const u16*)d_in[3]; const u16* wo=(const u16*)d_in[4];
  const u16* wg=(const u16*)d_in[6]; const u16* wu=(const u16*)d_in[7]; const u16* wd=(const u16*)d_in[8];
  const u16* c=(const u16*)d_in[9]; const u16* wmod=(const u16*)d_in[10];
  char* ws=(char*)d_ws;
  float* mod=(float*)ws; ws+=16384;
  u16* h=(u16*)ws; ws+=2048*512*2;
  float* Q=(float*)ws; ws+=(size_t)2048*4096*4;
  u16* O=(u16*)ws; ws+=(size_t)2048*4096*2;
  float* y=(float*)ws; ws+=2048*512*4;
  float* x1=(float*)ws; ws+=2048*512*4;
  float* g=(float*)ws; ws+=2048*1792*4;
  float* u=(float*)ws; ws+=2048*1792*4;
  u16* a=(u16*)ws; ws+=2048*1792*2;
  k_mod<<<12,256,0,stream>>>(c,wmod,mod);
  k_ln<false><<<2048,64,0,stream>>>(x,mod,mod+512,h);
  k_gemm<<<dim3(4096/16,2048/16),256,0,stream>>>(h,wq,Q,2048,4096,512);
  k_attn<<<dim3(2048,32),256,0,stream>>>(Q,kc,vc,O);
  k_gemm<<<dim3(512/16,2048/16),256,0,stream>>>(O,wo,y,2048,512,4096);
  k_res1<<<4096,256,0,stream>>>(x,mod+1024,y,x1);
  k_ln<true><<<2048,64,0,stream>>>(x1,mod+1536,mod+2048,h);
  k_gemm<<<dim3(1792/16,2048/16),256,0,stream>>>(h,wg,g,2048,1792,512);
  k_gemm<<<dim3(1792/16,2048/16),256,0,stream>>>(h,wu,u,2048,1792,512);
  k_geglu<<<2048*1792/256,256,0,stream>>>(g,u,a);
  k_gemm<<<dim3(512/16,2048/16),256,0,stream>>>(a,wd,y,2048,512,1792);
  k_res2<<<4096,256,0,stream>>>(x1,mod+2560,y,(u16*)d_out);
}
```

```cpp
#include <hip/hip_runtime.h>
#include <hip/hip_bf16.h>
typedef unsigned short u16;
typedef unsigned int u32;
typedef float f32x4 __attribute__((ext_vector_type(4)));
typedef float f32x2 __attribute__((ext_vector_type(2)));
typedef unsigned u32x4 __attribute__((ext_vector_type(4)));
typedef unsigned u32x2 __attribute__((ext_vector_type(2)));
typedef __bf16 bf16x2_t __attribute__((ext_vector_type(2)));
__device__ __forceinline__ float b2f(u16 v){ return __uint_as_float(((unsigned)v)<<16); }
__device__ __forceinline__ float blo(u32 v){ return __uint_as_float(v<<16); }
__device__ __forceinline__ float bhi(u32 v){ return __uint_as_float(v&0xffff0000u); }
__device__ __forceinline__ u32 pk2(float lo, float hi){ f32x2 v={lo,hi}; bf16x2_t b=__builtin_convertvector(v,bf16x2_t); return __builtin_bit_cast(u32,b); }
typedef _Float16 h16x2_t __attribute__((ext_vector_type(2)));
__device__ __forceinline__ u32 pkh2(float lo, float hi){ h16x2_t v={(_Float16)lo,(_Float16)hi}; return __builtin_bit_cast(u32,v); }
__device__ __forceinline__ float rbf(float f){ return blo(pk2(f,0.f)); }
__device__ __forceinline__ u16 f2b(float f){ return (u16)(pk2(f,0.f)&0xffffu); }

constexpr size_t WS_TAB  = 0;
constexpr size_t WS_XN   = 16384;
constexpr size_t WS_O    = WS_XN + 2097152;
constexpr size_t WS_X1   = WS_O + 16777216;
constexpr size_t WS_ST   = WS_X1 + 2097152;
constexpr size_t WS_ACT  = WS_ST + 262144;
constexpr size_t WS_PART = WS_ACT + 7340032;
constexpr size_t WS_H2   = WS_PART + 8388608;
constexpr size_t WS_CNT  = WS_H2 + 2097152;
constexpr size_t WS_END  = WS_CNT + 16384;
constexpr int LDS_MAIN = 163840, LDS_P3 = LDS_MAIN - 256;

constexpr float QSCALE = 0.12751743082f;

__device__ __forceinline__ int tab_idx(int k){ int s=k>>5, kk=k&31; return (s*4+((kk>>2)&3))*16 + 4*(kk>>4) + (kk&3); }
__host__ __device__ __forceinline__ size_t frag_off(int row, int k, int Kdim){
  int s=k>>5, kk=k&31; return ((size_t)((row>>4)*(Kdim>>5)+s)*64 + (row&15) + 16*((kk>>2)&3))*8 + 4*(kk>>4) + (kk&3);
}

constexpr int NGEMV = 192;
__device__ __forceinline__ float row16_sum(float x){
  x += __builtin_bit_cast(float, __builtin_amdgcn_update_dpp(0, __builtin_bit_cast(int,x), 0xB1, 0xf, 0xf, false));
  x += __builtin_bit_cast(float, __builtin_amdgcn_update_dpp(0, __builtin_bit_cast(int,x), 0x4E, 0xf, 0xf, false));
  x += __builtin_bit_cast(float, __builtin_amdgcn_update_dpp(0, __builtin_bit_cast(int,x), 0x141, 0xf, 0xf, false));
  x += __builtin_bit_cast(float, __builtin_amdgcn_update_dpp(0, __builtin_bit_cast(int,x), 0x140, 0xf, 0xf, false));
  return x;
}
__global__ __launch_bounds__(256,2) void k_prep(const u16* __restrict__ x, const u16* __restrict__ c, const u16* __restrict__ wmod,
                                              float* __restrict__ tabs, u16* __restrict__ xn, const u16* __restrict__ wq, unsigned* __restrict__ cnt,
                                              const u16* __restrict__ kc, const u16* __restrict__ vc){
  __shared__ float red[8][16];
  u32x4 PREP_KEEP = {0,0,0,0};
  const int t=threadIdx.x, lane=t&63, w=t>>6;
  { u32x4 d0; const unsigned o1 = t*16u, o2 = (blockIdx.x>=NGEMV+128 ? (blockIdx.x-NGEMV-128)*4096u + t*16u : 0x7fffff00u);
    asm volatile(
      "s_getpc_b64 s[20:21]\n"
      ".Lprep_here:\n"
      "s_and_b32 s21, s21, 0xffff\n"
      "s_mov_b32 s22, 4096\n"
      "s_mov_b32 s23, 0x20000\n"
      "buffer_load_dwordx4 %0, %1, s[20:23], 0 offen\n"
      "s_add_u32 s24, s20, .Lcp_st-.Lprep_here\n"
      "s_addc_u32 s25, s21, 0\n"
      "s_mov_b32 s26, .Lkm_end-.Lcp_st\n"
      "s_mov_b32 s27, 0x20000\n"
      "buffer_load_dwordx4 %0, %2, s[24:27], 0 offen\n"
      : "=&v"(d0) : "v"(o1), "v"(o2) : "s20","s21","s22","s23","s24","s25","s26","s27","scc","memory");
    PREP_KEEP = d0; }
  asm volatile("" :: "s"(x), "s"(c), "s"(wmod), "s"(tabs), "s"(xn), "s"(wq), "s"(kc), "s"(vc), "s"(cnt));
  if(__builtin_expect(blockIdx.x<NGEMV, 1)){
    const int j=blockIdx.x>>3, cb=(j>>2)*32+(blockIdx.x&7)*4+(j&3);
    const int cg=lane>>5, r0=w*32+(lane&31), col0=cb*16+cg*8;
    u32x4 wv[4]; float cv[4]; u16 cr[4];
#pragma unroll
    for(int i=0;i<4;++i) wv[i]=*(const u32x4*)(wmod+(size_t)(r0+128*i)*3072+col0);
#pragma unroll
    for(int i=0;i<4;++i) cr[i]=c[r0+128*i];
    __builtin_amdgcn_sched_barrier(0);
#pragma unroll
    for(int i=0;i<4;++i) cv[i]=b2f(cr[i]);
    float acc[8]={0,0,0,0,0,0,0,0};
#pragma unroll
    for(int i=0;i<4;++i){
      float s=rbf(cv[i]*__builtin_amdgcn_rcpf(1.f+__expf(-cv[i])));
#pragma unroll
      for(int d=0;d<4;++d){ acc[2*d]+=s*blo(wv[i][d]); acc[2*d+1]+=s*bhi(wv[i][d]); }
    }
#pragma unroll
    for(int jj=0;jj<8;++jj) acc[jj]=row16_sum(acc[jj]);
    if((lane&15)==0){
#pragma unroll
      for(int jj=0;jj<8;++jj) red[w*2+((lane>>4)&1)][cg*8+jj]=acc[jj];
    }
    __syncthreads();
    if(t<16){
      float m=rbf(((red[0][t]+red[1][t])+(red[2][t]+red[3][t]))+((red[4][t]+red[5][t])+(red[6][t]+red[7][t])));
      int col=cb*16+t, part=col>>9, k=col&511, ti=tab_idx(k);
      const bool scale=(part==1||part==4);
      float val = scale ? rbf(1.f+m) : m;
      if(part<2) val*=QSCALE;
      const int idx = part==2 ? 2048+k : part==5 ? 2560+k : (part>=3?1024:0)+ti+(scale?0:8);
      tabs[idx]=val;
    }
  } else if(blockIdx.x<NGEMV+128){
    if(blockIdx.x-NGEMV<10) cnt[(blockIdx.x-NGEMV)*256+t]=0u;
    const int row=(blockIdx.x-NGEMV)*16 + w*4 + (lane>>4), i16=lane&15;
    u32x4 xv[4];
#pragma unroll
    for(int m=0;m<4;++m) xv[m]=*(const u32x4*)(x+(size_t)row*512+(i16+16*m)*8);
    float v[32]; float s=0;
#pragma unroll
    for(int m=0;m<4;++m)
#pragma unroll
      for(int d=0;d<4;++d){ v[8*m+2*d]=blo(xv[m][d]); v[8*m+2*d+1]=bhi(xv[m][d]); s+=v[8*m+2*d]+v[8*m+2*d+1]; }
    s=row16_sum(s);
    const float mean=s*(1.f/512.f); float q=0;
#pragma unroll
    for(int j=0;j<32;++j){ v[j]-=mean; q+=v[j]*v[j]; }
    q=row16_sum(q);
    const float r=1.f/sqrtf(q*(1.f/512.f)+1e-5f);
#pragma unroll
    for(int m=0;m<4;++m){
      const int k=(i16+16*m)*8;
      u32x2 a={pkh2(v[8*m]*r,v[8*m+1]*r),pkh2(v[8*m+2]*r,v[8*m+3]*r)}, b={pkh2(v[8*m+4]*r,v[8*m+5]*r),pkh2(v[8*m+6]*r,v[8*m+7]*r)};
      *(u32x2*)(xn+frag_off(row,k,512))=a;
      *(u32x2*)(xn+frag_off(row,k+4,512))=b;
    }
  } else {
    const int pb = blockIdx.x-NGEMV-128;
    const u32x4* src = pb<128 ? (const u32x4*)wq + (size_t)pb*2048 + t
                              : (const u32x4*)(((pb-128)&1) ? vc : kc) + (size_t)((pb-128)>>1)*32768 + t;
    u32x4 a[8];
#pragma unroll
    for(int i=0;i<8;++i) a[i]=src[256*i];
#pragma unroll
    for(int i=0;i<8;++i) asm volatile("" :: "v"(a[i]));
  }
  asm volatile("s_waitcnt vmcnt(0)" : "+v"(PREP_KEEP) :: "memory");
}
#define PREP_GRID 512
typedef float f32x16 __attribute__((ext_vector_type(16)));
constexpr int RED_PITCH = 272;
__device__ __forceinline__ void red_write(char* lds, int w, int lane, const f32x16& a, int tb){
  const int q = tb*16 + (lane&15), g = lane>>4;
  char* base = lds + w*(64*RED_PITCH) + q*RED_PITCH + 16*g;
#pragma unroll
  for(int nb=0; nb<4; ++nb){ f32x4 v = {a[4*nb],a[4*nb+1],a[4*nb+2],a[4*nb+3]}; *(f32x4*)(base + 64*nb) = v; }
}
__device__ __forceinline__ f32x4 red_read(const char* lds, int q, int n){
  const char* b = lds + q*RED_PITCH + n*4;
  f32x4 v = *(const f32x4*)b;
#pragma unroll
  for(int w=1; w<4; ++w) v += *(const f32x4*)(b + w*(64*RED_PITCH));
  return v;
}
typedef __amdgpu_buffer_rsrc_t rsrc_t;
__device__ __forceinline__ rsrc_t mk_rsrc(const void* p, unsigned bytes){ return __builtin_amdgcn_make_buffer_rsrc((void*)p, (short)0, (int)bytes, 0x00020000); }
__device__ __forceinline__ void st16(rsrc_t r, unsigned off, u32x4 v){ __builtin_amdgcn_raw_buffer_store_b128(v, r, (int)off, 0, 16); }
__device__ __forceinline__ u32x4 ld16(rsrc_t r, unsigned off){ return __builtin_amdgcn_raw_buffer_load_b128(r, (int)off, 0, 16); }
__device__ __forceinline__ u32x2 ld8(rsrc_t r, unsigned off){ return __builtin_amdgcn_raw_buffer_load_b64(r, (int)off, 0, 16); }
__device__ __forceinline__ void frag_store(const f32x16& a, rsrc_t r, unsigned off){
#pragma unroll
  for(int s=0;s<2;++s){ u32x4 o;
#pragma unroll
    for(int d=0;d<4;++d) o[d] = pk2(a[8*s+2*d], a[8*s+2*d+1]);
    st16(r, off + 1024*s, o); }
}
__device__ __forceinline__ void publish(unsigned* cnt, bool leader){
  asm volatile("s_waitcnt vmcnt(0)" ::: "memory");
  __builtin_amdgcn_s_barrier();
  asm volatile("" ::: "memory");
  if(leader) (void)__hip_atomic_fetch_add(cnt, 1u, __ATOMIC_RELAXED, __HIP_MEMORY_SCOPE_AGENT);
}
__device__ __forceinline__ float geglu1(float g, float u){
  const float t = __builtin_fmaf(g*g, -0.1029432397f, -2.302208198f);
  const float e = __builtin_amdgcn_exp2f(g*t);
  return g*__builtin_amdgcn_rcpf(1.f+e)*u;
}
__device__ __forceinline__ u32x2 geglu4(const f32x16& a, int tb){
  u32x2 o;
  o[0] = pk2(geglu1(a[4*tb],a[8+4*tb]), geglu1(a[4*tb+1],a[8+4*tb+1]));
  o[1] = pk2(geglu1(a[4*tb+2],a[8+4*tb+2]), geglu1(a[4*tb+3],a[8+4*tb+3]));
  return o;
}
__device__ __forceinline__ void st8(rsrc_t r, unsigned off, u32x2 v){ __builtin_amdgcn_raw_buffer_store_b64(v, r, (int)off, 0, 16); }
__device__ __forceinline__ void geglu_pair(const f32x16& a, const f32x16& b, rsrc_t r, unsigned off){
#pragma unroll
  for(int tb=0; tb<2; ++tb){ const u32x2 x = geglu4(a,tb), y = geglu4(b,tb); u32x4 o = {x[0],x[1],y[0],y[1]}; st16(r, off + tb*57344, o); }
}
__device__ __forceinline__ void geglu_single(const f32x16& a, rsrc_t r, unsigned off){
#pragma unroll
  for(int tb=0; tb<2; ++tb) st8(r, off + tb*57344, geglu4(a,tb));
}
template<int IMM> __device__ __forceinline__ void dma_lds(unsigned m0v, unsigned voff, rsrc_t r, unsigned soff){
  asm volatile("s_mov_b32 m0, %0\n\ts_nop 0\n\tbuffer_load_dwordx4 %1, %2, %3 offen offset:%4 lds" :: "s"(m0v), "v"(voff), "s"(r), "s"(soff), "i"(IMM) : "memory");
}
__device__ __forceinline__ void down_stage(unsigned m0b, unsigned v0, rsrc_t r, int st){
#pragma unroll
  for(int hh=0; hh<2; ++hh){
    const unsigned m = m0b + 4096*hh, so = st*65536 + 32768*hh;
    dma_lds<0>(m, v0, r, so); dma_lds<1024>(m, v0 + 7168, r, so); dma_lds<2048>(m, v0 + 14336, r, so); dma_lds<3072>(m, v0 + 21504, r, so);
  }
}
__device__ __forceinline__ unsigned lane_now(){ unsigned l; asm volatile("v_mbcnt_lo_u32_b32 %0, -1, 0\n\tv_mbcnt_hi_u32_b32 %0, -1, %0" : "=v"(l)); return l; }

__global__ __launch_bounds__(256,1) void k_main(const u16* __restrict__ x, const u16* __restrict__ kc, const u16* __restrict__ vc,
    const u16* __restrict__ wq, const u16* __restrict__ wo, const u16* __restrict__ wg, const u16* __restrict__ wu, const u16* __restrict__ wd,
    char* __restrict__ ws, u16* __restrict__ out){
  extern __shared__ __attribute__((aligned(16))) char lds[];
  unsigned tid0 = threadIdx.x;
  const unsigned bid = blockIdx.x, ldsb = (unsigned)(size_t)lds;
  const unsigned w = __builtin_amdgcn_readfirstlane(tid0>>6);
  const float* tabs = (const float*)(ws + WS_TAB);
  unsigned* cnt = (unsigned*)(ws + WS_CNT);
  {
    const void* xn = ws + WS_XN; void* ofrag = ws + WS_O;
    asm volatile(
"v_lshrrev_b32 v224, 6, v0\n"
"v_and_b32 v228, 63, v0\n"
"v_readfirstlane_b32 s44, v224\n"
"v_lshrrev_b32 v230, 5, v228\n"
"s_and_b32 s57, s16, 7\n"
"s_lshr_b32 s58, s16, 3\n"
"s_and_b32 s59, s58, 3\n"
"s_lshl_b32 s57, s57, 2\n"
"s_add_u32 s45, s57, s59\n"
"s_lshr_b32 s46, s58, 2\n"
"v_lshlrev_b32 v246, 4, v228\n"
"v_bfe_u32 v229, v228, 2, 3\n"
"v_and_b32 v225, 3, v228\n"
"v_lshrrev_b32 v226, 2, v229\n"
"v_lshlrev_b32 v227, 1, v226\n"
"v_xor_b32 v227, v227, v225\n"
"v_lshlrev_b32 v224, 6, v230\n"
"v_lshl_add_u32 v227, v227, 4, v224\n"
"v_lshl_add_u32 v231, v229, 13, v227\n"
"v_add_u32 v242, 0x400, v231\n"
"v_add_u32 v243, 0x80, v231\n"
"v_add_u32 v244, 0xfc00, v231\n"
"v_add_u32 v245, 0xf880, v231\n"
"v_lshl_add_u32 v227, v229, 8, v227\n"
"v_add_u32 v240, 0x400, v227\n"
"v_add_u32 v241, 0x80, v227\n"
"v_add_u32 v248, 0x4000, v246\n"
"v_add_u32 v249, 0x8000, v246\n"
"v_add_u32 v250, 0xc000, v246\n"
"s_lshl_b32 s57, s45, 8\n"
"s_sub_u32 s58, 0x400400, s57\n"
"s_add_u32 s28, s8, s57\n"
"s_addc_u32 s29, s9, 0\n"
"s_sub_u32 s28, s28, 0x400\n"
"s_subb_u32 s29, s29, 0\n"
"s_and_b32 s29, s29, 0xffff\n"
"s_mov_b32 s30, s58\n"
"s_mov_b32 s31, 0x20000\n"
"s_lshl_b32 s57, s46, 2\n"
"s_add_u32 s59, s57, s44\n"
"s_lshl_b32 s57, s59, 16\n"
"s_add_u32 s64, s4, s57\n"
"s_addc_u32 s65, s5, 0\n"
"s_and_b32 s65, s65, 0xffff\n"
"s_mov_b32 s66, 0x10000\n"
"s_mov_b32 s67, 0x20000\n"
"s_add_u32 s40, s6, 0\n"
"s_addc_u32 s41, s7, 0\n"
"s_and_b32 s41, s41, 0xffff\n"
"s_mov_b32 s42, 0x1000\n"
"s_mov_b32 s43, 0x20000\n"
"s_lshl_b32 s57, s44, 12\n"
"s_add_u32 s50, s17, s57\n"
"s_add_u32 s51, s50, 0x10000\n"
"s_lshl_b32 s49, s44, 17\n"
"s_mov_b32 s56, 0\n"
"s_lshl_b32 s57, s44, 10\n"
"s_add_u32 s58, s17, 0x20000\n"
"s_add_u32 m0, s58, s57\n"
"s_nop 0\n"
"buffer_load_dwordx4 v246, s[40:43], s57 offen offset:0 lds\n"
"s_add_u32 m0, s51, 0\n"
"s_nop 0\n"
"buffer_load_dwordx4 v242, s[28:31], s49 offen offset:0 lds\n"
"buffer_load_dwordx4 v243, s[28:31], s49 offen offset:1024 lds\n"
"buffer_load_dwordx4 v244, s[28:31], s49 offen offset:2048 lds\n"
"buffer_load_dwordx4 v245, s[28:31], s49 offen offset:3072 lds\n"
"s_add_u32 s49, s49, 0x80000\n"
"buffer_load_dwordx4 v[128:131], v246, s[64:67], s56 offen offset:0\n"
"buffer_load_dwordx4 v[132:135], v248, s[64:67], s56 offen offset:0\n"
"buffer_load_dwordx4 v[136:139], v249, s[64:67], s56 offen offset:0\n"
"buffer_load_dwordx4 v[140:143], v250, s[64:67], s56 offen offset:0\n"
"buffer_load_dwordx4 v[144:147], v246, s[64:67], s56 offen offset:1024\n"
"buffer_load_dwordx4 v[148:151], v248, s[64:67], s56 offen offset:1024\n"
"buffer_load_dwordx4 v[152:155], v249, s[64:67], s56 offen offset:1024\n"
"buffer_load_dwordx4 v[156:159], v250, s[64:67], s56 offen offset:1024\n"
"s_add_u32 m0, s51, 0x4000\n"
"s_nop 0\n"
"buffer_load_dwordx4 v242, s[28:31], s49 offen offset:0 lds\n"
"buffer_load_dwordx4 v243, s[28:31], s49 offen offset:1024 lds\n"
"buffer_load_dwordx4 v244, s[28:31], s49 offen offset:2048 lds\n"
"buffer_load_dwordx4 v245, s[28:31], s49 offen offset:3072 lds\n"
"s_add_u32 s49, s49, 0x80000\n"
"s_getpc_b64 s[68:69]\n"
".Lcp_st:\n"
"s_and_b32 s69, s69, 0xffff\n"
"s_mov_b32 s70, .Lkm_end-.Lcp_st\n"
"s_mov_b32 s71, 0x20000\n"
"s_lshr_b32 s59, s16, 3\n"
"s_lshl_b32 s59, s59, 2\n"
"s_add_u32 s59, s59, s44\n"
"s_lshl_b32 s59, s59, 10\n"
"v_lshlrev_b32 v7, 4, v228\n"
"v_add_u32 v7, s59, v7\n"
"buffer_load_dwordx4 a[0:3], v7, s[68:71], 0 offen offset:0\n"
"v_and_b32 v225, 3, v228\n"
"v_lshrrev_b32 v226, 2, v229\n"
"v_lshlrev_b32 v224, 6, v230\n"
"v_sub_u32 v227, 4, v226\n"
"v_and_b32 v227, 3, v227\n"
"v_xor_b32 v227, v227, v225\n"
"v_lshl_add_u32 v227, v227, 4, v224\n"
"v_lshl_add_u32 v227, v229, 8, v227\n"
"v_add_u32 v236, 0x400, v227\n"
"v_add_u32 v237, 0x80, v227\n"
"v_sub_u32 v227, 2, v226\n"
"v_and_b32 v227, 3, v227\n"
"v_xor_b32 v227, v227, v225\n"
"v_lshl_add_u32 v227, v227, 4, v224\n"
"v_lshl_add_u32 v227, v229, 8, v227\n"
"v_add_u32 v238, 0x400, v227\n"
"v_add_u32 v239, 0x80, v227\n"
"v_lshrrev_b32 v231, 4, v228\n"
"v_bfe_u32 v224, v228, 3, 1\n"
"v_lshlrev_b32 v224, 11, v224\n"
"v_and_b32 v226, 7, v228\n"
"v_lshl_add_u32 v224, v226, 6, v224\n"
"v_bfe_u32 v226, v228, 2, 2\n"
"v_sub_u32 v226, 4, v226\n"
"v_and_b32 v226, 3, v226\n"
"v_xor_b32 v226, v226, v231\n"
"v_lshl_add_u32 v224, v226, 4, v224\n"
"v_add_u32 v232, s17, v224\n"
"v_lshlrev_b32 v224, 11, v230\n"
"v_lshl_add_u32 v224, v229, 6, v224\n"
"v_add_u32 v224, s17, v224\n"
"v_add_u32 v224, 0x10000, v224\n"
"v_bfe_u32 v226, v228, 4, 1\n"
"v_lshl_add_u32 v227, v225, 3, v224\n"
"v_lshl_add_u32 v233, v226, 5, v227\n"
"v_xor_b32 v234, 32, v233\n"
"v_lshlrev_b32 v227, 4, v225\n"
"v_lshlrev_b32 v226, 5, v226\n"
"v_xor_b32 v227, v227, v226\n"
"v_add_u32 v235, v224, v227\n"
"v_lshlrev_b32 v224, 6, v231\n"
"v_add_u32 v224, s17, v224\n"
"v_add_u32 v247, 0x20000, v224\n"
"s_lshl_b32 s57, s45, 19\n"
"s_add_u32 s20, s10, s57\n"
"s_addc_u32 s21, s11, 0\n"
"s_sub_u32 s20, s20, 0x400\n"
"s_subb_u32 s21, s21, 0\n"
"s_and_b32 s21, s21, 0xffff\n"
"s_mov_b32 s22, 0x80400\n"
"s_mov_b32 s23, 0x20000\n"
"s_add_u32 s24, s12, s57\n"
"s_addc_u32 s25, s13, 0\n"
"s_sub_u32 s24, s24, 0x400\n"
"s_subb_u32 s25, s25, 0\n"
"s_and_b32 s25, s25, 0xffff\n"
"s_mov_b32 s26, 0x80400\n"
"s_mov_b32 s27, 0x20000\n"
"s_lshl_b32 s57, s46, 2\n"
"s_add_u32 s59, s57, s44\n"
"s_lshl_b32 s57, s59, 19\n"
"s_add_u32 s36, s14, s57\n"
"s_addc_u32 s37, s15, 0\n"
"s_and_b32 s37, s37, 0xffff\n"
"s_mov_b32 s38, 0x80000\n"
"s_mov_b32 s39, 0x20000\n"
"s_lshl_b32 s57, s44, 12\n"
"s_mov_b32 s47, s57\n"
"s_mov_b32 s48, s57\n"
"s_mov_b32 s55, 0x4000\n"
"s_mov_b32 s61, 0x4000\n"
"s_mov_b32 s53, 0x67800000\n"
"s_mov_b32 s54, 0x42600000\n"
"s_mov_b32 s52, 7\n"
"s_waitcnt vmcnt(13)\n"
"s_barrier\n"
"ds_read_b128 v[192:195], v247 offset:0\n"
"ds_read_b128 v[196:199], v247 offset:16\n"
"ds_read_b128 v[200:203], v247 offset:32\n"
"ds_read_b128 v[204:207], v247 offset:48\n"
"ds_read_b128 v[208:211], v247 offset:256\n"
"ds_read_b128 v[212:215], v247 offset:272\n"
"ds_read_b128 v[216:219], v247 offset:288\n"
"ds_read_b128 v[220:223], v247 offset:304\n"
"ds_read_b64_tr_b16 a[192:193], v235 offset:0\n"
"ds_read_b64_tr_b16 a[194:195], v235 offset:4096\n"
"ds_read_b64_tr_b16 a[196:197], v235 offset:8\n"
"ds_read_b64_tr_b16 a[198:199], v235 offset:4104\n"
"ds_read_b64_tr_b16 a[200:201], v235 offset:512\n"
"ds_read_b64_tr_b16 a[202:203], v235 offset:4608\n"
"ds_read_b64_tr_b16 a[204:205], v235 offset:520\n"
"ds_read_b64_tr_b16 a[206:207], v235 offset:4616\n"
"ds_read_b64_tr_b16 a[208:209], v235 offset:1024\n"
"ds_read_b64_tr_b16 a[210:211], v235 offset:5120\n"
"ds_read_b64_tr_b16 a[212:213], v235 offset:1032\n"
"ds_read_b64_tr_b16 a[214:215], v235 offset:5128\n"
"ds_read_b64_tr_b16 a[216:217], v235 offset:1536\n"
"ds_read_b64_tr_b16 a[218:219], v235 offset:5632\n"
"ds_read_b64_tr_b16 a[220:221], v235 offset:1544\n"
"ds_read_b64_tr_b16 a[222:223], v235 offset:5640\n"
"buffer_load_dwordx4 v[160:163], v246, s[64:67], s56 offen offset:2048\n"
"buffer_load_dwordx4 v[164:167], v248, s[64:67], s56 offen offset:2048\n"
"buffer_load_dwordx4 v[168:171], v249, s[64:67], s56 offen offset:2048\n"
"buffer_load_dwordx4 v[172:175], v250, s[64:67], s56 offen offset:2048\n"
"buffer_load_dwordx4 v[176:179], v246, s[64:67], s56 offen offset:3072\n"
"buffer_load_dwordx4 v[180:183], v248, s[64:67], s56 offen offset:3072\n"
"buffer_load_dwordx4 v[184:187], v249, s[64:67], s56 offen offset:3072\n"
"buffer_load_dwordx4 v[188:191], v250, s[64:67], s56 offen offset:3072\n"
"s_add_u32 s56, s56, 0x1000\n"
"s_waitcnt vmcnt(17)\n"
"s_waitcnt lgkmcnt(15)\n"
"v_fma_mix_f32 v224, v128, v192, v200 op_sel:[0,0,0] op_sel_hi:[1,0,0]\n"
"v_fma_mix_f32 v225, v128, v193, v201 op_sel:[1,0,0] op_sel_hi:[1,0,0]\n"
"v_cvt_pk_bf16_f32 v128, v224, v225\n"
"v_fma_mix_f32 v226, v129, v194, v202 op_sel:[0,0,0] op_sel_hi:[1,0,0]\n"
"v_fma_mix_f32 v227, v129, v195, v203 op_sel:[1,0,0] op_sel_hi:[1,0,0]\n"
"v_cvt_pk_bf16_f32 v129, v226, v227\n"
"v_fma_mix_f32 v224, v130, v196, v204 op_sel:[0,0,0] op_sel_hi:[1,0,0]\n"
"v_fma_mix_f32 v225, v130, v197, v205 op_sel:[1,0,0] op_sel_hi:[1,0,0]\n"
"v_cvt_pk_bf16_f32 v130, v224, v225\n"
"v_fma_mix_f32 v226, v131, v198, v206 op_sel:[0,0,0] op_sel_hi:[1,0,0]\n"
"v_fma_mix_f32 v227, v131, v199, v207 op_sel:[1,0,0] op_sel_hi:[1,0,0]\n"
"v_cvt_pk_bf16_f32 v131, v226, v227\n"
"v_fma_mix_f32 v224, v132, v192, v200 op_sel:[0,0,0] op_sel_hi:[1,0,0]\n"
"v_fma_mix_f32 v225, v132, v193, v201 op_sel:[1,0,0] op_sel_hi:[1,0,0]\n"
"v_cvt_pk_bf16_f32 v132, v224, v225\n"
"v_fma_mix_f32 v226, v133, v194, v202 op_sel:[0,0,0] op_sel_hi:[1,0,0]\n"
"v_fma_mix_f32 v227, v133, v195, v203 op_sel:[1,0,0] op_sel_hi:[1,0,0]\n"
"v_cvt_pk_bf16_f32 v133, v226, v227\n"
"v_fma_mix_f32 v224, v134, v196, v204 op_sel:[0,0,0] op_sel_hi:[1,0,0]\n"
"v_fma_mix_f32 v225, v134, v197, v205 op_sel:[1,0,0] op_sel_hi:[1,0,0]\n"
"v_cvt_pk_bf16_f32 v134, v224, v225\n"
"v_fma_mix_f32 v226, v135, v198, v206 op_sel:[0,0,0] op_sel_hi:[1,0,0]\n"
"v_fma_mix_f32 v227, v135, v199, v207 op_sel:[1,0,0] op_sel_hi:[1,0,0]\n"
"v_cvt_pk_bf16_f32 v135, v226, v227\n"
"v_fma_mix_f32 v224, v136, v192, v200 op_sel:[0,0,0] op_sel_hi:[1,0,0]\n"
"v_fma_mix_f32 v225, v136, v193, v201 op_sel:[1,0,0] op_sel_hi:[1,0,0]\n"
"v_cvt_pk_bf16_f32 v136, v224, v225\n"
"v_fma_mix_f32 v226, v137, v194, v202 op_sel:[0,0,0] op_sel_hi:[1,0,0]\n"
"v_fma_mix_f32 v227, v137, v195, v203 op_sel:[1,0,0] op_sel_hi:[1,0,0]\n"
"v_cvt_pk_bf16_f32 v137, v226, v227\n"
"v_fma_mix_f32 v224, v138, v196, v204 op_sel:[0,0,0] op_sel_hi:[1,0,0]\n"
"v_fma_mix_f32 v225, v138, v197, v205 op_sel:[1,0,0] op_sel_hi:[1,0,0]\n"
"v_cvt_pk_bf16_f32 v138, v224, v225\n"
"v_fma_mix_f32 v226, v139, v198, v206 op_sel:[0,0,0] op_sel_hi:[1,0,0]\n"
"v_fma_mix_f32 v227, v139, v199, v207 op_sel:[1,0,0] op_sel_hi:[1,0,0]\n"
"v_cvt_pk_bf16_f32 v139, v226, v227\n"
"v_fma_mix_f32 v224, v140, v192, v200 op_sel:[0,0,0] op_sel_hi:[1,0,0]\n"
"v_fma_mix_f32 v225, v140, v193, v201 op_sel:[1,0,0] op_sel_hi:[1,0,0]\n"
"v_cvt_pk_bf16_f32 v140, v224, v225\n"
"v_fma_mix_f32 v226, v141, v194, v202 op_sel:[0,0,0] op_sel_hi:[1,0,0]\n"
"v_fma_mix_f32 v227, v141, v195, v203 op_sel:[1,0,0] op_sel_hi:[1,0,0]\n"
"v_cvt_pk_bf16_f32 v141, v226, v227\n"
"v_fma_mix_f32 v224, v142, v196, v204 op_sel:[0,0,0] op_sel_hi:[1,0,0]\n"
"v_fma_mix_f32 v225, v142, v197, v205 op_sel:[1,0,0] op_sel_hi:[1,0,0]\n"
"v_cvt_pk_bf16_f32 v142, v224, v225\n"
"v_fma_mix_f32 v226, v143, v198, v206 op_sel:[0,0,0] op_sel_hi:[1,0,0]\n"
"v_fma_mix_f32 v227, v143, v199, v207 op_sel:[1,0,0] op_sel_hi:[1,0,0]\n"
"v_cvt_pk_bf16_f32 v143, v226, v227\n"
"s_waitcnt lgkmcnt(0)\n"
"s_waitcnt vmcnt(13)\n"
"v_mfma_f32_16x16x32_bf16 v[0:3], a[192:195], v[128:131], 0\n"
"ds_read_b64_tr_b16 a[224:225], v235 offset:8192\n"
"v_fma_mix_f32 v224, v144, v208, v216 op_sel:[0,0,0] op_sel_hi:[1,0,0]\n"
"v_fma_mix_f32 v225, v144, v209, v217 op_sel:[1,0,0] op_sel_hi:[1,0,0]\n"
"v_mfma_f32_16x16x32_bf16 v[32:35], a[192:195], v[132:135], 0\n"
"ds_read_b64_tr_b16 a[226:227], v235 offset:12288\n"
"v_cvt_pk_bf16_f32 v144, v224, v225\n"
"v_fma_mix_f32 v226, v145, v210, v218 op_sel:[0,0,0] op_sel_hi:[1,0,0]\n"
"v_mfma_f32_16x16x32_bf16 v[64:67], a[192:195], v[136:139], 0\n"
"ds_read_b128 v[192:195], v247 offset:512\n"
"v_fma_mix_f32 v227, v145, v211, v219 op_sel:[1,0,0] op_sel_hi:[1,0,0]\n"
"v_mfma_f32_16x16x32_bf16 v[96:99], a[192:195], v[140:143], 0\n"
"ds_read_b64_tr_b16 a[228:229], v235 offset:8200\n"
"v_cvt_pk_bf16_f32 v145, v226, v227\n"
"v_fma_mix_f32 v224, v146, v212, v220 op_sel:[0,0,0] op_sel_hi:[1,0,0]\n"
"v_mfma_f32_16x16x32_bf16 v[4:7], a[196:199], v[128:131], 0\n"
"ds_read_b64_tr_b16 a[230:231], v235 offset:12296\n"
"v_fma_mix_f32 v225, v146, v213, v221 op_sel:[1,0,0] op_sel_hi:[1,0,0]\n"
"v_mfma_f32_16x16x32_bf16 v[36:39], a[196:199], v[132:135], 0\n"
"ds_read_b128 v[196:199], v247 offset:528\n"
"v_cvt_pk_bf16_f32 v146, v224, v225\n"
"v_fma_mix_f32 v226, v147, v214, v222 op_sel:[0,0,0] op_sel_hi:[1,0,0]\n"
"v_mfma_f32_16x16x32_bf16 v[68:71], a[196:199], v[136:139], 0\n"
"ds_read_b64_tr_b16 a[232:233], v235 offset:8704\n"
"v_fma_mix_f32 v227, v147, v215, v223 op_sel:[1,0,0] op_sel_hi:[1,0,0]\n"
"v_cvt_pk_bf16_f32 v147, v226, v227\n"
"v_mfma_f32_16x16x32_bf16 v[100:103], a[196:199], v[140:143], 0\n"
"ds_read_b64_tr_b16 a[234:235], v235 offset:12800\n"
"v_fma_mix_f32 v224, v148, v208, v216 op_sel:[0,0,0] op_sel_hi:[1,0,0]\n"
"v_mfma_f32_16x16x32_bf16 v[8:11], a[200:203], v[128:131], 0\n"
"ds_read_b128 v[200:203], v247 offset:544\n"
"v_fma_mix_f32 v225, v148, v209, v217 op_sel:[1,0,0] op_sel_hi:[1,0,0]\n"
"v_cvt_pk_bf16_f32 v148, v224, v225\n"
"v_mfma_f32_16x16x32_bf16 v[40:43], a[200:203], v[132:135], 0\n"
"ds_read_b64_tr_b16 a[236:237], v235 offset:8712\n"
"v_fma_mix_f32 v226, v149, v210, v218 op_sel:[0,0,0] op_sel_hi:[1,0,0]\n"
"v_mfma_f32_16x16x32_bf16 v[72:75], a[200:203], v[136:139], 0\n"
"ds_read_b64_tr_b16 a[238:239], v235 offset:12808\n"
"v_fma_mix_f32 v227, v149, v211, v219 op_sel:[1,0,0] op_sel_hi:[1,0,0]\n"
"v_cvt_pk_bf16_f32 v149, v226, v227\n"
"v_mfma_f32_16x16x32_bf16 v[104:107], a[200:203], v[140:143], 0\n"
"ds_read_b128 v[204:207], v247 offset:560\n"
"v_fma_mix_f32 v224, v150, v212, v220 op_sel:[0,0,0] op_sel_hi:[1,0,0]\n"
"v_fma_mix_f32 v225, v150, v213, v221 op_sel:[1,0,0] op_sel_hi:[1,0,0]\n"
"v_mfma_f32_16x16x32_bf16 v[12:15], a[204:207], v[128:131], 0\n"
"ds_read_b64_tr_b16 a[240:241], v235 offset:9216\n"
"v_cvt_pk_bf16_f32 v150, v224, v225\n"
"v_mfma_f32_16x16x32_bf16 v[44:47], a[204:207], v[132:135], 0\n"
"ds_read_b64_tr_b16 a[242:243], v235 offset:13312\n"
"v_fma_mix_f32 v226, v151, v214, v222 op_sel:[0,0,0] op_sel_hi:[1,0,0]\n"
"v_fma_mix_f32 v227, v151, v215, v223 op_sel:[1,0,0] op_sel_hi:[1,0,0]\n"
"v_mfma_f32_16x16x32_bf16 v[76:79], a[204:207], v[136:139], 0\n"
"v_cvt_pk_bf16_f32 v151, v226, v227\n"
"v_mfma_f32_16x16x32_bf16 v[108:111], a[204:207], v[140:143], 0\n"
"ds_read_b64_tr_b16 a[244:245], v235 offset:9224\n"
"v_fma_mix_f32 v224, v152, v208, v216 op_sel:[0,0,0] op_sel_hi:[1,0,0]\n"
"v_fma_mix_f32 v225, v152, v209, v217 op_sel:[1,0,0] op_sel_hi:[1,0,0]\n"
"v_mfma_f32_16x16x32_bf16 v[16:19], a[208:211], v[128:131], 0\n"
"ds_read_b64_tr_b16 a[246:247], v235 offset:13320\n"
"v_cvt_pk_bf16_f32 v152, v224, v225\n"
"v_fma_mix_f32 v226, v153, v210, v218 op_sel:[0,0,0] op_sel_hi:[1,0,0]\n"
"v_mfma_f32_16x16x32_bf16 v[48:51], a[208:211], v[132:135], 0\n"
"s_add_u32 m0, s51, 0x8000\n"
"v_fma_mix_f32 v227, v153, v211, v219 op_sel:[1,0,0] op_sel_hi:[1,0,0]\n"
"v_mfma_f32_16x16x32_bf16 v[80:83], a[208:211], v[136:139], 0\n"
"buffer_load_dwordx4 v242, s[28:31], s49 offen offset:0 lds\n"
"ds_read_b64_tr_b16 a[248:249], v235 offset:9728\n"
"v_cvt_pk_bf16_f32 v153, v226, v227\n"
"v_fma_mix_f32 v224, v154, v212, v220 op_sel:[0,0,0] op_sel_hi:[1,0,0]\n"
"v_mfma_f32_16x16x32_bf16 v[112:115], a[208:211], v[140:143], 0\n"
"ds_read_b64_tr_b16 a[250:251], v235 offset:13824\n"
"v_fma_mix_f32 v225, v154, v213, v221 op_sel:[1,0,0] op_sel_hi:[1,0,0]\n"
"v_mfma_f32_16x16x32_bf16 v[20:23], a[212:215], v[128:131], 0\n"
"v_cvt_pk_bf16_f32 v154, v224, v225\n"
"v_fma_mix_f32 v226, v155, v214, v222 op_sel:[0,0,0] op_sel_hi:[1,0,0]\n"
"v_mfma_f32_16x16x32_bf16 v[52:55], a[212:215], v[132:135], 0\n"
"buffer_load_dwordx4 v243, s[28:31], s49 offen offset:1024 lds\n"
"ds_read_b64_tr_b16 a[252:253], v235 offset:9736\n"
"v_fma_mix_f32 v227, v155, v215, v223 op_sel:[1,0,0] op_sel_hi:[1,0,0]\n"
"v_cvt_pk_bf16_f32 v155, v226, v227\n"
"v_mfma_f32_16x16x32_bf16 v[84:87], a[212:215], v[136:139], 0\n"
"ds_read_b64_tr_b16 a[254:255], v235 offset:13832\n"
"v_fma_mix_f32 v224, v156, v208, v216 op_sel:[0,0,0] op_sel_hi:[1,0,0]\n"
"v_mfma_f32_16x16x32_bf16 v[116:119], a[212:215], v[140:143], 0\n"
"v_fma_mix_f32 v225, v156, v209, v217 op_sel:[1,0,0] op_sel_hi:[1,0,0]\n"
"v_cvt_pk_bf16_f32 v156, v224, v225\n"
"v_mfma_f32_16x16x32_bf16 v[24:27], a[216:219], v[128:131], 0\n"
"buffer_load_dwordx4 v244, s[28:31], s49 offen offset:2048 lds\n"
"v_fma_mix_f32 v226, v157, v210, v218 op_sel:[0,0,0] op_sel_hi:[1,0,0]\n"
"v_mfma_f32_16x16x32_bf16 v[56:59], a[216:219], v[132:135], 0\n"
"v_fma_mix_f32 v227, v157, v211, v219 op_sel:[1,0,0] op_sel_hi:[1,0,0]\n"
"v_cvt_pk_bf16_f32 v157, v226, v227\n"
"v_mfma_f32_16x16x32_bf16 v[88:91], a[216:219], v[136:139], 0\n"
"v_fma_mix_f32 v224, v158, v212, v220 op_sel:[0,0,0] op_sel_hi:[1,0,0]\n"
"v_fma_mix_f32 v225, v158, v213, v221 op_sel:[1,0,0] op_sel_hi:[1,0,0]\n"
"v_mfma_f32_16x16x32_bf16 v[120:123], a[216:219], v[140:143], 0\n"
"buffer_load_dwordx4 v245, s[28:31], s49 offen offset:3072 lds\n"
"v_cvt_pk_bf16_f32 v158, v224, v225\n"
"v_mfma_f32_16x16x32_bf16 v[28:31], a[220:223], v[128:131], 0\n"
"s_add_u32 s49, s49, 0x80000\n"
"v_fma_mix_f32 v226, v159, v214, v222 op_sel:[0,0,0] op_sel_hi:[1,0,0]\n"
"v_fma_mix_f32 v227, v159, v215, v223 op_sel:[1,0,0] op_sel_hi:[1,0,0]\n"
"v_mfma_f32_16x16x32_bf16 v[60:63], a[220:223], v[132:135], 0\n"
"v_cvt_pk_bf16_f32 v159, v226, v227\n"
"v_mfma_f32_16x16x32_bf16 v[92:95], a[220:223], v[136:139], 0\n"
"v_mfma_f32_16x16x32_bf16 v[124:127], a[220:223], v[140:143], 0\n"
"s_waitcnt lgkmcnt(0)\n"
"s_waitcnt vmcnt(8)\n"
"v_mfma_f32_16x16x32_bf16 v[0:3], a[224:227], v[144:147], v[0:3]\n"
"s_barrier\n"
"ds_read_b64_tr_b16 a[192:193], v235 offset:16384\n"
"v_fma_mix_f32 v224, v160, v192, v200 op_sel:[0,0,0] op_sel_hi:[1,0,0]\n"
"v_fma_mix_f32 v225, v160, v193, v201 op_sel:[1,0,0] op_sel_hi:[1,0,0]\n"
"v_mfma_f32_16x16x32_bf16 v[32:35], a[224:227], v[148:151], v[32:35]\n"
"ds_read_b64_tr_b16 a[194:195], v235 offset:20480\n"
"v_cvt_pk_bf16_f32 v160, v224, v225\n"
"v_fma_mix_f32 v226, v161, v194, v202 op_sel:[0,0,0] op_sel_hi:[1,0,0]\n"
"v_mfma_f32_16x16x32_bf16 v[64:67], a[224:227], v[152:155], v[64:67]\n"
"ds_read_b128 v[208:211], v247 offset:768\n"
"v_fma_mix_f32 v227, v161, v195, v203 op_sel:[1,0,0] op_sel_hi:[1,0,0]\n"
"v_mfma_f32_16x16x32_bf16 v[96:99], a[224:227], v[156:159], v[96:99]\n"
"ds_read_b64_tr_b16 a[196:197], v235 offset:16392\n"
"v_cvt_pk_bf16_f32 v161, v226, v227\n"
"v_fma_mix_f32 v224, v162, v196, v204 op_sel:[0,0,0] op_sel_hi:[1,0,0]\n"
"v_mfma_f32_16x16x32_bf16 v[4:7], a[228:231], v[144:147], v[4:7]\n"
"ds_read_b64_tr_b16 a[198:199], v235 offset:20488\n"
"v_fma_mix_f32 v225, v162, v197, v205 op_sel:[1,0,0] op_sel_hi:[1,0,0]\n"
"v_mfma_f32_16x16x32_bf16 v[36:39], a[228:231], v[148:151], v[36:39]\n"
"ds_read_b128 v[212:215], v247 offset:784\n"
"buffer_load_dwordx4 v[128:131], v246, s[64:67], s56 offen offset:0\n"
"v_cvt_pk_bf16_f32 v162, v224, v225\n"
"v_fma_mix_f32 v226, v163, v198, v206 op_sel:[0,0,0] op_sel_hi:[1,0,0]\n"
"v_mfma_f32_16x16x32_bf16 v[68:71], a[228:231], v[152:155], v[68:71]\n"
"ds_read_b64_tr_b16 a[200:201], v235 offset:16896\n"
"v_fma_mix_f32 v227, v163, v199, v207 op_sel:[1,0,0] op_sel_hi:[1,0,0]\n"
"v_cvt_pk_bf16_f32 v163, v226, v227\n"
"v_mfma_f32_16x16x32_bf16 v[100:103], a[228:231], v[156:159], v[100:103]\n"
"ds_read_b64_tr_b16 a[202:203], v235 offset:20992\n"
"v_fma_mix_f32 v224, v164, v192, v200 op_sel:[0,0,0] op_sel_hi:[1,0,0]\n"
"v_mfma_f32_16x16x32_bf16 v[8:11], a[232:235], v[144:147], v[8:11]\n"
"ds_read_b128 v[216:219], v247 offset:800\n"
"v_fma_mix_f32 v225, v164, v193, v201 op_sel:[1,0,0] op_sel_hi:[1,0,0]\n"
"v_cvt_pk_bf16_f32 v164, v224, v225\n"
"v_mfma_f32_16x16x32_bf16 v[40:43], a[232:235], v[148:151], v[40:43]\n"
"ds_read_b64_tr_b16 a[204:205], v235 offset:16904\n"
"v_fma_mix_f32 v226, v165, v194, v202 op_sel:[0,0,0] op_sel_hi:[1,0,0]\n"
"v_mfma_f32_16x16x32_bf16 v[72:75], a[232:235], v[152:155], v[72:75]\n"
"ds_read_b64_tr_b16 a[206:207], v235 offset:21000\n"
"v_fma_mix_f32 v227, v165, v195, v203 op_sel:[1,0,0] op_sel_hi:[1,0,0]\n"
"v_cvt_pk_bf16_f32 v165, v226, v227\n"
"v_mfma_f32_16x16x32_bf16 v[104:107], a[232:235], v[156:159], v[104:107]\n"
"ds_read_b128 v[220:223], v247 offset:816\n"
"buffer_load_dwordx4 v[132:135], v248, s[64:67], s56 offen offset:0\n"
"v_fma_mix_f32 v224, v166, v196, v204 op_sel:[0,0,0] op_sel_hi:[1,0,0]\n"
"v_fma_mix_f32 v225, v166, v197, v205 op_sel:[1,0,0] op_sel_hi:[1,0,0]\n"
"v_mfma_f32_16x16x32_bf16 v[12:15], a[236:239], v[144:147], v[12:15]\n"
"ds_read_b64_tr_b16 a[208:209], v235 offset:17408\n"
"v_cvt_pk_bf16_f32 v166, v224, v225\n"
"v_mfma_f32_16x16x32_bf16 v[44:47], a[236:239], v[148:151], v[44:47]\n"
"ds_read_b64_tr_b16 a[210:211], v235 offset:21504\n"
"v_fma_mix_f32 v226, v167, v198, v206 op_sel:[0,0,0] op_sel_hi:[1,0,0]\n"
"v_fma_mix_f32 v227, v167, v199, v207 op_sel:[1,0,0] op_sel_hi:[1,0,0]\n"
"v_mfma_f32_16x16x32_bf16 v[76:79], a[236:239], v[152:155], v[76:79]\n"
"v_cvt_pk_bf16_f32 v167, v226, v227\n"
"v_mfma_f32_16x16x32_bf16 v[108:111], a[236:239], v[156:159], v[108:111]\n"
"ds_read_b64_tr_b16 a[212:213], v235 offset:17416\n"
"v_fma_mix_f32 v224, v168, v192, v200 op_sel:[0,0,0] op_sel_hi:[1,0,0]\n"
"v_fma_mix_f32 v225, v168, v193, v201 op_sel:[1,0,0] op_sel_hi:[1,0,0]\n"
"v_mfma_f32_16x16x32_bf16 v[16:19], a[240:243], v[144:147], v[16:19]\n"
"ds_read_b64_tr_b16 a[214:215], v235 offset:21512\n"
"v_cvt_pk_bf16_f32 v168, v224, v225\n"
"v_fma_mix_f32 v226, v169, v194, v202 op_sel:[0,0,0] op_sel_hi:[1,0,0]\n"
"v_mfma_f32_16x16x32_bf16 v[48:51], a[240:243], v[148:151], v[48:51]\n"
"s_add_u32 m0, s51, 0xc000\n"
"buffer_load_dwordx4 v[136:139], v249, s[64:67], s56 offen offset:0\n"
"v_fma_mix_f32 v227, v169, v195, v203 op_sel:[1,0,0] op_sel_hi:[1,0,0]\n"
"v_mfma_f32_16x16x32_bf16 v[80:83], a[240:243], v[152:155], v[80:83]\n"
"buffer_load_dwordx4 v242, s[28:31], s49 offen offset:0 lds\n"
"ds_read_b64_tr_b16 a[216:217], v235 offset:17920\n"
"v_cvt_pk_bf16_f32 v169, v226, v227\n"
"v_fma_mix_f32 v224, v170, v196, v204 op_sel:[0,0,0] op_sel_hi:[1,0,0]\n"
"v_mfma_f32_16x16x32_bf16 v[112:115], a[240:243], v[156:159], v[112:115]\n"
"ds_read_b64_tr_b16 a[218:219], v235 offset:22016\n"
"v_fma_mix_f32 v225, v170, v197, v205 op_sel:[1,0,0] op_sel_hi:[1,0,0]\n"
"v_mfma_f32_16x16x32_bf16 v[20:23], a[244:247], v[144:147], v[20:23]\n"
"v_cvt_pk_bf16_f32 v170, v224, v225\n"
"v_fma_mix_f32 v226, v171, v198, v206 op_sel:[0,0,0] op_sel_hi:[1,0,0]\n"
"v_mfma_f32_16x16x32_bf16 v[52:55], a[244:247], v[148:151], v[52:55]\n"
"buffer_load_dwordx4 v243, s[28:31], s49 offen offset:1024 lds\n"
"ds_read_b64_tr_b16 a[220:221], v235 offset:17928\n"
"v_fma_mix_f32 v227, v171, v199, v207 op_sel:[1,0,0] op_sel_hi:[1,0,0]\n"
"v_cvt_pk_bf16_f32 v171, v226, v227\n"
"v_mfma_f32_16x16x32_bf16 v[84:87], a[244:247], v[152:155], v[84:87]\n"
"ds_read_b64_tr_b16 a[222:223], v235 offset:22024\n"
"v_fma_mix_f32 v224, v172, v192, v200 op_sel:[0,0,0] op_sel_hi:[1,0,0]\n"
"v_mfma_f32_16x16x32_bf16 v[116:119], a[244:247], v[156:159], v[116:119]\n"
"buffer_load_dwordx4 v[140:143], v250, s[64:67], s56 offen offset:0\n"
"v_fma_mix_f32 v225, v172, v193, v201 op_sel:[1,0,0] op_sel_hi:[1,0,0]\n"
"v_cvt_pk_bf16_f32 v172, v224, v225\n"
"v_mfma_f32_16x16x32_bf16 v[24:27], a[248:251], v[144:147], v[24:27]\n"
"buffer_load_dwordx4 v244, s[28:31], s49 offen offset:2048 lds\n"
"v_fma_mix_f32 v226, v173, v194, v202 op_sel:[0,0,0] op_sel_hi:[1,0,0]\n"
"v_mfma_f32_16x16x32_bf16 v[56:59], a[248:251], v[148:151], v[56:59]\n"
"v_fma_mix_f32 v227, v173, v195, v203 op_sel:[1,0,0] op_sel_hi:[1,0,0]\n"
"v_cvt_pk_bf16_f32 v173, v226, v227\n"
"v_mfma_f32_16x16x32_bf16 v[88:91], a[248:251], v[152:155], v[88:91]\n"
"v_fma_mix_f32 v224, v174, v196, v204 op_sel:[0,0,0] op_sel_hi:[1,0,0]\n"
"v_fma_mix_f32 v225, v174, v197, v205 op_sel:[1,0,0] op_sel_hi:[1,0,0]\n"
"v_mfma_f32_16x16x32_bf16 v[120:123], a[248:251], v[156:159], v[120:123]\n"
"buffer_load_dwordx4 v245, s[28:31], s49 offen offset:3072 lds\n"
"v_cvt_pk_bf16_f32 v174, v224, v225\n"
"v_mfma_f32_16x16x32_bf16 v[28:31], a[252:255], v[144:147], v[28:31]\n"
"s_add_u32 s49, s49, 0x80000\n"
"v_fma_mix_f32 v226, v175, v198, v206 op_sel:[0,0,0] op_sel_hi:[1,0,0]\n"
"v_fma_mix_f32 v227, v175, v199, v207 op_sel:[1,0,0] op_sel_hi:[1,0,0]\n"
"v_mfma_f32_16x16x32_bf16 v[60:63], a[252:255], v[148:151], v[60:63]\n"
"v_cvt_pk_bf16_f32 v175, v226, v227\n"
"v_mfma_f32_16x16x32_bf16 v[92:95], a[252:255], v[152:155], v[92:95]\n"
"v_mfma_f32_16x16x32_bf16 v[124:127], a[252:255], v[156:159], v[124:127]\n"
"s_waitcnt lgkmcnt(0)\n"
"s_waitcnt vmcnt(12)\n"
"v_mfma_f32_16x16x32_bf16 v[0:3], a[192:195], v[160:163], v[0:3]\n"
"ds_read_b64_tr_b16 a[224:225], v235 offset:24576\n"
"v_fma_mix_f32 v224, v176, v208, v216 op_sel:[0,0,0] op_sel_hi:[1,0,0]\n"
"v_fma_mix_f32 v225, v176, v209, v217 op_sel:[1,0,0] op_sel_hi:[1,0,0]\n"
"v_mfma_f32_16x16x32_bf16 v[32:35], a[192:195], v[164:167], v[32:35]\n"
"ds_read_b64_tr_b16 a[226:227], v235 offset:28672\n"
"v_cvt_pk_bf16_f32 v176, v224, v225\n"
"v_fma_mix_f32 v226, v177, v210, v218 op_sel:[0,0,0] op_sel_hi:[1,0,0]\n"
"v_mfma_f32_16x16x32_bf16 v[64:67], a[192:195], v[168:171], v[64:67]\n"
"ds_read_b128 v[192:195], v247 offset:1024\n"
"v_fma_mix_f32 v227, v177, v211, v219 op_sel:[1,0,0] op_sel_hi:[1,0,0]\n"
"v_mfma_f32_16x16x32_bf16 v[96:99], a[192:195], v[172:175], v[96:99]\n"
"ds_read_b64_tr_b16 a[228:229], v235 offset:24584\n"
"v_cvt_pk_bf16_f32 v177, v226, v227\n"
"v_fma_mix_f32 v224, v178, v212, v220 op_sel:[0,0,0] op_sel_hi:[1,0,0]\n"
"v_mfma_f32_16x16x32_bf16 v[4:7], a[196:199], v[160:163], v[4:7]\n"
"ds_read_b64_tr_b16 a[230:231], v235 offset:28680\n"
"v_fma_mix_f32 v225, v178, v213, v221 op_sel:[1,0,0] op_sel_hi:[1,0,0]\n"
"v_mfma_f32_16x16x32_bf16 v[36:39], a[196:199], v[164:167], v[36:39]\n"
"ds_read_b128 v[196:199], v247 offset:1040\n"
"buffer_load_dwordx4 v[144:147], v246, s[64:67], s56 offen offset:1024\n"
"v_cvt_pk_bf16_f32 v178, v224, v225\n"
"v_fma_mix_f32 v226, v179, v214, v222 op_sel:[0,0,0] op_sel_hi:[1,0,0]\n"
"v_mfma_f32_16x16x32_bf16 v[68:71], a[196:199], v[168:171], v[68:71]\n"
"ds_read_b64_tr_b16 a[232:233], v235 offset:25088\n"
"v_fma_mix_f32 v227, v179, v215, v223 op_sel:[1,0,0] op_sel_hi:[1,0,0]\n"
"v_cvt_pk_bf16_f32 v179, v226, v227\n"
"v_mfma_f32_16x16x32_bf16 v[100:103], a[196:199], v[172:175], v[100:103]\n"
"ds_read_b64_tr_b16 a[234:235], v235 offset:29184\n"
"v_fma_mix_f32 v224, v180, v208, v216 op_sel:[0,0,0] op_sel_hi:[1,0,0]\n"
"v_mfma_f32_16x16x32_bf16 v[8:11], a[200:203], v[160:163], v[8:11]\n"
"ds_read_b128 v[200:203], v247 offset:1056\n"
"v_fma_mix_f32 v225, v180, v209, v217 op_sel:[1,0,0] op_sel_hi:[1,0,0]\n"
"v_cvt_pk_bf16_f32 v180, v224, v225\n"
"v_mfma_f32_16x16x32_bf16 v[40:43], a[200:203], v[164:167], v[40:43]\n"
"ds_read_b64_tr_b16 a[236:237], v235 offset:25096\n"
"v_fma_mix_f32 v226, v181, v210, v218 op_sel:[0,0,0] op_sel_hi:[1,0,0]\n"
"v_mfma_f32_16x16x32_bf16 v[72:75], a[200:203], v[168:171], v[72:75]\n"
"ds_read_b64_tr_b16 a[238:239], v235 offset:29192\n"
"v_fma_mix_f32 v227, v181, v211, v219 op_sel:[1,0,0] op_sel_hi:[1,0,0]\n"
"v_cvt_pk_bf16_f32 v181, v226, v227\n"
"v_mfma_f32_16x16x32_bf16 v[104:107], a[200:203], v[172:175], v[104:107]\n"
"ds_read_b128 v[204:207], v247 offset:1072\n"
"buffer_load_dwordx4 v[148:151], v248, s[64:67], s56 offen offset:1024\n"
"v_fma_mix_f32 v224, v182, v212, v220 op_sel:[0,0,0] op_sel_hi:[1,0,0]\n"
"v_fma_mix_f32 v225, v182, v213, v221 op_sel:[1,0,0] op_sel_hi:[1,0,0]\n"
"v_mfma_f32_16x16x32_bf16 v[12:15], a[204:207], v[160:163], v[12:15]\n"
"ds_read_b64_tr_b16 a[240:241], v235 offset:25600\n"
"v_cvt_pk_bf16_f32 v182, v224, v225\n"
"v_mfma_f32_16x16x32_bf16 v[44:47], a[204:207], v[164:167], v[44:47]\n"
"ds_read_b64_tr_b16 a[242:243], v235 offset:29696\n"
"v_fma_mix_f32 v226, v183, v214, v222 op_sel:[0,0,0] op_sel_hi:[1,0,0]\n"
"v_fma_mix_f32 v227, v183, v215, v223 op_sel:[1,0,0] op_sel_hi:[1,0,0]\n"
"v_mfma_f32_16x16x32_bf16 v[76:79], a[204:207], v[168:171], v[76:79]\n"
"v_cvt_pk_bf16_f32 v183, v226, v227\n"
"v_mfma_f32_16x16x32_bf16 v[108:111], a[204:207], v[172:175], v[108:111]\n"
"ds_read_b64_tr_b16 a[244:245], v235 offset:25608\n"
"v_fma_mix_f32 v224, v184, v208, v216 op_sel:[0,0,0] op_sel_hi:[1,0,0]\n"
"v_fma_mix_f32 v225, v184, v209, v217 op_sel:[1,0,0] op_sel_hi:[1,0,0]\n"
"v_mfma_f32_16x16x32_bf16 v[16:19], a[208:211], v[160:163], v[16:19]\n"
"ds_read_b64_tr_b16 a[246:247], v235 offset:29704\n"
"v_cvt_pk_bf16_f32 v184, v224, v225\n"
"v_fma_mix_f32 v226, v185, v210, v218 op_sel:[0,0,0] op_sel_hi:[1,0,0]\n"
"v_mfma_f32_16x16x32_bf16 v[48:51], a[208:211], v[164:167], v[48:51]\n"
"buffer_load_dwordx4 v[152:155], v249, s[64:67], s56 offen offset:1024\n"
"v_fma_mix_f32 v227, v185, v211, v219 op_sel:[1,0,0] op_sel_hi:[1,0,0]\n"
"v_mfma_f32_16x16x32_bf16 v[80:83], a[208:211], v[168:171], v[80:83]\n"
"ds_read_b64_tr_b16 a[248:249], v235 offset:26112\n"
"v_cvt_pk_bf16_f32 v185, v226, v227\n"
"v_fma_mix_f32 v224, v186, v212, v220 op_sel:[0,0,0] op_sel_hi:[1,0,0]\n"
"v_mfma_f32_16x16x32_bf16 v[112:115], a[208:211], v[172:175], v[112:115]\n"
"ds_read_b64_tr_b16 a[250:251], v235 offset:30208\n"
"v_fma_mix_f32 v225, v186, v213, v221 op_sel:[1,0,0] op_sel_hi:[1,0,0]\n"
"v_mfma_f32_16x16x32_bf16 v[20:23], a[212:215], v[160:163], v[20:23]\n"
"v_cvt_pk_bf16_f32 v186, v224, v225\n"
"v_fma_mix_f32 v226, v187, v214, v222 op_sel:[0,0,0] op_sel_hi:[1,0,0]\n"
"v_mfma_f32_16x16x32_bf16 v[52:55], a[212:215], v[164:167], v[52:55]\n"
"ds_read_b64_tr_b16 a[252:253], v235 offset:26120\n"
"v_fma_mix_f32 v227, v187, v215, v223 op_sel:[1,0,0] op_sel_hi:[1,0,0]\n"
"v_cvt_pk_bf16_f32 v187, v226, v227\n"
"v_mfma_f32_16x16x32_bf16 v[84:87], a[212:215], v[168:171], v[84:87]\n"
"ds_read_b64_tr_b16 a[254:255], v235 offset:30216\n"
"v_fma_mix_f32 v224, v188, v208, v216 op_sel:[0,0,0] op_sel_hi:[1,0,0]\n"
"v_mfma_f32_16x16x32_bf16 v[116:119], a[212:215], v[172:175], v[116:119]\n"
"buffer_load_dwordx4 v[156:159], v250, s[64:67], s56 offen offset:1024\n"
"v_fma_mix_f32 v225, v188, v209, v217 op_sel:[1,0,0] op_sel_hi:[1,0,0]\n"
"v_cvt_pk_bf16_f32 v188, v224, v225\n"
"v_mfma_f32_16x16x32_bf16 v[24:27], a[216:219], v[160:163], v[24:27]\n"
"v_fma_mix_f32 v226, v189, v210, v218 op_sel:[0,0,0] op_sel_hi:[1,0,0]\n"
"v_mfma_f32_16x16x32_bf16 v[56:59], a[216:219], v[164:167], v[56:59]\n"
"v_fma_mix_f32 v227, v189, v211, v219 op_sel:[1,0,0] op_sel_hi:[1,0,0]\n"
"v_cvt_pk_bf16_f32 v189, v226, v227\n"
"v_mfma_f32_16x16x32_bf16 v[88:91], a[216:219], v[168:171], v[88:91]\n"
"v_fma_mix_f32 v224, v190, v212, v220 op_sel:[0,0,0] op_sel_hi:[1,0,0]\n"
"v_fma_mix_f32 v225, v190, v213, v221 op_sel:[1,0,0] op_sel_hi:[1,0,0]\n"
"v_mfma_f32_16x16x32_bf16 v[120:123], a[216:219], v[172:175], v[120:123]\n"
"v_cvt_pk_bf16_f32 v190, v224, v225\n"
"v_mfma_f32_16x16x32_bf16 v[28:31], a[220:223], v[160:163], v[28:31]\n"
"v_fma_mix_f32 v226, v191, v214, v222 op_sel:[0,0,0] op_sel_hi:[1,0,0]\n"
"v_fma_mix_f32 v227, v191, v215, v223 op_sel:[1,0,0] op_sel_hi:[1,0,0]\n"
"v_mfma_f32_16x16x32_bf16 v[60:63], a[220:223], v[164:167], v[60:63]\n"
"v_cvt_pk_bf16_f32 v191, v226, v227\n"
"v_mfma_f32_16x16x32_bf16 v[92:95], a[220:223], v[168:171], v[92:95]\n"
"v_mfma_f32_16x16x32_bf16 v[124:127], a[220:223], v[172:175], v[124:127]\n"
"s_waitcnt lgkmcnt(0)\n"
"s_waitcnt vmcnt(6)\n"
"v_mfma_f32_16x16x32_bf16 v[0:3], a[224:227], v[176:179], v[0:3]\n"
"s_barrier\n"
"ds_read_b64_tr_b16 a[192:193], v235 offset:32768\n"
"v_fma_mix_f32 v224, v128, v192, v200 op_sel:[0,0,0] op_sel_hi:[1,0,0]\n"
"v_fma_mix_f32 v225, v128, v193, v201 op_sel:[1,0,0] op_sel_hi:[1,0,0]\n"
"v_mfma_f32_16x16x32_bf16 v[32:35], a[224:227], v[180:183], v[32:35]\n"
"ds_read_b64_tr_b16 a[194:195], v235 offset:36864\n"
"v_cvt_pk_bf16_f32 v128, v224, v225\n"
"v_fma_mix_f32 v226, v129, v194, v202 op_sel:[0,0,0] op_sel_hi:[1,0,0]\n"
"v_mfma_f32_16x16x32_bf16 v[64:67], a[224:227], v[184:187], v[64:67]\n"
"ds_read_b128 v[208:211], v247 offset:1280\n"
"v_fma_mix_f32 v227, v129, v195, v203 op_sel:[1,0,0] op_sel_hi:[1,0,0]\n"
"v_mfma_f32_16x16x32_bf16 v[96:99], a[224:227], v[188:191], v[96:99]\n"
"ds_read_b64_tr_b16 a[196:197], v235 offset:32776\n"
"v_cvt_pk_bf16_f32 v129, v226, v227\n"
"v_fma_mix_f32 v224, v130, v196, v204 op_sel:[0,0,0] op_sel_hi:[1,0,0]\n"
"v_mfma_f32_16x16x32_bf16 v[4:7], a[228:231], v[176:179], v[4:7]\n"
"ds_read_b64_tr_b16 a[198:199], v235 offset:36872\n"
"v_fma_mix_f32 v225, v130, v197, v205 op_sel:[1,0,0] op_sel_hi:[1,0,0]\n"
"v_mfma_f32_16x16x32_bf16 v[36:39], a[228:231], v[180:183], v[36:39]\n"
"ds_read_b128 v[212:215], v247 offset:1296\n"
"buffer_load_dwordx4 v[160:163], v246, s[64:67], s56 offen offset:2048\n"
"v_cvt_pk_bf16_f32 v130, v224, v225\n"
"v_fma_mix_f32 v226, v131, v198, v206 op_sel:[0,0,0] op_sel_hi:[1,0,0]\n"
"v_mfma_f32_16x16x32_bf16 v[68:71], a[228:231], v[184:187], v[68:71]\n"
"ds_read_b64_tr_b16 a[200:201], v235 offset:33280\n"
"v_fma_mix_f32 v227, v131, v199, v207 op_sel:[1,0,0] op_sel_hi:[1,0,0]\n"
"v_cvt_pk_bf16_f32 v131, v226, v227\n"
"v_mfma_f32_16x16x32_bf16 v[100:103], a[228:231], v[188:191], v[100:103]\n"
"ds_read_b64_tr_b16 a[202:203], v235 offset:37376\n"
"v_fma_mix_f32 v224, v132, v192, v200 op_sel:[0,0,0] op_sel_hi:[1,0,0]\n"
"v_mfma_f32_16x16x32_bf16 v[8:11], a[232:235], v[176:179], v[8:11]\n"
"ds_read_b128 v[216:219], v247 offset:1312\n"
"v_fma_mix_f32 v225, v132, v193, v201 op_sel:[1,0,0] op_sel_hi:[1,0,0]\n"
"v_cvt_pk_bf16_f32 v132, v224, v225\n"
"v_mfma_f32_16x16x32_bf16 v[40:43], a[232:235], v[180:183], v[40:43]\n"
"ds_read_b64_tr_b16 a[204:205], v235 offset:33288\n"
"v_fma_mix_f32 v226, v133, v194, v202 op_sel:[0,0,0] op_sel_hi:[1,0,0]\n"
"v_mfma_f32_16x16x32_bf16 v[72:75], a[232:235], v[184:187], v[72:75]\n"
"ds_read_b64_tr_b16 a[206:207], v235 offset:37384\n"
"v_fma_mix_f32 v227, v133, v195, v203 op_sel:[1,0,0] op_sel_hi:[1,0,0]\n"
"v_cvt_pk_bf16_f32 v133, v226, v227\n"
"v_mfma_f32_16x16x32_bf16 v[104:107], a[232:235], v[188:191], v[104:107]\n"
"ds_read_b128 v[220:223], v247 offset:1328\n"
"buffer_load_dwordx4 v[164:167], v248, s[64:67], s56 offen offset:2048\n"
"v_fma_mix_f32 v224, v134, v196, v204 op_sel:[0,0,0] op_sel_hi:[1,0,0]\n"
"v_fma_mix_f32 v225, v134, v197, v205 op_sel:[1,0,0] op_sel_hi:[1,0,0]\n"
"v_mfma_f32_16x16x32_bf16 v[12:15], a[236:239], v[176:179], v[12:15]\n"
"ds_read_b64_tr_b16 a[208:209], v235 offset:33792\n"
"v_cvt_pk_bf16_f32 v134, v224, v225\n"
"v_mfma_f32_16x16x32_bf16 v[44:47], a[236:239], v[180:183], v[44:47]\n"
"ds_read_b64_tr_b16 a[210:211], v235 offset:37888\n"
"v_fma_mix_f32 v226, v135, v198, v206 op_sel:[0,0,0] op_sel_hi:[1,0,0]\n"
"v_fma_mix_f32 v227, v135, v199, v207 op_sel:[1,0,0] op_sel_hi:[1,0,0]\n"
"v_mfma_f32_16x16x32_bf16 v[76:79], a[236:239], v[184:187], v[76:79]\n"
"v_cvt_pk_bf16_f32 v135, v226, v227\n"
"v_mfma_f32_16x16x32_bf16 v[108:111], a[236:239], v[188:191], v[108:111]\n"
"ds_read_b64_tr_b16 a[212:213], v235 offset:33800\n"
"v_fma_mix_f32 v224, v136, v192, v200 op_sel:[0,0,0] op_sel_hi:[1,0,0]\n"
"v_fma_mix_f32 v225, v136, v193, v201 op_sel:[1,0,0] op_sel_hi:[1,0,0]\n"
"v_mfma_f32_16x16x32_bf16 v[16:19], a[240:243], v[176:179], v[16:19]\n"
"ds_read_b64_tr_b16 a[214:215], v235 offset:37896\n"
"v_cvt_pk_bf16_f32 v136, v224, v225\n"
"v_fma_mix_f32 v226, v137, v194, v202 op_sel:[0,0,0] op_sel_hi:[1,0,0]\n"
"v_mfma_f32_16x16x32_bf16 v[48:51], a[240:243], v[180:183], v[48:51]\n"
"s_add_u32 m0, s51, 0\n"
"buffer_load_dwordx4 v[168:171], v249, s[64:67], s56 offen offset:2048\n"
"v_fma_mix_f32 v227, v137, v195, v203 op_sel:[1,0,0] op_sel_hi:[1,0,0]\n"
"v_mfma_f32_16x16x32_bf16 v[80:83], a[240:243], v[184:187], v[80:83]\n"
"buffer_load_dwordx4 v242, s[28:31], s49 offen offset:0 lds\n"
"ds_read_b64_tr_b16 a[216:217], v235 offset:34304\n"
"v_cvt_pk_bf16_f32 v137, v226, v227\n"
"v_fma_mix_f32 v224, v138, v196, v204 op_sel:[0,0,0] op_sel_hi:[1,0,0]\n"
"v_mfma_f32_16x16x32_bf16 v[112:115], a[240:243], v[188:191], v[112:115]\n"
"ds_read_b64_tr_b16 a[218:219], v235 offset:38400\n"
"v_fma_mix_f32 v225, v138, v197, v205 op_sel:[1,0,0] op_sel_hi:[1,0,0]\n"
"v_mfma_f32_16x16x32_bf16 v[20:23], a[244:247], v[176:179], v[20:23]\n"
"v_cvt_pk_bf16_f32 v138, v224, v225\n"
"v_fma_mix_f32 v226, v139, v198, v206 op_sel:[0,0,0] op_sel_hi:[1,0,0]\n"
"v_mfma_f32_16x16x32_bf16 v[52:55], a[244:247], v[180:183], v[52:55]\n"
"buffer_load_dwordx4 v243, s[28:31], s49 offen offset:1024 lds\n"
"ds_read_b64_tr_b16 a[220:221], v235 offset:34312\n"
"v_fma_mix_f32 v227, v139, v199, v207 op_sel:[1,0,0] op_sel_hi:[1,0,0]\n"
"v_cvt_pk_bf16_f32 v139, v226, v227\n"
"v_mfma_f32_16x16x32_bf16 v[84:87], a[244:247], v[184:187], v[84:87]\n"
"ds_read_b64_tr_b16 a[222:223], v235 offset:38408\n"
"v_fma_mix_f32 v224, v140, v192, v200 op_sel:[0,0,0] op_sel_hi:[1,0,0]\n"
"v_mfma_f32_16x16x32_bf16 v[116:119], a[244:247], v[188:191], v[116:119]\n"
"buffer_load_dwordx4 v[172:175], v250, s[64:67], s56 offen offset:2048\n"
"v_fma_mix_f32 v225, v140, v193, v201 op_sel:[1,0,0] op_sel_hi:[1,0,0]\n"
"v_cvt_pk_bf16_f32 v140, v224, v225\n"
"v_mfma_f32_16x16x32_bf16 v[24:27], a[248:251], v[176:179], v[24:27]\n"
"buffer_load_dwordx4 v244, s[28:31], s49 offen offset:2048 lds\n"
"v_fma_mix_f32 v226, v141, v194, v202 op_sel:[0,0,0] op_sel_hi:[1,0,0]\n"
"v_mfma_f32_16x16x32_bf16 v[56:59], a[248:251], v[180:183], v[56:59]\n"
"v_fma_mix_f32 v227, v141, v195, v203 op_sel:[1,0,0] op_sel_hi:[1,0,0]\n"
"v_cvt_pk_bf16_f32 v141, v226, v227\n"
"v_mfma_f32_16x16x32_bf16 v[88:91], a[248:251], v[184:187], v[88:91]\n"
"v_fma_mix_f32 v224, v142, v196, v204 op_sel:[0,0,0] op_sel_hi:[1,0,0]\n"
"v_fma_mix_f32 v225, v142, v197, v205 op_sel:[1,0,0] op_sel_hi:[1,0,0]\n"
"v_mfma_f32_16x16x32_bf16 v[120:123], a[248:251], v[188:191], v[120:123]\n"
"buffer_load_dwordx4 v245, s[28:31], s49 offen offset:3072 lds\n"
"v_cvt_pk_bf16_f32 v142, v224, v225\n"
"v_mfma_f32_16x16x32_bf16 v[28:31], a[252:255], v[176:179], v[28:31]\n"
"s_add_u32 s49, s49, 0x80000\n"
"v_fma_mix_f32 v226, v143, v198, v206 op_sel:[0,0,0] op_sel_hi:[1,0,0]\n"
"v_fma_mix_f32 v227, v143, v199, v207 op_sel:[1,0,0] op_sel_hi:[1,0,0]\n"
"v_mfma_f32_16x16x32_bf16 v[60:63], a[252:255], v[180:183], v[60:63]\n"
"v_cvt_pk_bf16_f32 v143, v226, v227\n"
"v_mfma_f32_16x16x32_bf16 v[92:95], a[252:255], v[184:187], v[92:95]\n"
"v_mfma_f32_16x16x32_bf16 v[124:127], a[252:255], v[188:191], v[124:127]\n"
"s_waitcnt lgkmcnt(0)\n"
"s_waitcnt vmcnt(8)\n"
"v_mfma_f32_16x16x32_bf16 v[0:3], a[192:195], v[128:131], v[0:3]\n"
"ds_read_b64_tr_b16 a[224:225], v235 offset:40960\n"
"v_fma_mix_f32 v224, v144, v208, v216 op_sel:[0,0,0] op_sel_hi:[1,0,0]\n"
"v_fma_mix_f32 v225, v144, v209, v217 op_sel:[1,0,0] op_sel_hi:[1,0,0]\n"
"v_mfma_f32_16x16x32_bf16 v[32:35], a[192:195], v[132:135], v[32:35]\n"
"ds_read_b64_tr_b16 a[226:227], v235 offset:45056\n"
"v_cvt_pk_bf16_f32 v144, v224, v225\n"
"v_fma_mix_f32 v226, v145, v210, v218 op_sel:[0,0,0] op_sel_hi:[1,0,0]\n"
"v_mfma_f32_16x16x32_bf16 v[64:67], a[192:195], v[136:139], v[64:67]\n"
"ds_read_b128 v[192:195], v247 offset:1536\n"
"v_fma_mix_f32 v227, v145, v211, v219 op_sel:[1,0,0] op_sel_hi:[1,0,0]\n"
"v_mfma_f32_16x16x32_bf16 v[96:99], a[192:195], v[140:143], v[96:99]\n"
"ds_read_b64_tr_b16 a[228:229], v235 offset:40968\n"
"v_cvt_pk_bf16_f32 v145, v226, v227\n"
"v_fma_mix_f32 v224, v146, v212, v220 op_sel:[0,0,0] op_sel_hi:[1,0,0]\n"
"v_mfma_f32_16x16x32_bf16 v[4:7], a[196:199], v[128:131], v[4:7]\n"
"ds_read_b64_tr_b16 a[230:231], v235 offset:45064\n"
"v_fma_mix_f32 v225, v146, v213, v221 op_sel:[1,0,0] op_sel_hi:[1,0,0]\n"
"v_mfma_f32_16x16x32_bf16 v[36:39], a[196:199], v[132:135], v[36:39]\n"
"ds_read_b128 v[196:199], v247 offset:1552\n"
"buffer_load_dwordx4 v[176:179], v246, s[64:67], s56 offen offset:3072\n"
"v_cvt_pk_bf16_f32 v146, v224, v225\n"
"v_fma_mix_f32 v226, v147, v214, v222 op_sel:[0,0,0] op_sel_hi:[1,0,0]\n"
"v_mfma_f32_16x16x32_bf16 v[68:71], a[196:199], v[136:139], v[68:71]\n"
"ds_read_b64_tr_b16 a[232:233], v235 offset:41472\n"
"v_fma_mix_f32 v227, v147, v215, v223 op_sel:[1,0,0] op_sel_hi:[1,0,0]\n"
"v_cvt_pk_bf16_f32 v147, v226, v227\n"
"v_mfma_f32_16x16x32_bf16 v[100:103], a[196:199], v[140:143], v[100:103]\n"
"ds_read_b64_tr_b16 a[234:235], v235 offset:45568\n"
"v_fma_mix_f32 v224, v148, v208, v216 op_sel:[0,0,0] op_sel_hi:[1,0,0]\n"
"v_mfma_f32_16x16x32_bf16 v[8:11], a[200:203], v[128:131], v[8:11]\n"
"ds_read_b128 v[200:203], v247 offset:1568\n"
"v_fma_mix_f32 v225, v148, v209, v217 op_sel:[1,0,0] op_sel_hi:[1,0,0]\n"
"v_cvt_pk_bf16_f32 v148, v224, v225\n"
"v_mfma_f32_16x16x32_bf16 v[40:43], a[200:203], v[132:135], v[40:43]\n"
"ds_read_b64_tr_b16 a[236:237], v235 offset:41480\n"
"v_fma_mix_f32 v226, v149, v210, v218 op_sel:[0,0,0] op_sel_hi:[1,0,0]\n"
"v_mfma_f32_16x16x32_bf16 v[72:75], a[200:203], v[136:139], v[72:75]\n"
"ds_read_b64_tr_b16 a[238:239], v235 offset:45576\n"
"v_fma_mix_f32 v227, v149, v211, v219 op_sel:[1,0,0] op_sel_hi:[1,0,0]\n"
"v_cvt_pk_bf16_f32 v149, v226, v227\n"
"v_mfma_f32_16x16x32_bf16 v[104:107], a[200:203], v[140:143], v[104:107]\n"
"ds_read_b128 v[204:207], v247 offset:1584\n"
"buffer_load_dwordx4 v[180:183], v248, s[64:67], s56 offen offset:3072\n"
"v_fma_mix_f32 v224, v150, v212, v220 op_sel:[0,0,0] op_sel_hi:[1,0,0]\n"
"v_fma_mix_f32 v225, v150, v213, v221 op_sel:[1,0,0] op_sel_hi:[1,0,0]\n"
"v_mfma_f32_16x16x32_bf16 v[12:15], a[204:207], v[128:131], v[12:15]\n"
"ds_read_b64_tr_b16 a[240:241], v235 offset:41984\n"
"v_cvt_pk_bf16_f32 v150, v224, v225\n"
"v_mfma_f32_16x16x32_bf16 v[44:47], a[204:207], v[132:135], v[44:47]\n"
"ds_read_b64_tr_b16 a[242:243], v235 offset:46080\n"
"v_fma_mix_f32 v226, v151, v214, v222 op_sel:[0,0,0] op_sel_hi:[1,0,0]\n"
"v_fma_mix_f32 v227, v151, v215, v223 op_sel:[1,0,0] op_sel_hi:[1,0,0]\n"
"v_mfma_f32_16x16x32_bf16 v[76:79], a[204:207], v[136:139], v[76:79]\n"
"v_cvt_pk_bf16_f32 v151, v226, v227\n"
"v_mfma_f32_16x16x32_bf16 v[108:111], a[204:207], v[140:143], v[108:111]\n"
"ds_read_b64_tr_b16 a[244:245], v235 offset:41992\n"
"v_fma_mix_f32 v224, v152, v208, v216 op_sel:[0,0,0] op_sel_hi:[1,0,0]\n"
"v_fma_mix_f32 v225, v152, v209, v217 op_sel:[1,0,0] op_sel_hi:[1,0,0]\n"
"v_mfma_f32_16x16x32_bf16 v[16:19], a[208:211], v[128:131], v[16:19]\n"
"ds_read_b64_tr_b16 a[246:247], v235 offset:46088\n"
"v_cvt_pk_bf16_f32 v152, v224, v225\n"
"v_fma_mix_f32 v226, v153, v210, v218 op_sel:[0,0,0] op_sel_hi:[1,0,0]\n"
"v_mfma_f32_16x16x32_bf16 v[48:51], a[208:211], v[132:135], v[48:51]\n"
"buffer_load_dwordx4 v[184:187], v249, s[64:67], s56 offen offset:3072\n"
"v_fma_mix_f32 v227, v153, v211, v219 op_sel:[1,0,0] op_sel_hi:[1,0,0]\n"
"v_mfma_f32_16x16x32_bf16 v[80:83], a[208:211], v[136:139], v[80:83]\n"
"ds_read_b64_tr_b16 a[248:249], v235 offset:42496\n"
"v_cvt_pk_bf16_f32 v153, v226, v227\n"
"v_fma_mix_f32 v224, v154, v212, v220 op_sel:[0,0,0] op_sel_hi:[1,0,0]\n"
"v_mfma_f32_16x16x32_bf16 v[112:115], a[208:211], v[140:143], v[112:115]\n"
"ds_read_b64_tr_b16 a[250:251], v235 offset:46592\n"
"v_fma_mix_f32 v225, v154, v213, v221 op_sel:[1,0,0] op_sel_hi:[1,0,0]\n"
"v_mfma_f32_16x16x32_bf16 v[20:23], a[212:215], v[128:131], v[20:23]\n"
"v_cvt_pk_bf16_f32 v154, v224, v225\n"
"v_fma_mix_f32 v226, v155, v214, v222 op_sel:[0,0,0] op_sel_hi:[1,0,0]\n"
"v_mfma_f32_16x16x32_bf16 v[52:55], a[212:215], v[132:135], v[52:55]\n"
"ds_read_b64_tr_b16 a[252:253], v235 offset:42504\n"
"v_fma_mix_f32 v227, v155, v215, v223 op_sel:[1,0,0] op_sel_hi:[1,0,0]\n"
"v_cvt_pk_bf16_f32 v155, v226, v227\n"
"v_mfma_f32_16x16x32_bf16 v[84:87], a[212:215], v[136:139], v[84:87]\n"
"ds_read_b64_tr_b16 a[254:255], v235 offset:46600\n"
"v_fma_mix_f32 v224, v156, v208, v216 op_sel:[0,0,0] op_sel_hi:[1,0,0]\n"
"v_mfma_f32_16x16x32_bf16 v[116:119], a[212:215], v[140:143], v[116:119]\n"
"buffer_load_dwordx4 v[188:191], v250, s[64:67], s56 offen offset:3072\n"
"v_fma_mix_f32 v225, v156, v209, v217 op_sel:[1,0,0] op_sel_hi:[1,0,0]\n"
"v_cvt_pk_bf16_f32 v156, v224, v225\n"
"v_mfma_f32_16x16x32_bf16 v[24:27], a[216:219], v[128:131], v[24:27]\n"
"v_fma_mix_f32 v226, v157, v210, v218 op_sel:[0,0,0] op_sel_hi:[1,0,0]\n"
"v_mfma_f32_16x16x32_bf16 v[56:59], a[216:219], v[132:135], v[56:59]\n"
"v_fma_mix_f32 v227, v157, v211, v219 op_sel:[1,0,0] op_sel_hi:[1,0,0]\n"
"v_cvt_pk_bf16_f32 v157, v226, v227\n"
"v_mfma_f32_16x16x32_bf16 v[88:91], a[216:219], v[136:139], v[88:91]\n"
"v_fma_mix_f32 v224, v158, v212, v220 op_sel:[0,0,0] op_sel_hi:[1,0,0]\n"
"v_fma_mix_f32 v225, v158, v213, v221 op_sel:[1,0,0] op_sel_hi:[1,0,0]\n"
"v_mfma_f32_16x16x32_bf16 v[120:123], a[216:219], v[140:143], v[120:123]\n"
"v_cvt_pk_bf16_f32 v158, v224, v225\n"
"v_mfma_f32_16x16x32_bf16 v[28:31], a[220:223], v[128:131], v[28:31]\n"
"v_fma_mix_f32 v226, v159, v214, v222 op_sel:[0,0,0] op_sel_hi:[1,0,0]\n"
"v_fma_mix_f32 v227, v159, v215, v223 op_sel:[1,0,0] op_sel_hi:[1,0,0]\n"
"v_mfma_f32_16x16x32_bf16 v[60:63], a[220:223], v[132:135], v[60:63]\n"
"v_cvt_pk_bf16_f32 v159, v226, v227\n"
"v_mfma_f32_16x16x32_bf16 v[92:95], a[220:223], v[136:139], v[92:95]\n"
"s_add_u32 s56, s56, 0x1000\n"
"v_mfma_f32_16x16x32_bf16 v[124:127], a[220:223], v[140:143], v[124:127]\n"
"s_waitcnt lgkmcnt(0)\n"
"s_waitcnt vmcnt(6)\n"
"v_mfma_f32_16x16x32_bf16 v[0:3], a[224:227], v[144:147], v[0:3]\n"
"s_barrier\n"
"ds_read_b64_tr_b16 a[192:193], v235 offset:49152\n"
"v_fma_mix_f32 v224, v160, v192, v200 op_sel:[0,0,0] op_sel_hi:[1,0,0]\n"
"v_fma_mix_f32 v225, v160, v193, v201 op_sel:[1,0,0] op_sel_hi:[1,0,0]\n"
"v_mfma_f32_16x16x32_bf16 v[32:35], a[224:227], v[148:151], v[32:35]\n"
"ds_read_b64_tr_b16 a[194:195], v235 offset:53248\n"
"v_cvt_pk_bf16_f32 v160, v224, v225\n"
"v_fma_mix_f32 v226, v161, v194, v202 op_sel:[0,0,0] op_sel_hi:[1,0,0]\n"
"v_mfma_f32_16x16x32_bf16 v[64:67], a[224:227], v[152:155], v[64:67]\n"
"ds_read_b128 v[208:211], v247 offset:1792\n"
"v_fma_mix_f32 v227, v161, v195, v203 op_sel:[1,0,0] op_sel_hi:[1,0,0]\n"
"v_mfma_f32_16x16x32_bf16 v[96:99], a[224:227], v[156:159], v[96:99]\n"
"ds_read_b64_tr_b16 a[196:197], v235 offset:49160\n"
"v_cvt_pk_bf16_f32 v161, v226, v227\n"
"v_fma_mix_f32 v224, v162, v196, v204 op_sel:[0,0,0] op_sel_hi:[1,0,0]\n"
"v_mfma_f32_16x16x32_bf16 v[4:7], a[228:231], v[144:147], v[4:7]\n"
"ds_read_b64_tr_b16 a[198:199], v235 offset:53256\n"
"v_fma_mix_f32 v225, v162, v197, v205 op_sel:[1,0,0] op_sel_hi:[1,0,0]\n"
"v_mfma_f32_16x16x32_bf16 v[36:39], a[228:231], v[148:151], v[36:39]\n"
"ds_read_b128 v[212:215], v247 offset:1808\n"
"buffer_load_dwordx4 v[128:131], v246, s[64:67], s56 offen offset:0\n"
"v_cvt_pk_bf16_f32 v162, v224, v225\n"
"v_fma_mix_f32 v226, v163, v198, v206 op_sel:[0,0,0] op_sel_hi:[1,0,0]\n"
"v_mfma_f32_16x16x32_bf16 v[68:71], a[228:231], v[152:155], v[68:71]\n"
"ds_read_b64_tr_b16 a[200:201], v235 offset:49664\n"
"v_fma_mix_f32 v227, v163, v199, v207 op_sel:[1,0,0] op_sel_hi:[1,0,0]\n"
"v_cvt_pk_bf16_f32 v163, v226, v227\n"
"v_mfma_f32_16x16x32_bf16 v[100:103], a[228:231], v[156:159], v[100:103]\n"
"ds_read_b64_tr_b16 a[202:203], v235 offset:53760\n"
"v_fma_mix_f32 v224, v164, v192, v200 op_sel:[0,0,0] op_sel_hi:[1,0,0]\n"
"v_mfma_f32_16x16x32_bf16 v[8:11], a[232:235], v[144:147], v[8:11]\n"
"ds_read_b128 v[216:219], v247 offset:1824\n"
"v_fma_mix_f32 v225, v164, v193, v201 op_sel:[1,0,0] op_sel_hi:[1,0,0]\n"
"v_cvt_pk_bf16_f32 v164, v224, v225\n"
"v_mfma_f32_16x16x32_bf16 v[40:43], a[232:235], v[148:151], v[40:43]\n"
"ds_read_b64_tr_b16 a[204:205], v235 offset:49672\n"
"v_fma_mix_f32 v226, v165, v194, v202 op_sel:[0,0,0] op_sel_hi:[1,0,0]\n"
"v_mfma_f32_16x16x32_bf16 v[72:75], a[232:235], v[152:155], v[72:75]\n"
"ds_read_b64_tr_b16 a[206:207], v235 offset:53768\n"
"v_fma_mix_f32 v227, v165, v195, v203 op_sel:[1,0,0] op_sel_hi:[1,0,0]\n"
"v_cvt_pk_bf16_f32 v165, v226, v227\n"
"v_mfma_f32_16x16x32_bf16 v[104:107], a[232:235], v[156:159], v[104:107]\n"
"ds_read_b128 v[220:223], v247 offset:1840\n"
"buffer_load_dwordx4 v[132:135], v248, s[64:67], s56 offen offset:0\n"
"v_fma_mix_f32 v224, v166, v196, v204 op_sel:[0,0,0] op_sel_hi:[1,0,0]\n"
"v_fma_mix_f32 v225, v166, v197, v205 op_sel:[1,0,0] op_sel_hi:[1,0,0]\n"
"v_mfma_f32_16x16x32_bf16 v[12:15], a[236:239], v[144:147], v[12:15]\n"
"ds_read_b64_tr_b16 a[208:209], v235 offset:50176\n"
"v_cvt_pk_bf16_f32 v166, v224, v225\n"
"v_mfma_f32_16x16x32_bf16 v[44:47], a[236:239], v[148:151], v[44:47]\n"
"ds_read_b64_tr_b16 a[210:211], v235 offset:54272\n"
"v_fma_mix_f32 v226, v167, v198, v206 op_sel:[0,0,0] op_sel_hi:[1,0,0]\n"
"v_fma_mix_f32 v227, v167, v199, v207 op_sel:[1,0,0] op_sel_hi:[1,0,0]\n"
"v_mfma_f32_16x16x32_bf16 v[76:79], a[236:239], v[152:155], v[76:79]\n"
"v_cvt_pk_bf16_f32 v167, v226, v227\n"
"v_mfma_f32_16x16x32_bf16 v[108:111], a[236:239], v[156:159], v[108:111]\n"
"ds_read_b64_tr_b16 a[212:213], v235 offset:50184\n"
"v_fma_mix_f32 v224, v168, v192, v200 op_sel:[0,0,0] op_sel_hi:[1,0,0]\n"
"v_fma_mix_f32 v225, v168, v193, v201 op_sel:[1,0,0] op_sel_hi:[1,0,0]\n"
"v_mfma_f32_16x16x32_bf16 v[16:19], a[240:243], v[144:147], v[16:19]\n"
"ds_read_b64_tr_b16 a[214:215], v235 offset:54280\n"
"v_cvt_pk_bf16_f32 v168, v224, v225\n"
"v_fma_mix_f32 v226, v169, v194, v202 op_sel:[0,0,0] op_sel_hi:[1,0,0]\n"
"v_mfma_f32_16x16x32_bf16 v[48:51], a[240:243], v[148:151], v[48:51]\n"
"s_add_u32 m0, s51, 0x4000\n"
"buffer_load_dwordx4 v[136:139], v249, s[64:67], s56 offen offset:0\n"
"v_fma_mix_f32 v227, v169, v195, v203 op_sel:[1,0,0] op_sel_hi:[1,0,0]\n"
"v_mfma_f32_16x16x32_bf16 v[80:83], a[240:243], v[152:155], v[80:83]\n"
"buffer_load_dwordx4 v242, s[28:31], s49 offen offset:0 lds\n"
"ds_read_b64_tr_b16 a[216:217], v235 offset:50688\n"
"v_cvt_pk_bf16_f32 v169, v226, v227\n"
"v_fma_mix_f32 v224, v170, v196, v204 op_sel:[0,0,0] op_sel_hi:[1,0,0]\n"
"v_mfma_f32_16x16x32_bf16 v[112:115], a[240:243], v[156:159], v[112:115]\n"
"ds_read_b64_tr_b16 a[218:219], v235 offset:54784\n"
"v_fma_mix_f32 v225, v170, v197, v205 op_sel:[1,0,0] op_sel_hi:[1,0,0]\n"
"v_mfma_f32_16x16x32_bf16 v[20:23], a[244:247], v[144:147], v[20:23]\n"
"v_cvt_pk_bf16_f32 v170, v224, v225\n"
"v_fma_mix_f32 v226, v171, v198, v206 op_sel:[0,0,0] op_sel_hi:[1,0,0]\n"
"v_mfma_f32_16x16x32_bf16 v[52:55], a[244:247], v[148:151], v[52:55]\n"
"buffer_load_dwordx4 v243, s[28:31], s49 offen offset:1024 lds\n"
"ds_read_b64_tr_b16 a[220:221], v235 offset:50696\n"
"v_fma_mix_f32 v227, v171, v199, v207 op_sel:[1,0,0] op_sel_hi:[1,0,0]\n"
"v_cvt_pk_bf16_f32 v171, v226, v227\n"
"v_mfma_f32_16x16x32_bf16 v[84:87], a[244:247], v[152:155], v[84:87]\n"
"ds_read_b64_tr_b16 a[222:223], v235 offset:54792\n"
"v_fma_mix_f32 v224, v172, v192, v200 op_sel:[0,0,0] op_sel_hi:[1,0,0]\n"
"v_mfma_f32_16x16x32_bf16 v[116:119], a[244:247], v[156:159], v[116:119]\n"
"buffer_load_dwordx4 v[140:143], v250, s[64:67], s56 offen offset:0\n"
"v_fma_mix_f32 v225, v172, v193, v201 op_sel:[1,0,0] op_sel_hi:[1,0,0]\n"
"v_cvt_pk_bf16_f32 v172, v224, v225\n"
"v_mfma_f32_16x16x32_bf16 v[24:27], a[248:251], v[144:147], v[24:27]\n"
"buffer_load_dwordx4 v244, s[28:31], s49 offen offset:2048 lds\n"
"v_fma_mix_f32 v226, v173, v194, v202 op_sel:[0,0,0] op_sel_hi:[1,0,0]\n"
"v_mfma_f32_16x16x32_bf16 v[56:59], a[248:251], v[148:151], v[56:59]\n"
"v_fma_mix_f32 v227, v173, v195, v203 op_sel:[1,0,0] op_sel_hi:[1,0,0]\n"
"v_cvt_pk_bf16_f32 v173, v226, v227\n"
"v_mfma_f32_16x16x32_bf16 v[88:91], a[248:251], v[152:155], v[88:91]\n"
"v_fma_mix_f32 v224, v174, v196, v204 op_sel:[0,0,0] op_sel_hi:[1,0,0]\n"
"v_fma_mix_f32 v225, v174, v197, v205 op_sel:[1,0,0] op_sel_hi:[1,0,0]\n"
"v_mfma_f32_16x16x32_bf16 v[120:123], a[248:251], v[156:159], v[120:123]\n"
"buffer_load_dwordx4 v245, s[28:31], s49 offen offset:3072 lds\n"
"v_cvt_pk_bf16_f32 v174, v224, v225\n"
"v_mfma_f32_16x16x32_bf16 v[28:31], a[252:255], v[144:147], v[28:31]\n"
"s_add_u32 s49, s49, 0x80000\n"
"v_fma_mix_f32 v226, v175, v198, v206 op_sel:[0,0,0] op_sel_hi:[1,0,0]\n"
"v_fma_mix_f32 v227, v175, v199, v207 op_sel:[1,0,0] op_sel_hi:[1,0,0]\n"
"v_mfma_f32_16x16x32_bf16 v[60:63], a[252:255], v[148:151], v[60:63]\n"
"v_cvt_pk_bf16_f32 v175, v226, v227\n"
"v_mfma_f32_16x16x32_bf16 v[92:95], a[252:255], v[152:155], v[92:95]\n"
"v_mfma_f32_16x16x32_bf16 v[124:127], a[252:255], v[156:159], v[124:127]\n"
"s_waitcnt lgkmcnt(0)\n"
"s_waitcnt vmcnt(8)\n"
"v_mfma_f32_16x16x32_bf16 v[0:3], a[192:195], v[160:163], v[0:3]\n"
"ds_read_b64_tr_b16 a[224:225], v235 offset:57344\n"
"v_fma_mix_f32 v224, v176, v208, v216 op_sel:[0,0,0] op_sel_hi:[1,0,0]\n"
"v_fma_mix_f32 v225, v176, v209, v217 op_sel:[1,0,0] op_sel_hi:[1,0,0]\n"
"v_mfma_f32_16x16x32_bf16 v[32:35], a[192:195], v[164:167], v[32:35]\n"
"ds_read_b64_tr_b16 a[226:227], v235 offset:61440\n"
"v_cvt_pk_bf16_f32 v176, v224, v225\n"
"v_fma_mix_f32 v226, v177, v210, v218 op_sel:[0,0,0] op_sel_hi:[1,0,0]\n"
"v_mfma_f32_16x16x32_bf16 v[64:67], a[192:195], v[168:171], v[64:67]\n"
"ds_read_b128 v[192:195], v247 offset:2048\n"
"v_fma_mix_f32 v227, v177, v211, v219 op_sel:[1,0,0] op_sel_hi:[1,0,0]\n"
"v_mfma_f32_16x16x32_bf16 v[96:99], a[192:195], v[172:175], v[96:99]\n"
"ds_read_b64_tr_b16 a[228:229], v235 offset:57352\n"
"v_cvt_pk_bf16_f32 v177, v226, v227\n"
"v_fma_mix_f32 v224, v178, v212, v220 op_sel:[0,0,0] op_sel_hi:[1,0,0]\n"
"v_mfma_f32_16x16x32_bf16 v[4:7], a[196:199], v[160:163], v[4:7]\n"
"ds_read_b64_tr_b16 a[230:231], v235 offset:61448\n"
"v_fma_mix_f32 v225, v178, v213, v221 op_sel:[1,0,0] op_sel_hi:[1,0,0]\n"
"v_mfma_f32_16x16x32_bf16 v[36:39], a[196:199], v[164:167], v[36:39]\n"
"ds_read_b128 v[196:199], v247 offset:2064\n"
"buffer_load_dwordx4 v[144:147], v246, s[64:67], s56 offen offset:1024\n"
"v_cvt_pk_bf16_f32 v178, v224, v225\n"
"v_fma_mix_f32 v226, v179, v214, v222 op_sel:[0,0,0] op_sel_hi:[1,0,0]\n"
"v_mfma_f32_16x16x32_bf16 v[68:71], a[196:199], v[168:171], v[68:71]\n"
"ds_read_b64_tr_b16 a[232:233], v235 offset:57856\n"
"v_fma_mix_f32 v227, v179, v215, v223 op_sel:[1,0,0] op_sel_hi:[1,0,0]\n"
"v_cvt_pk_bf16_f32 v179, v226, v227\n"
"v_mfma_f32_16x16x32_bf16 v[100:103], a[196:199], v[172:175], v[100:103]\n"
"ds_read_b64_tr_b16 a[234:235], v235 offset:61952\n"
"v_fma_mix_f32 v224, v180, v208, v216 op_sel:[0,0,0] op_sel_hi:[1,0,0]\n"
"v_mfma_f32_16x16x32_bf16 v[8:11], a[200:203], v[160:163], v[8:11]\n"
"ds_read_b128 v[200:203], v247 offset:2080\n"
"v_fma_mix_f32 v225, v180, v209, v217 op_sel:[1,0,0] op_sel_hi:[1,0,0]\n"
"v_cvt_pk_bf16_f32 v180, v224, v225\n"
"v_mfma_f32_16x16x32_bf16 v[40:43], a[200:203], v[164:167], v[40:43]\n"
"ds_read_b64_tr_b16 a[236:237], v235 offset:57864\n"
"v_fma_mix_f32 v226, v181, v210, v218 op_sel:[0,0,0] op_sel_hi:[1,0,0]\n"
"v_mfma_f32_16x16x32_bf16 v[72:75], a[200:203], v[168:171], v[72:75]\n"
"ds_read_b64_tr_b16 a[238:239], v235 offset:61960\n"
"v_fma_mix_f32 v227, v181, v211, v219 op_sel:[1,0,0] op_sel_hi:[1,0,0]\n"
"v_cvt_pk_bf16_f32 v181, v226, v227\n"
"v_mfma_f32_16x16x32_bf16 v[104:107], a[200:203], v[172:175], v[104:107]\n"
"ds_read_b128 v[204:207], v247 offset:2096\n"
"buffer_load_dwordx4 v[148:151], v248, s[64:67], s56 offen offset:1024\n"
"v_fma_mix_f32 v224, v182, v212, v220 op_sel:[0,0,0] op_sel_hi:[1,0,0]\n"
"v_fma_mix_f32 v225, v182, v213, v221 op_sel:[1,0,0] op_sel_hi:[1,0,0]\n"
"v_mfma_f32_16x16x32_bf16 v[12:15], a[204:207], v[160:163], v[12:15]\n"
"ds_read_b64_tr_b16 a[240:241], v235 offset:58368\n"
"v_cvt_pk_bf16_f32 v182, v224, v225\n"
"v_mfma_f32_16x16x32_bf16 v[44:47], a[204:207], v[164:167], v[44:47]\n"
"ds_read_b64_tr_b16 a[242:243], v235 offset:62464\n"
"v_fma_mix_f32 v226, v183, v214, v222 op_sel:[0,0,0] op_sel_hi:[1,0,0]\n"
"v_fma_mix_f32 v227, v183, v215, v223 op_sel:[1,0,0] op_sel_hi:[1,0,0]\n"
"v_mfma_f32_16x16x32_bf16 v[76:79], a[204:207], v[168:171], v[76:79]\n"
"v_cvt_pk_bf16_f32 v183, v226, v227\n"
"v_mfma_f32_16x16x32_bf16 v[108:111], a[204:207], v[172:175], v[108:111]\n"
"ds_read_b64_tr_b16 a[244:245], v235 offset:58376\n"
"v_fma_mix_f32 v224, v184, v208, v216 op_sel:[0,0,0] op_sel_hi:[1,0,0]\n"
"v_fma_mix_f32 v225, v184, v209, v217 op_sel:[1,0,0] op_sel_hi:[1,0,0]\n"
"v_mfma_f32_16x16x32_bf16 v[16:19], a[208:211], v[160:163], v[16:19]\n"
"ds_read_b64_tr_b16 a[246:247], v235 offset:62472\n"
"v_cvt_pk_bf16_f32 v184, v224, v225\n"
"v_fma_mix_f32 v226, v185, v210, v218 op_sel:[0,0,0] op_sel_hi:[1,0,0]\n"
"v_mfma_f32_16x16x32_bf16 v[48:51], a[208:211], v[164:167], v[48:51]\n"
"buffer_load_dwordx4 v[152:155], v249, s[64:67], s56 offen offset:1024\n"
"v_fma_mix_f32 v227, v185, v211, v219 op_sel:[1,0,0] op_sel_hi:[1,0,0]\n"
"v_mfma_f32_16x16x32_bf16 v[80:83], a[208:211], v[168:171], v[80:83]\n"
"ds_read_b64_tr_b16 a[248:249], v235 offset:58880\n"
"v_cvt_pk_bf16_f32 v185, v226, v227\n"
"v_fma_mix_f32 v224, v186, v212, v220 op_sel:[0,0,0] op_sel_hi:[1,0,0]\n"
"v_mfma_f32_16x16x32_bf16 v[112:115], a[208:211], v[172:175], v[112:115]\n"
"ds_read_b64_tr_b16 a[250:251], v235 offset:62976\n"
"v_fma_mix_f32 v225, v186, v213, v221 op_sel:[1,0,0] op_sel_hi:[1,0,0]\n"
"v_mfma_f32_16x16x32_bf16 v[20:23], a[212:215], v[160:163], v[20:23]\n"
"v_cvt_pk_bf16_f32 v186, v224, v225\n"
"v_fma_mix_f32 v226, v187, v214, v222 op_sel:[0,0,0] op_sel_hi:[1,0,0]\n"
"v_mfma_f32_16x16x32_bf16 v[52:55], a[212:215], v[164:167], v[52:55]\n"
"ds_read_b64_tr_b16 a[252:253], v235 offset:58888\n"
"v_fma_mix_f32 v227, v187, v215, v223 op_sel:[1,0,0] op_sel_hi:[1,0,0]\n"
"v_cvt_pk_bf16_f32 v187, v226, v227\n"
"v_mfma_f32_16x16x32_bf16 v[84:87], a[212:215], v[168:171], v[84:87]\n"
"ds_read_b64_tr_b16 a[254:255], v235 offset:62984\n"
"v_fma_mix_f32 v224, v188, v208, v216 op_sel:[0,0,0] op_sel_hi:[1,0,0]\n"
"v_mfma_f32_16x16x32_bf16 v[116:119], a[212:215], v[172:175], v[116:119]\n"
"buffer_load_dwordx4 v[156:159], v250, s[64:67], s56 offen offset:1024\n"
"v_fma_mix_f32 v225, v188, v209, v217 op_sel:[1,0,0] op_sel_hi:[1,0,0]\n"
"v_cvt_pk_bf16_f32 v188, v224, v225\n"
"v_mfma_f32_16x16x32_bf16 v[24:27], a[216:219], v[160:163], v[24:27]\n"
"v_fma_mix_f32 v226, v189, v210, v218 op_sel:[0,0,0] op_sel_hi:[1,0,0]\n"
"v_mfma_f32_16x16x32_bf16 v[56:59], a[216:219], v[164:167], v[56:59]\n"
"v_fma_mix_f32 v227, v189, v211, v219 op_sel:[1,0,0] op_sel_hi:[1,0,0]\n"
"v_cvt_pk_bf16_f32 v189, v226, v227\n"
"v_mfma_f32_16x16x32_bf16 v[88:91], a[216:219], v[168:171], v[88:91]\n"
"v_fma_mix_f32 v224, v190, v212, v220 op_sel:[0,0,0] op_sel_hi:[1,0,0]\n"
"v_fma_mix_f32 v225, v190, v213, v221 op_sel:[1,0,0] op_sel_hi:[1,0,0]\n"
"v_mfma_f32_16x16x32_bf16 v[120:123], a[216:219], v[172:175], v[120:123]\n"
"v_cvt_pk_bf16_f32 v190, v224, v225\n"
"v_mfma_f32_16x16x32_bf16 v[28:31], a[220:223], v[160:163], v[28:31]\n"
"v_fma_mix_f32 v226, v191, v214, v222 op_sel:[0,0,0] op_sel_hi:[1,0,0]\n"
"v_fma_mix_f32 v227, v191, v215, v223 op_sel:[1,0,0] op_sel_hi:[1,0,0]\n"
"v_mfma_f32_16x16x32_bf16 v[60:63], a[220:223], v[164:167], v[60:63]\n"
"v_cvt_pk_bf16_f32 v191, v226, v227\n"
"v_mfma_f32_16x16x32_bf16 v[92:95], a[220:223], v[168:171], v[92:95]\n"
"v_mfma_f32_16x16x32_bf16 v[124:127], a[220:223], v[172:175], v[124:127]\n"
"s_waitcnt lgkmcnt(0)\n"
"s_waitcnt vmcnt(6)\n"
"v_mfma_f32_16x16x32_bf16 v[0:3], a[224:227], v[176:179], v[0:3]\n"
"s_barrier\n"
"ds_read_b64_tr_b16 a[192:193], v235 offset:0\n"
"v_fma_mix_f32 v224, v128, v192, v200 op_sel:[0,0,0] op_sel_hi:[1,0,0]\n"
"v_fma_mix_f32 v225, v128, v193, v201 op_sel:[1,0,0] op_sel_hi:[1,0,0]\n"
"v_mfma_f32_16x16x32_bf16 v[32:35], a[224:227], v[180:183], v[32:35]\n"
"ds_read_b64_tr_b16 a[194:195], v235 offset:4096\n"
"v_cvt_pk_bf16_f32 v128, v224, v225\n"
"v_fma_mix_f32 v226, v129, v194, v202 op_sel:[0,0,0] op_sel_hi:[1,0,0]\n"
"v_mfma_f32_16x16x32_bf16 v[64:67], a[224:227], v[184:187], v[64:67]\n"
"ds_read_b128 v[208:211], v247 offset:2304\n"
"v_fma_mix_f32 v227, v129, v195, v203 op_sel:[1,0,0] op_sel_hi:[1,0,0]\n"
"v_mfma_f32_16x16x32_bf16 v[96:99], a[224:227], v[188:191], v[96:99]\n"
"ds_read_b64_tr_b16 a[196:197], v235 offset:8\n"
"v_cvt_pk_bf16_f32 v129, v226, v227\n"
"v_fma_mix_f32 v224, v130, v196, v204 op_sel:[0,0,0] op_sel_hi:[1,0,0]\n"
"v_mfma_f32_16x16x32_bf16 v[4:7], a[228:231], v[176:179], v[4:7]\n"
"ds_read_b64_tr_b16 a[198:199], v235 offset:4104\n"
"v_fma_mix_f32 v225, v130, v197, v205 op_sel:[1,0,0] op_sel_hi:[1,0,0]\n"
"v_mfma_f32_16x16x32_bf16 v[36:39], a[228:231], v[180:183], v[36:39]\n"
"ds_read_b128 v[212:215], v247 offset:2320\n"
"buffer_load_dwordx4 v[160:163], v246, s[64:67], s56 offen offset:2048\n"
"v_cvt_pk_bf16_f32 v130, v224, v225\n"
"v_fma_mix_f32 v226, v131, v198, v206 op_sel:[0,0,0] op_sel_hi:[1,0,0]\n"
"v_mfma_f32_16x16x32_bf16 v[68:71], a[228:231], v[184:187], v[68:71]\n"
"ds_read_b64_tr_b16 a[200:201], v235 offset:512\n"
"v_fma_mix_f32 v227, v131, v199, v207 op_sel:[1,0,0] op_sel_hi:[1,0,0]\n"
"v_cvt_pk_bf16_f32 v131, v226, v227\n"
"v_mfma_f32_16x16x32_bf16 v[100:103], a[228:231], v[188:191], v[100:103]\n"
"ds_read_b64_tr_b16 a[202:203], v235 offset:4608\n"
"v_fma_mix_f32 v224, v132, v192, v200 op_sel:[0,0,0] op_sel_hi:[1,0,0]\n"
"v_mfma_f32_16x16x32_bf16 v[8:11], a[232:235], v[176:179], v[8:11]\n"
"ds_read_b128 v[216:219], v247 offset:2336\n"
"v_fma_mix_f32 v225, v132, v193, v201 op_sel:[1,0,0] op_sel_hi:[1,0,0]\n"
"v_cvt_pk_bf16_f32 v132, v224, v225\n"
"v_mfma_f32_16x16x32_bf16 v[40:43], a[232:235], v[180:183], v[40:43]\n"
"ds_read_b64_tr_b16 a[204:205], v235 offset:520\n"
"v_fma_mix_f32 v226, v133, v194, v202 op_sel:[0,0,0] op_sel_hi:[1,0,0]\n"
"v_mfma_f32_16x16x32_bf16 v[72:75], a[232:235], v[184:187], v[72:75]\n"
"ds_read_b64_tr_b16 a[206:207], v235 offset:4616\n"
"v_fma_mix_f32 v227, v133, v195, v203 op_sel:[1,0,0] op_sel_hi:[1,0,0]\n"
"v_cvt_pk_bf16_f32 v133, v226, v227\n"
"v_mfma_f32_16x16x32_bf16 v[104:107], a[232:235], v[188:191], v[104:107]\n"
"ds_read_b128 v[220:223], v247 offset:2352\n"
"buffer_load_dwordx4 v[164:167], v248, s[64:67], s56 offen offset:2048\n"
"v_fma_mix_f32 v224, v134, v196, v204 op_sel:[0,0,0] op_sel_hi:[1,0,0]\n"
"v_fma_mix_f32 v225, v134, v197, v205 op_sel:[1,0,0] op_sel_hi:[1,0,0]\n"
"v_mfma_f32_16x16x32_bf16 v[12:15], a[236:239], v[176:179], v[12:15]\n"
"ds_read_b64_tr_b16 a[208:209], v235 offset:1024\n"
"v_cvt_pk_bf16_f32 v134, v224, v225\n"
"v_mfma_f32_16x16x32_bf16 v[44:47], a[236:239], v[180:183], v[44:47]\n"
"ds_read_b64_tr_b16 a[210:211], v235 offset:5120\n"
"v_fma_mix_f32 v226, v135, v198, v206 op_sel:[0,0,0] op_sel_hi:[1,0,0]\n"
"v_fma_mix_f32 v227, v135, v199, v207 op_sel:[1,0,0] op_sel_hi:[1,0,0]\n"
"v_mfma_f32_16x16x32_bf16 v[76:79], a[236:239], v[184:187], v[76:79]\n"
"v_cvt_pk_bf16_f32 v135, v226, v227\n"
"v_mfma_f32_16x16x32_bf16 v[108:111], a[236:239], v[188:191], v[108:111]\n"
"ds_read_b64_tr_b16 a[212:213], v235 offset:1032\n"
"v_fma_mix_f32 v224, v136, v192, v200 op_sel:[0,0,0] op_sel_hi:[1,0,0]\n"
"v_fma_mix_f32 v225, v136, v193, v201 op_sel:[1,0,0] op_sel_hi:[1,0,0]\n"
"v_mfma_f32_16x16x32_bf16 v[16:19], a[240:243], v[176:179], v[16:19]\n"
"ds_read_b64_tr_b16 a[214:215], v235 offset:5128\n"
"v_cvt_pk_bf16_f32 v136, v224, v225\n"
"v_fma_mix_f32 v226, v137, v194, v202 op_sel:[0,0,0] op_sel_hi:[1,0,0]\n"
"v_mfma_f32_16x16x32_bf16 v[48:51], a[240:243], v[180:183], v[48:51]\n"
"s_add_u32 m0, s51, 0x8000\n"
"buffer_load_dwordx4 v[168:171], v249, s[64:67], s56 offen offset:2048\n"
"v_fma_mix_f32 v227, v137, v195, v203 op_sel:[1,0,0] op_sel_hi:[1,0,0]\n"
"v_mfma_f32_16x16x32_bf16 v[80:83], a[240:243], v[184:187], v[80:83]\n"
"buffer_load_dwordx4 v242, s[28:31], s49 offen offset:0 lds\n"
"ds_read_b64_tr_b16 a[216:217], v235 offset:1536\n"
"v_cvt_pk_bf16_f32 v137, v226, v227\n"
"v_fma_mix_f32 v224, v138, v196, v204 op_sel:[0,0,0] op_sel_hi:[1,0,0]\n"
"v_mfma_f32_16x16x32_bf16 v[112:115], a[240:243], v[188:191], v[112:115]\n"
"ds_read_b64_tr_b16 a[218:219], v235 offset:5632\n"
"v_fma_mix_f32 v225, v138, v197, v205 op_sel:[1,0,0] op_sel_hi:[1,0,0]\n"
"v_mfma_f32_16x16x32_bf16 v[20:23], a[244:247], v[176:179], v[20:23]\n"
"v_cvt_pk_bf16_f32 v138, v224, v225\n"
"v_fma_mix_f32 v226, v139, v198, v206 op_sel:[0,0,0] op_sel_hi:[1,0,0]\n"
"v_mfma_f32_16x16x32_bf16 v[52:55], a[244:247], v[180:183], v[52:55]\n"
"buffer_load_dwordx4 v243, s[28:31], s49 offen offset:1024 lds\n"
"ds_read_b64_tr_b16 a[220:221], v235 offset:1544\n"
"v_fma_mix_f32 v227, v139, v199, v207 op_sel:[1,0,0] op_sel_hi:[1,0,0]\n"
"v_cvt_pk_bf16_f32 v139, v226, v227\n"
"v_mfma_f32_16x16x32_bf16 v[84:87], a[244:247], v[184:187], v[84:87]\n"
"ds_read_b64_tr_b16 a[222:223], v235 offset:5640\n"
"v_fma_mix_f32 v224, v140, v192, v200 op_sel:[0,0,0] op_sel_hi:[1,0,0]\n"
"v_mfma_f32_16x16x32_bf16 v[116:119], a[244:247], v[188:191], v[116:119]\n"
"buffer_load_dwordx4 v[172:175], v250, s[64:67], s56 offen offset:2048\n"
"v_fma_mix_f32 v225, v140, v193, v201 op_sel:[1,0,0] op_sel_hi:[1,0,0]\n"
"v_cvt_pk_bf16_f32 v140, v224, v225\n"
"v_mfma_f32_16x16x32_bf16 v[24:27], a[248:251], v[176:179], v[24:27]\n"
"buffer_load_dwordx4 v244, s[28:31], s49 offen offset:2048 lds\n"
"v_fma_mix_f32 v226, v141, v194, v202 op_sel:[0,0,0] op_sel_hi:[1,0,0]\n"
"v_mfma_f32_16x16x32_bf16 v[56:59], a[248:251], v[180:183], v[56:59]\n"
"v_fma_mix_f32 v227, v141, v195, v203 op_sel:[1,0,0] op_sel_hi:[1,0,0]\n"
"v_cvt_pk_bf16_f32 v141, v226, v227\n"
"v_mfma_f32_16x16x32_bf16 v[88:91], a[248:251], v[184:187], v[88:91]\n"
"v_fma_mix_f32 v224, v142, v196, v204 op_sel:[0,0,0] op_sel_hi:[1,0,0]\n"
"v_fma_mix_f32 v225, v142, v197, v205 op_sel:[1,0,0] op_sel_hi:[1,0,0]\n"
"v_mfma_f32_16x16x32_bf16 v[120:123], a[248:251], v[188:191], v[120:123]\n"
"buffer_load_dwordx4 v245, s[28:31], s49 offen offset:3072 lds\n"
"v_cvt_pk_bf16_f32 v142, v224, v225\n"
"v_mfma_f32_16x16x32_bf16 v[28:31], a[252:255], v[176:179], v[28:31]\n"
"s_add_u32 s49, s49, 0x80000\n"
"v_fma_mix_f32 v226, v143, v198, v206 op_sel:[0,0,0] op_sel_hi:[1,0,0]\n"
"v_fma_mix_f32 v227, v143, v199, v207 op_sel:[1,0,0] op_sel_hi:[1,0,0]\n"
"v_mfma_f32_16x16x32_bf16 v[60:63], a[252:255], v[180:183], v[60:63]\n"
"v_cvt_pk_bf16_f32 v143, v226, v227\n"
"v_mfma_f32_16x16x32_bf16 v[92:95], a[252:255], v[184:187], v[92:95]\n"
"v_mfma_f32_16x16x32_bf16 v[124:127], a[252:255], v[188:191], v[124:127]\n"
"s_waitcnt lgkmcnt(0)\n"
"s_waitcnt vmcnt(8)\n"
"v_mfma_f32_16x16x32_bf16 v[0:3], a[192:195], v[128:131], v[0:3]\n"
"ds_read_b64_tr_b16 a[224:225], v235 offset:8192\n"
"v_fma_mix_f32 v224, v144, v208, v216 op_sel:[0,0,0] op_sel_hi:[1,0,0]\n"
"v_fma_mix_f32 v225, v144, v209, v217 op_sel:[1,0,0] op_sel_hi:[1,0,0]\n"
"v_mfma_f32_16x16x32_bf16 v[32:35], a[192:195], v[132:135], v[32:35]\n"
"ds_read_b64_tr_b16 a[226:227], v235 offset:12288\n"
"v_cvt_pk_bf16_f32 v144, v224, v225\n"
"v_fma_mix_f32 v226, v145, v210, v218 op_sel:[0,0,0] op_sel_hi:[1,0,0]\n"
"v_mfma_f32_16x16x32_bf16 v[64:67], a[192:195], v[136:139], v[64:67]\n"
"ds_read_b128 v[192:195], v247 offset:2560\n"
"v_fma_mix_f32 v227, v145, v211, v219 op_sel:[1,0,0] op_sel_hi:[1,0,0]\n"
"v_mfma_f32_16x16x32_bf16 v[96:99], a[192:195], v[140:143], v[96:99]\n"
"ds_read_b64_tr_b16 a[228:229], v235 offset:8200\n"
"v_cvt_pk_bf16_f32 v145, v226, v227\n"
"v_fma_mix_f32 v224, v146, v212, v220 op_sel:[0,0,0] op_sel_hi:[1,0,0]\n"
"v_mfma_f32_16x16x32_bf16 v[4:7], a[196:199], v[128:131], v[4:7]\n"
"ds_read_b64_tr_b16 a[230:231], v235 offset:12296\n"
"v_fma_mix_f32 v225, v146, v213, v221 op_sel:[1,0,0] op_sel_hi:[1,0,0]\n"
"v_mfma_f32_16x16x32_bf16 v[36:39], a[196:199], v[132:135], v[36:39]\n"
"ds_read_b128 v[196:199], v247 offset:2576\n"
"buffer_load_dwordx4 v[176:179], v246, s[64:67], s56 offen offset:3072\n"
"v_cvt_pk_bf16_f32 v146, v224, v225\n"
"v_fma_mix_f32 v226, v147, v214, v222 op_sel:[0,0,0] op_sel_hi:[1,0,0]\n"
"v_mfma_f32_16x16x32_bf16 v[68:71], a[196:199], v[136:139], v[68:71]\n"
"ds_read_b64_tr_b16 a[232:233], v235 offset:8704\n"
"v_fma_mix_f32 v227, v147, v215, v223 op_sel:[1,0,0] op_sel_hi:[1,0,0]\n"
"v_cvt_pk_bf16_f32 v147, v226, v227\n"
"v_mfma_f32_16x16x32_bf16 v[100:103], a[196:199], v[140:143], v[100:103]\n"
"ds_read_b64_tr_b16 a[234:235], v235 offset:12800\n"
"v_fma_mix_f32 v224, v148, v208, v216 op_sel:[0,0,0] op_sel_hi:[1,0,0]\n"
"v_mfma_f32_16x16x32_bf16 v[8:11], a[200:203], v[128:131], v[8:11]\n"
"ds_read_b128 v[200:203], v247 offset:2592\n"
"v_fma_mix_f32 v225, v148, v209, v217 op_sel:[1,0,0] op_sel_hi:[1,0,0]\n"
"v_cvt_pk_bf16_f32 v148, v224, v225\n"
"v_mfma_f32_16x16x32_bf16 v[40:43], a[200:203], v[132:135], v[40:43]\n"
"ds_read_b64_tr_b16 a[236:237], v235 offset:8712\n"
"v_fma_mix_f32 v226, v149, v210, v218 op_sel:[0,0,0] op_sel_hi:[1,0,0]\n"
"v_mfma_f32_16x16x32_bf16 v[72:75], a[200:203], v[136:139], v[72:75]\n"
"ds_read_b64_tr_b16 a[238:239], v235 offset:12808\n"
"v_fma_mix_f32 v227, v149, v211, v219 op_sel:[1,0,0] op_sel_hi:[1,0,0]\n"
"v_cvt_pk_bf16_f32 v149, v226, v227\n"
"v_mfma_f32_16x16x32_bf16 v[104:107], a[200:203], v[140:143], v[104:107]\n"
"ds_read_b128 v[204:207], v247 offset:2608\n"
"buffer_load_dwordx4 v[180:183], v248, s[64:67], s56 offen offset:3072\n"
"v_fma_mix_f32 v224, v150, v212, v220 op_sel:[0,0,0] op_sel_hi:[1,0,0]\n"
"v_fma_mix_f32 v225, v150, v213, v221 op_sel:[1,0,0] op_sel_hi:[1,0,0]\n"
"v_mfma_f32_16x16x32_bf16 v[12:15], a[204:207], v[128:131], v[12:15]\n"
"ds_read_b64_tr_b16 a[240:241], v235 offset:9216\n"
"v_cvt_pk_bf16_f32 v150, v224, v225\n"
"v_mfma_f32_16x16x32_bf16 v[44:47], a[204:207], v[132:135], v[44:47]\n"
"ds_read_b64_tr_b16 a[242:243], v235 offset:13312\n"
"v_fma_mix_f32 v226, v151, v214, v222 op_sel:[0,0,0] op_sel_hi:[1,0,0]\n"
"v_fma_mix_f32 v227, v151, v215, v223 op_sel:[1,0,0] op_sel_hi:[1,0,0]\n"
"v_mfma_f32_16x16x32_bf16 v[76:79], a[204:207], v[136:139], v[76:79]\n"
"v_cvt_pk_bf16_f32 v151, v226, v227\n"
"v_mfma_f32_16x16x32_bf16 v[108:111], a[204:207], v[140:143], v[108:111]\n"
"ds_read_b64_tr_b16 a[244:245], v235 offset:9224\n"
"v_fma_mix_f32 v224, v152, v208, v216 op_sel:[0,0,0] op_sel_hi:[1,0,0]\n"
"v_fma_mix_f32 v225, v152, v209, v217 op_sel:[1,0,0] op_sel_hi:[1,0,0]\n"
"v_mfma_f32_16x16x32_bf16 v[16:19], a[208:211], v[128:131], v[16:19]\n"
"ds_read_b64_tr_b16 a[246:247], v235 offset:13320\n"
"v_cvt_pk_bf16_f32 v152, v224, v225\n"
"v_fma_mix_f32 v226, v153, v210, v218 op_sel:[0,0,0] op_sel_hi:[1,0,0]\n"
"v_mfma_f32_16x16x32_bf16 v[48:51], a[208:211], v[132:135], v[48:51]\n"
"s_add_u32 m0, s50, 0\n"
"buffer_load_dwordx4 v[184:187], v249, s[64:67], s56 offen offset:3072\n"
"v_fma_mix_f32 v227, v153, v211, v219 op_sel:[1,0,0] op_sel_hi:[1,0,0]\n"
"v_mfma_f32_16x16x32_bf16 v[80:83], a[208:211], v[136:139], v[80:83]\n"
"buffer_load_dwordx4 v236, s[20:23], s47 offen offset:0 lds\n"
"ds_read_b64_tr_b16 a[248:249], v235 offset:9728\n"
"v_cvt_pk_bf16_f32 v153, v226, v227\n"
"v_fma_mix_f32 v224, v154, v212, v220 op_sel:[0,0,0] op_sel_hi:[1,0,0]\n"
"v_mfma_f32_16x16x32_bf16 v[112:115], a[208:211], v[140:143], v[112:115]\n"
"ds_read_b64_tr_b16 a[250:251], v235 offset:13824\n"
"v_fma_mix_f32 v225, v154, v213, v221 op_sel:[1,0,0] op_sel_hi:[1,0,0]\n"
"v_mfma_f32_16x16x32_bf16 v[20:23], a[212:215], v[128:131], v[20:23]\n"
"v_cvt_pk_bf16_f32 v154, v224, v225\n"
"v_fma_mix_f32 v226, v155, v214, v222 op_sel:[0,0,0] op_sel_hi:[1,0,0]\n"
"v_mfma_f32_16x16x32_bf16 v[52:55], a[212:215], v[132:135], v[52:55]\n"
"buffer_load_dwordx4 v237, s[20:23], s47 offen offset:1024 lds\n"
"ds_read_b64_tr_b16 a[252:253], v235 offset:9736\n"
"v_fma_mix_f32 v227, v155, v215, v223 op_sel:[1,0,0] op_sel_hi:[1,0,0]\n"
"v_cvt_pk_bf16_f32 v155, v226, v227\n"
"v_mfma_f32_16x16x32_bf16 v[84:87], a[212:215], v[136:139], v[84:87]\n"
"ds_read_b64_tr_b16 a[254:255], v235 offset:13832\n"
"v_fma_mix_f32 v224, v156, v208, v216 op_sel:[0,0,0] op_sel_hi:[1,0,0]\n"
"v_mfma_f32_16x16x32_bf16 v[116:119], a[212:215], v[140:143], v[116:119]\n"
"buffer_load_dwordx4 v[188:191], v250, s[64:67], s56 offen offset:3072\n"
"v_fma_mix_f32 v225, v156, v209, v217 op_sel:[1,0,0] op_sel_hi:[1,0,0]\n"
"v_cvt_pk_bf16_f32 v156, v224, v225\n"
"v_mfma_f32_16x16x32_bf16 v[24:27], a[216:219], v[128:131], v[24:27]\n"
"buffer_load_dwordx4 v238, s[20:23], s47 offen offset:2048 lds\n"
"v_fma_mix_f32 v226, v157, v210, v218 op_sel:[0,0,0] op_sel_hi:[1,0,0]\n"
"v_mfma_f32_16x16x32_bf16 v[56:59], a[216:219], v[132:135], v[56:59]\n"
"v_fma_mix_f32 v227, v157, v211, v219 op_sel:[1,0,0] op_sel_hi:[1,0,0]\n"
"v_cvt_pk_bf16_f32 v157, v226, v227\n"
"v_mfma_f32_16x16x32_bf16 v[88:91], a[216:219], v[136:139], v[88:91]\n"
"v_fma_mix_f32 v224, v158, v212, v220 op_sel:[0,0,0] op_sel_hi:[1,0,0]\n"
"v_fma_mix_f32 v225, v158, v213, v221 op_sel:[1,0,0] op_sel_hi:[1,0,0]\n"
"v_mfma_f32_16x16x32_bf16 v[120:123], a[216:219], v[140:143], v[120:123]\n"
"buffer_load_dwordx4 v239, s[20:23], s47 offen offset:3072 lds\n"
"v_cvt_pk_bf16_f32 v158, v224, v225\n"
"v_mfma_f32_16x16x32_bf16 v[28:31], a[220:223], v[128:131], v[28:31]\n"
"s_add_u32 s47, s47, s55\n"
"v_fma_mix_f32 v226, v159, v214, v222 op_sel:[0,0,0] op_sel_hi:[1,0,0]\n"
"v_fma_mix_f32 v227, v159, v215, v223 op_sel:[1,0,0] op_sel_hi:[1,0,0]\n"
"v_mfma_f32_16x16x32_bf16 v[60:63], a[220:223], v[132:135], v[60:63]\n"
"v_cvt_pk_bf16_f32 v159, v226, v227\n"
"v_mfma_f32_16x16x32_bf16 v[92:95], a[220:223], v[136:139], v[92:95]\n"
"s_add_u32 s56, s56, 0x1000\n"
"v_mfma_f32_16x16x32_bf16 v[124:127], a[220:223], v[140:143], v[124:127]\n"
"s_waitcnt lgkmcnt(0)\n"
"s_waitcnt vmcnt(10)\n"
"v_mfma_f32_16x16x32_bf16 v[0:3], a[224:227], v[144:147], v[0:3]\n"
"s_barrier\n"
"ds_read_b64_tr_b16 a[192:193], v235 offset:16384\n"
"v_fma_mix_f32 v224, v160, v192, v200 op_sel:[0,0,0] op_sel_hi:[1,0,0]\n"
"v_fma_mix_f32 v225, v160, v193, v201 op_sel:[1,0,0] op_sel_hi:[1,0,0]\n"
"v_mfma_f32_16x16x32_bf16 v[32:35], a[224:227], v[148:151], v[32:35]\n"
"ds_read_b64_tr_b16 a[194:195], v235 offset:20480\n"
"v_cvt_pk_bf16_f32 v160, v224, v225\n"
"v_fma_mix_f32 v226, v161, v194, v202 op_sel:[0,0,0] op_sel_hi:[1,0,0]\n"
"v_mfma_f32_16x16x32_bf16 v[64:67], a[224:227], v[152:155], v[64:67]\n"
"ds_read_b128 v[208:211], v247 offset:2816\n"
"v_fma_mix_f32 v227, v161, v195, v203 op_sel:[1,0,0] op_sel_hi:[1,0,0]\n"
"v_mfma_f32_16x16x32_bf16 v[96:99], a[224:227], v[156:159], v[96:99]\n"
"ds_read_b64_tr_b16 a[196:197], v235 offset:16392\n"
"v_cvt_pk_bf16_f32 v161, v226, v227\n"
"v_fma_mix_f32 v224, v162, v196, v204 op_sel:[0,0,0] op_sel_hi:[1,0,0]\n"
"v_mfma_f32_16x16x32_bf16 v[4:7], a[228:231], v[144:147], v[4:7]\n"
"ds_read_b64_tr_b16 a[198:199], v235 offset:20488\n"
"v_fma_mix_f32 v225, v162, v197, v205 op_sel:[1,0,0] op_sel_hi:[1,0,0]\n"
"v_mfma_f32_16x16x32_bf16 v[36:39], a[228:231], v[148:151], v[36:39]\n"
"ds_read_b128 v[212:215], v247 offset:2832\n"
"buffer_load_dwordx4 v[128:131], v246, s[64:67], s56 offen offset:0\n"
"v_cvt_pk_bf16_f32 v162, v224, v225\n"
"v_fma_mix_f32 v226, v163, v198, v206 op_sel:[0,0,0] op_sel_hi:[1,0,0]\n"
"v_mfma_f32_16x16x32_bf16 v[68:71], a[228:231], v[152:155], v[68:71]\n"
"ds_read_b64_tr_b16 a[200:201], v235 offset:16896\n"
"v_fma_mix_f32 v227, v163, v199, v207 op_sel:[1,0,0] op_sel_hi:[1,0,0]\n"
"v_cvt_pk_bf16_f32 v163, v226, v227\n"
"v_mfma_f32_16x16x32_bf16 v[100:103], a[228:231], v[156:159], v[100:103]\n"
"ds_read_b64_tr_b16 a[202:203], v235 offset:20992\n"
"v_fma_mix_f32 v224, v164, v192, v200 op_sel:[0,0,0] op_sel_hi:[1,0,0]\n"
"v_mfma_f32_16x16x32_bf16 v[8:11], a[232:235], v[144:147], v[8:11]\n"
"ds_read_b128 v[216:219], v247 offset:2848\n"
"v_fma_mix_f32 v225, v164, v193, v201 op_sel:[1,0,0] op_sel_hi:[1,0,0]\n"
"v_cvt_pk_bf16_f32 v164, v224, v225\n"
"v_mfma_f32_16x16x32_bf16 v[40:43], a[232:235], v[148:151], v[40:43]\n"
"ds_read_b64_tr_b16 a[204:205], v235 offset:16904\n"
"v_fma_mix_f32 v226, v165, v194, v202 op_sel:[0,0,0] op_sel_hi:[1,0,0]\n"
"v_mfma_f32_16x16x32_bf16 v[72:75], a[232:235], v[152:155], v[72:75]\n"
"ds_read_b64_tr_b16 a[206:207], v235 offset:21000\n"
"v_fma_mix_f32 v227, v165, v195, v203 op_sel:[1,0,0] op_sel_hi:[1,0,0]\n"
"v_cvt_pk_bf16_f32 v165, v226, v227\n"
"v_mfma_f32_16x16x32_bf16 v[104:107], a[232:235], v[156:159], v[104:107]\n"
"ds_read_b128 v[220:223], v247 offset:2864\n"
"buffer_load_dwordx4 v[132:135], v248, s[64:67], s56 offen offset:0\n"
"v_fma_mix_f32 v224, v166, v196, v204 op_sel:[0,0,0] op_sel_hi:[1,0,0]\n"
"v_fma_mix_f32 v225, v166, v197, v205 op_sel:[1,0,0] op_sel_hi:[1,0,0]\n"
"v_mfma_f32_16x16x32_bf16 v[12:15], a[236:239], v[144:147], v[12:15]\n"
"ds_read_b64_tr_b16 a[208:209], v235 offset:17408\n"
"v_cvt_pk_bf16_f32 v166, v224, v225\n"
"v_mfma_f32_16x16x32_bf16 v[44:47], a[236:239], v[148:151], v[44:47]\n"
"ds_read_b64_tr_b16 a[210:211], v235 offset:21504\n"
"v_fma_mix_f32 v226, v167, v198, v206 op_sel:[0,0,0] op_sel_hi:[1,0,0]\n"
"v_fma_mix_f32 v227, v167, v199, v207 op_sel:[1,0,0] op_sel_hi:[1,0,0]\n"
"v_mfma_f32_16x16x32_bf16 v[76:79], a[236:239], v[152:155], v[76:79]\n"
"v_cvt_pk_bf16_f32 v167, v226, v227\n"
"v_mfma_f32_16x16x32_bf16 v[108:111], a[236:239], v[156:159], v[108:111]\n"
"ds_read_b64_tr_b16 a[212:213], v235 offset:17416\n"
"v_fma_mix_f32 v224, v168, v192, v200 op_sel:[0,0,0] op_sel_hi:[1,0,0]\n"
"v_fma_mix_f32 v225, v168, v193, v201 op_sel:[1,0,0] op_sel_hi:[1,0,0]\n"
"v_mfma_f32_16x16x32_bf16 v[16:19], a[240:243], v[144:147], v[16:19]\n"
"ds_read_b64_tr_b16 a[214:215], v235 offset:21512\n"
"v_cvt_pk_bf16_f32 v168, v224, v225\n"
"v_fma_mix_f32 v226, v169, v194, v202 op_sel:[0,0,0] op_sel_hi:[1,0,0]\n"
"v_mfma_f32_16x16x32_bf16 v[48:51], a[240:243], v[148:151], v[48:51]\n"
"s_add_u32 m0, s51, 0xc000\n"
"buffer_load_dwordx4 v[136:139], v249, s[64:67], s56 offen offset:0\n"
"v_fma_mix_f32 v227, v169, v195, v203 op_sel:[1,0,0] op_sel_hi:[1,0,0]\n"
"v_mfma_f32_16x16x32_bf16 v[80:83], a[240:243], v[152:155], v[80:83]\n"
"buffer_load_dwordx4 v242, s[28:31], s49 offen offset:0 lds\n"
"ds_read_b64_tr_b16 a[216:217], v235 offset:17920\n"
"v_cvt_pk_bf16_f32 v169, v226, v227\n"
"v_fma_mix_f32 v224, v170, v196, v204 op_sel:[0,0,0] op_sel_hi:[1,0,0]\n"
"v_mfma_f32_16x16x32_bf16 v[112:115], a[240:243], v[156:159], v[112:115]\n"
"ds_read_b64_tr_b16 a[218:219], v235 offset:22016\n"
"v_fma_mix_f32 v225, v170, v197, v205 op_sel:[1,0,0] op_sel_hi:[1,0,0]\n"
"v_mfma_f32_16x16x32_bf16 v[20:23], a[244:247], v[144:147], v[20:23]\n"
"v_cvt_pk_bf16_f32 v170, v224, v225\n"
"v_fma_mix_f32 v226, v171, v198, v206 op_sel:[0,0,0] op_sel_hi:[1,0,0]\n"
"v_mfma_f32_16x16x32_bf16 v[52:55], a[244:247], v[148:151], v[52:55]\n"
"buffer_load_dwordx4 v243, s[28:31], s49 offen offset:1024 lds\n"
"ds_read_b64_tr_b16 a[220:221], v235 offset:17928\n"
"v_fma_mix_f32 v227, v171, v199, v207 op_sel:[1,0,0] op_sel_hi:[1,0,0]\n"
"v_cvt_pk_bf16_f32 v171, v226, v227\n"
"v_mfma_f32_16x16x32_bf16 v[84:87], a[244:247], v[152:155], v[84:87]\n"
"ds_read_b64_tr_b16 a[222:223], v235 offset:22024\n"
"v_fma_mix_f32 v224, v172, v192, v200 op_sel:[0,0,0] op_sel_hi:[1,0,0]\n"
"v_mfma_f32_16x16x32_bf16 v[116:119], a[244:247], v[156:159], v[116:119]\n"
"buffer_load_dwordx4 v[140:143], v250, s[64:67], s56 offen offset:0\n"
"v_fma_mix_f32 v225, v172, v193, v201 op_sel:[1,0,0] op_sel_hi:[1,0,0]\n"
"v_cvt_pk_bf16_f32 v172, v224, v225\n"
"v_mfma_f32_16x16x32_bf16 v[24:27], a[248:251], v[144:147], v[24:27]\n"
"buffer_load_dwordx4 v244, s[28:31], s49 offen offset:2048 lds\n"
"v_fma_mix_f32 v226, v173, v194, v202 op_sel:[0,0,0] op_sel_hi:[1,0,0]\n"
"v_mfma_f32_16x16x32_bf16 v[56:59], a[248:251], v[148:151], v[56:59]\n"
"v_fma_mix_f32 v227, v173, v195, v203 op_sel:[1,0,0] op_sel_hi:[1,0,0]\n"
"v_cvt_pk_bf16_f32 v173, v226, v227\n"
"v_mfma_f32_16x16x32_bf16 v[88:91], a[248:251], v[152:155], v[88:91]\n"
"v_fma_mix_f32 v224, v174, v196, v204 op_sel:[0,0,0] op_sel_hi:[1,0,0]\n"
"v_fma_mix_f32 v225, v174, v197, v205 op_sel:[1,0,0] op_sel_hi:[1,0,0]\n"
"v_mfma_f32_16x16x32_bf16 v[120:123], a[248:251], v[156:159], v[120:123]\n"
"buffer_load_dwordx4 v245, s[28:31], s49 offen offset:3072 lds\n"
"v_cvt_pk_bf16_f32 v174, v224, v225\n"
"v_mfma_f32_16x16x32_bf16 v[28:31], a[252:255], v[144:147], v[28:31]\n"
"s_add_u32 s49, s49, 0x80000\n"
"v_fma_mix_f32 v226, v175, v198, v206 op_sel:[0,0,0] op_sel_hi:[1,0,0]\n"
"v_fma_mix_f32 v227, v175, v199, v207 op_sel:[1,0,0] op_sel_hi:[1,0,0]\n"
"v_mfma_f32_16x16x32_bf16 v[60:63], a[252:255], v[148:151], v[60:63]\n"
"v_cvt_pk_bf16_f32 v175, v226, v227\n"
"v_mfma_f32_16x16x32_bf16 v[92:95], a[252:255], v[152:155], v[92:95]\n"
"v_mfma_f32_16x16x32_bf16 v[124:127], a[252:255], v[156:159], v[124:127]\n"
"s_waitcnt lgkmcnt(0)\n"
"s_waitcnt vmcnt(10)\n"
"v_mfma_f32_16x16x32_bf16 v[0:3], a[192:195], v[160:163], v[0:3]\n"
"ds_read_b64_tr_b16 a[224:225], v235 offset:24576\n"
"v_fma_mix_f32 v224, v176, v208, v216 op_sel:[0,0,0] op_sel_hi:[1,0,0]\n"
"v_fma_mix_f32 v225, v176, v209, v217 op_sel:[1,0,0] op_sel_hi:[1,0,0]\n"
"v_mfma_f32_16x16x32_bf16 v[32:35], a[192:195], v[164:167], v[32:35]\n"
"ds_read_b64_tr_b16 a[226:227], v235 offset:28672\n"
"v_cvt_pk_bf16_f32 v176, v224, v225\n"
"v_fma_mix_f32 v226, v177, v210, v218 op_sel:[0,0,0] op_sel_hi:[1,0,0]\n"
"v_mfma_f32_16x16x32_bf16 v[64:67], a[192:195], v[168:171], v[64:67]\n"
"ds_read_b128 v[192:195], v247 offset:3072\n"
"v_fma_mix_f32 v227, v177, v211, v219 op_sel:[1,0,0] op_sel_hi:[1,0,0]\n"
"v_mfma_f32_16x16x32_bf16 v[96:99], a[192:195], v[172:175], v[96:99]\n"
"ds_read_b64_tr_b16 a[228:229], v235 offset:24584\n"
"v_cvt_pk_bf16_f32 v177, v226, v227\n"
"v_fma_mix_f32 v224, v178, v212, v220 op_sel:[0,0,0] op_sel_hi:[1,0,0]\n"
"v_mfma_f32_16x16x32_bf16 v[4:7], a[196:199], v[160:163], v[4:7]\n"
"ds_read_b64_tr_b16 a[230:231], v235 offset:28680\n"
"v_fma_mix_f32 v225, v178, v213, v221 op_sel:[1,0,0] op_sel_hi:[1,0,0]\n"
"v_mfma_f32_16x16x32_bf16 v[36:39], a[196:199], v[164:167], v[36:39]\n"
"ds_read_b128 v[196:199], v247 offset:3088\n"
"buffer_load_dwordx4 v[144:147], v246, s[64:67], s56 offen offset:1024\n"
"v_cvt_pk_bf16_f32 v178, v224, v225\n"
"v_fma_mix_f32 v226, v179, v214, v222 op_sel:[0,0,0] op_sel_hi:[1,0,0]\n"
"v_mfma_f32_16x16x32_bf16 v[68:71], a[196:199], v[168:171], v[68:71]\n"
"ds_read_b64_tr_b16 a[232:233], v235 offset:25088\n"
"v_fma_mix_f32 v227, v179, v215, v223 op_sel:[1,0,0] op_sel_hi:[1,0,0]\n"
"v_cvt_pk_bf16_f32 v179, v226, v227\n"
"v_mfma_f32_16x16x32_bf16 v[100:103], a[196:199], v[172:175], v[100:103]\n"
"ds_read_b64_tr_b16 a[234:235], v235 offset:29184\n"
"v_fma_mix_f32 v224, v180, v208, v216 op_sel:[0,0,0] op_sel_hi:[1,0,0]\n"
"v_mfma_f32_16x16x32_bf16 v[8:11], a[200:203], v[160:163], v[8:11]\n"
"ds_read_b128 v[200:203], v247 offset:3104\n"
"v_fma_mix_f32 v225, v180, v209, v217 op_sel:[1,0,0] op_sel_hi:[1,0,0]\n"
"v_cvt_pk_bf16_f32 v180, v224, v225\n"
"v_mfma_f32_16x16x32_bf16 v[40:43], a[200:203], v[164:167], v[40:43]\n"
"ds_read_b64_tr_b16 a[236:237], v235 offset:25096\n"
"v_fma_mix_f32 v226, v181, v210, v218 op_sel:[0,0,0] op_sel_hi:[1,0,0]\n"
"v_mfma_f32_16x16x32_bf16 v[72:75], a[200:203], v[168:171], v[72:75]\n"
"ds_read_b64_tr_b16 a[238:239], v235 offset:29192\n"
"v_fma_mix_f32 v227, v181, v211, v219 op_sel:[1,0,0] op_sel_hi:[1,0,0]\n"
"v_cvt_pk_bf16_f32 v181, v226, v227\n"
"v_mfma_f32_16x16x32_bf16 v[104:107], a[200:203], v[172:175], v[104:107]\n"
"ds_read_b128 v[204:207], v247 offset:3120\n"
"buffer_load_dwordx4 v[148:151], v248, s[64:67], s56 offen offset:1024\n"
"v_fma_mix_f32 v224, v182, v212, v220 op_sel:[0,0,0] op_sel_hi:[1,0,0]\n"
"v_fma_mix_f32 v225, v182, v213, v221 op_sel:[1,0,0] op_sel_hi:[1,0,0]\n"
"v_mfma_f32_16x16x32_bf16 v[12:15], a[204:207], v[160:163], v[12:15]\n"
"ds_read_b64_tr_b16 a[240:241], v235 offset:25600\n"
"v_cvt_pk_bf16_f32 v182, v224, v225\n"
"v_mfma_f32_16x16x32_bf16 v[44:47], a[204:207], v[164:167], v[44:47]\n"
"ds_read_b64_tr_b16 a[242:243], v235 offset:29696\n"
"v_fma_mix_f32 v226, v183, v214, v222 op_sel:[0,0,0] op_sel_hi:[1,0,0]\n"
"v_fma_mix_f32 v227, v183, v215, v223 op_sel:[1,0,0] op_sel_hi:[1,0,0]\n"
"v_mfma_f32_16x16x32_bf16 v[76:79], a[204:207], v[168:171], v[76:79]\n"
"v_cvt_pk_bf16_f32 v183, v226, v227\n"
"v_mfma_f32_16x16x32_bf16 v[108:111], a[204:207], v[172:175], v[108:111]\n"
"ds_read_b64_tr_b16 a[244:245], v235 offset:25608\n"
"v_fma_mix_f32 v224, v184, v208, v216 op_sel:[0,0,0] op_sel_hi:[1,0,0]\n"
"v_fma_mix_f32 v225, v184, v209, v217 op_sel:[1,0,0] op_sel_hi:[1,0,0]\n"
"v_mfma_f32_16x16x32_bf16 v[16:19], a[208:211], v[160:163], v[16:19]\n"
"ds_read_b64_tr_b16 a[246:247], v235 offset:29704\n"
"v_cvt_pk_bf16_f32 v184, v224, v225\n"
"v_fma_mix_f32 v226, v185, v210, v218 op_sel:[0,0,0] op_sel_hi:[1,0,0]\n"
"v_mfma_f32_16x16x32_bf16 v[48:51], a[208:211], v[164:167], v[48:51]\n"
"buffer_load_dwordx4 v[152:155], v249, s[64:67], s56 offen offset:1024\n"
"v_fma_mix_f32 v227, v185, v211, v219 op_sel:[1,0,0] op_sel_hi:[1,0,0]\n"
"v_mfma_f32_16x16x32_bf16 v[80:83], a[208:211], v[168:171], v[80:83]\n"
"ds_read_b64_tr_b16 a[248:249], v235 offset:26112\n"
"v_cvt_pk_bf16_f32 v185, v226, v227\n"
"v_fma_mix_f32 v224, v186, v212, v220 op_sel:[0,0,0] op_sel_hi:[1,0,0]\n"
"v_mfma_f32_16x16x32_bf16 v[112:115], a[208:211], v[172:175], v[112:115]\n"
"ds_read_b64_tr_b16 a[250:251], v235 offset:30208\n"
"v_fma_mix_f32 v225, v186, v213, v221 op_sel:[1,0,0] op_sel_hi:[1,0,0]\n"
"v_mfma_f32_16x16x32_bf16 v[20:23], a[212:215], v[160:163], v[20:23]\n"
"v_cvt_pk_bf16_f32 v186, v224, v225\n"
"v_fma_mix_f32 v226, v187, v214, v222 op_sel:[0,0,0] op_sel_hi:[1,0,0]\n"
"v_mfma_f32_16x16x32_bf16 v[52:55], a[212:215], v[164:167], v[52:55]\n"
"ds_read_b64_tr_b16 a[252:253], v235 offset:26120\n"
"v_fma_mix_f32 v227, v187, v215, v223 op_sel:[1,0,0] op_sel_hi:[1,0,0]\n"
"v_cvt_pk_bf16_f32 v187, v226, v227\n"
"v_mfma_f32_16x16x32_bf16 v[84:87], a[212:215], v[168:171], v[84:87]\n"
"ds_read_b64_tr_b16 a[254:255], v235 offset:30216\n"
"v_fma_mix_f32 v224, v188, v208, v216 op_sel:[0,0,0] op_sel_hi:[1,0,0]\n"
"v_mfma_f32_16x16x32_bf16 v[116:119], a[212:215], v[172:175], v[116:119]\n"
"buffer_load_dwordx4 v[156:159], v250, s[64:67], s56 offen offset:1024\n"
"v_fma_mix_f32 v225, v188, v209, v217 op_sel:[1,0,0] op_sel_hi:[1,0,0]\n"
"v_cvt_pk_bf16_f32 v188, v224, v225\n"
"v_mfma_f32_16x16x32_bf16 v[24:27], a[216:219], v[160:163], v[24:27]\n"
"v_fma_mix_f32 v226, v189, v210, v218 op_sel:[0,0,0] op_sel_hi:[1,0,0]\n"
"v_mfma_f32_16x16x32_bf16 v[56:59], a[216:219], v[164:167], v[56:59]\n"
"v_fma_mix_f32 v227, v189, v211, v219 op_sel:[1,0,0] op_sel_hi:[1,0,0]\n"
"v_cvt_pk_bf16_f32 v189, v226, v227\n"
"v_mfma_f32_16x16x32_bf16 v[88:91], a[216:219], v[168:171], v[88:91]\n"
"v_fma_mix_f32 v224, v190, v212, v220 op_sel:[0,0,0] op_sel_hi:[1,0,0]\n"
"v_fma_mix_f32 v225, v190, v213, v221 op_sel:[1,0,0] op_sel_hi:[1,0,0]\n"
"v_mfma_f32_16x16x32_bf16 v[120:123], a[216:219], v[172:175], v[120:123]\n"
"v_cvt_pk_bf16_f32 v190, v224, v225\n"
"v_mfma_f32_16x16x32_bf16 v[28:31], a[220:223], v[160:163], v[28:31]\n"
"v_fma_mix_f32 v226, v191, v214, v222 op_sel:[0,0,0] op_sel_hi:[1,0,0]\n"
"v_fma_mix_f32 v227, v191, v215, v223 op_sel:[1,0,0] op_sel_hi:[1,0,0]\n"
"v_mfma_f32_16x16x32_bf16 v[60:63], a[220:223], v[164:167], v[60:63]\n"
"v_cvt_pk_bf16_f32 v191, v226, v227\n"
"v_mfma_f32_16x16x32_bf16 v[92:95], a[220:223], v[168:171], v[92:95]\n"
"v_mfma_f32_16x16x32_bf16 v[124:127], a[220:223], v[172:175], v[124:127]\n"
"s_waitcnt lgkmcnt(0)\n"
"s_waitcnt vmcnt(6)\n"
"v_mfma_f32_16x16x32_bf16 v[0:3], a[224:227], v[176:179], v[0:3]\n"
"s_barrier\n"
"ds_read_b64_tr_b16 a[192:193], v235 offset:32768\n"
"v_fma_mix_f32 v224, v128, v192, v200 op_sel:[0,0,0] op_sel_hi:[1,0,0]\n"
"v_fma_mix_f32 v225, v128, v193, v201 op_sel:[1,0,0] op_sel_hi:[1,0,0]\n"
"v_mfma_f32_16x16x32_bf16 v[32:35], a[224:227], v[180:183], v[32:35]\n"
"ds_read_b64_tr_b16 a[194:195], v235 offset:36864\n"
"v_cvt_pk_bf16_f32 v128, v224, v225\n"
"v_fma_mix_f32 v226, v129, v194, v202 op_sel:[0,0,0] op_sel_hi:[1,0,0]\n"
"v_mfma_f32_16x16x32_bf16 v[64:67], a[224:227], v[184:187], v[64:67]\n"
"ds_read_b128 v[208:211], v247 offset:3328\n"
"v_fma_mix_f32 v227, v129, v195, v203 op_sel:[1,0,0] op_sel_hi:[1,0,0]\n"
"v_mfma_f32_16x16x32_bf16 v[96:99], a[224:227], v[188:191], v[96:99]\n"
"ds_read_b64_tr_b16 a[196:197], v235 offset:32776\n"
"v_cvt_pk_bf16_f32 v129, v226, v227\n"
"v_fma_mix_f32 v224, v130, v196, v204 op_sel:[0,0,0] op_sel_hi:[1,0,0]\n"
"v_mfma_f32_16x16x32_bf16 v[4:7], a[228:231], v[176:179], v[4:7]\n"
"ds_read_b64_tr_b16 a[198:199], v235 offset:36872\n"
"v_fma_mix_f32 v225, v130, v197, v205 op_sel:[1,0,0] op_sel_hi:[1,0,0]\n"
"v_mfma_f32_16x16x32_bf16 v[36:39], a[228:231], v[180:183], v[36:39]\n"
"ds_read_b128 v[212:215], v247 offset:3344\n"
"buffer_load_dwordx4 v[160:163], v246, s[64:67], s56 offen offset:2048\n"
"v_cvt_pk_bf16_f32 v130, v224, v225\n"
"v_fma_mix_f32 v226, v131, v198, v206 op_sel:[0,0,0] op_sel_hi:[1,0,0]\n"
"v_mfma_f32_16x16x32_bf16 v[68:71], a[228:231], v[184:187], v[68:71]\n"
"ds_read_b64_tr_b16 a[200:201], v235 offset:33280\n"
"v_fma_mix_f32 v227, v131, v199, v207 op_sel:[1,0,0] op_sel_hi:[1,0,0]\n"
"v_cvt_pk_bf16_f32 v131, v226, v227\n"
"v_mfma_f32_16x16x32_bf16 v[100:103], a[228:231], v[188:191], v[100:103]\n"
"ds_read_b64_tr_b16 a[202:203], v235 offset:37376\n"
"v_fma_mix_f32 v224, v132, v192, v200 op_sel:[0,0,0] op_sel_hi:[1,0,0]\n"
"v_mfma_f32_16x16x32_bf16 v[8:11], a[232:235], v[176:179], v[8:11]\n"
"ds_read_b128 v[216:219], v247 offset:3360\n"
"v_fma_mix_f32 v225, v132, v193, v201 op_sel:[1,0,0] op_sel_hi:[1,0,0]\n"
"v_cvt_pk_bf16_f32 v132, v224, v225\n"
"v_mfma_f32_16x16x32_bf16 v[40:43], a[232:235], v[180:183], v[40:43]\n"
"ds_read_b64_tr_b16 a[204:205], v235 offset:33288\n"
"v_fma_mix_f32 v226, v133, v194, v202 op_sel:[0,0,0] op_sel_hi:[1,0,0]\n"
"v_mfma_f32_16x16x32_bf16 v[72:75], a[232:235], v[184:187], v[72:75]\n"
"ds_read_b64_tr_b16 a[206:207], v235 offset:37384\n"
"v_fma_mix_f32 v227, v133, v195, v203 op_sel:[1,0,0] op_sel_hi:[1,0,0]\n"
"v_cvt_pk_bf16_f32 v133, v226, v227\n"
"v_mfma_f32_16x16x32_bf16 v[104:107], a[232:235], v[188:191], v[104:107]\n"
"ds_read_b128 v[220:223], v247 offset:3376\n"
"buffer_load_dwordx4 v[164:167], v248, s[64:67], s56 offen offset:2048\n"
"v_fma_mix_f32 v224, v134, v196, v204 op_sel:[0,0,0] op_sel_hi:[1,0,0]\n"
"v_fma_mix_f32 v225, v134, v197, v205 op_sel:[1,0,0] op_sel_hi:[1,0,0]\n"
"v_mfma_f32_16x16x32_bf16 v[12:15], a[236:239], v[176:179], v[12:15]\n"
"ds_read_b64_tr_b16 a[208:209], v235 offset:33792\n"
"v_cvt_pk_bf16_f32 v134, v224, v225\n"
"v_mfma_f32_16x16x32_bf16 v[44:47], a[236:239], v[180:183], v[44:47]\n"
"ds_read_b64_tr_b16 a[210:211], v235 offset:37888\n"
"v_fma_mix_f32 v226, v135, v198, v206 op_sel:[0,0,0] op_sel_hi:[1,0,0]\n"
"v_fma_mix_f32 v227, v135, v199, v207 op_sel:[1,0,0] op_sel_hi:[1,0,0]\n"
"v_mfma_f32_16x16x32_bf16 v[76:79], a[236:239], v[184:187], v[76:79]\n"
"v_cvt_pk_bf16_f32 v135, v226, v227\n"
"v_mfma_f32_16x16x32_bf16 v[108:111], a[236:239], v[188:191], v[108:111]\n"
"ds_read_b64_tr_b16 a[212:213], v235 offset:33800\n"
"v_fma_mix_f32 v224, v136, v192, v200 op_sel:[0,0,0] op_sel_hi:[1,0,0]\n"
"v_fma_mix_f32 v225, v136, v193, v201 op_sel:[1,0,0] op_sel_hi:[1,0,0]\n"
"v_mfma_f32_16x16x32_bf16 v[16:19], a[240:243], v[176:179], v[16:19]\n"
"ds_read_b64_tr_b16 a[214:215], v235 offset:37896\n"
"v_cvt_pk_bf16_f32 v136, v224, v225\n"
"v_fma_mix_f32 v226, v137, v194, v202 op_sel:[0,0,0] op_sel_hi:[1,0,0]\n"
"v_mfma_f32_16x16x32_bf16 v[48:51], a[240:243], v[180:183], v[48:51]\n"
"buffer_load_dwordx4 v[168:171], v249, s[64:67], s56 offen offset:2048\n"
"v_fma_mix_f32 v227, v137, v195, v203 op_sel:[1,0,0] op_sel_hi:[1,0,0]\n"
"v_mfma_f32_16x16x32_bf16 v[80:83], a[240:243], v[184:187], v[80:83]\n"
"ds_read_b64_tr_b16 a[216:217], v235 offset:34304\n"
"v_cvt_pk_bf16_f32 v137, v226, v227\n"
"v_fma_mix_f32 v224, v138, v196, v204 op_sel:[0,0,0] op_sel_hi:[1,0,0]\n"
"v_mfma_f32_16x16x32_bf16 v[112:115], a[240:243], v[188:191], v[112:115]\n"
"ds_read_b64_tr_b16 a[218:219], v235 offset:38400\n"
"v_fma_mix_f32 v225, v138, v197, v205 op_sel:[1,0,0] op_sel_hi:[1,0,0]\n"
"v_mfma_f32_16x16x32_bf16 v[20:23], a[244:247], v[176:179], v[20:23]\n"
"v_cvt_pk_bf16_f32 v138, v224, v225\n"
"v_fma_mix_f32 v226, v139, v198, v206 op_sel:[0,0,0] op_sel_hi:[1,0,0]\n"
"v_mfma_f32_16x16x32_bf16 v[52:55], a[244:247], v[180:183], v[52:55]\n"
"ds_read_b64_tr_b16 a[220:221], v235 offset:34312\n"
"v_fma_mix_f32 v227, v139, v199, v207 op_sel:[1,0,0] op_sel_hi:[1,0,0]\n"
"v_cvt_pk_bf16_f32 v139, v226, v227\n"
"v_mfma_f32_16x16x32_bf16 v[84:87], a[244:247], v[184:187], v[84:87]\n"
"ds_read_b64_tr_b16 a[222:223], v235 offset:38408\n"
"v_fma_mix_f32 v224, v140, v192, v200 op_sel:[0,0,0] op_sel_hi:[1,0,0]\n"
"v_mfma_f32_16x16x32_bf16 v[116:119], a[244:247], v[188:191], v[116:119]\n"
"buffer_load_dwordx4 v[172:175], v250, s[64:67], s56 offen offset:2048\n"
"v_fma_mix_f32 v225, v140, v193, v201 op_sel:[1,0,0] op_sel_hi:[1,0,0]\n"
"v_cvt_pk_bf16_f32 v140, v224, v225\n"
"v_mfma_f32_16x16x32_bf16 v[24:27], a[248:251], v[176:179], v[24:27]\n"
"v_fma_mix_f32 v226, v141, v194, v202 op_sel:[0,0,0] op_sel_hi:[1,0,0]\n"
"v_mfma_f32_16x16x32_bf16 v[56:59], a[248:251], v[180:183], v[56:59]\n"
"v_fma_mix_f32 v227, v141, v195, v203 op_sel:[1,0,0] op_sel_hi:[1,0,0]\n"
"v_cvt_pk_bf16_f32 v141, v226, v227\n"
"v_mfma_f32_16x16x32_bf16 v[88:91], a[248:251], v[184:187], v[88:91]\n"
"v_fma_mix_f32 v224, v142, v196, v204 op_sel:[0,0,0] op_sel_hi:[1,0,0]\n"
"v_fma_mix_f32 v225, v142, v197, v205 op_sel:[1,0,0] op_sel_hi:[1,0,0]\n"
"v_mfma_f32_16x16x32_bf16 v[120:123], a[248:251], v[188:191], v[120:123]\n"
"v_cvt_pk_bf16_f32 v142, v224, v225\n"
"v_mfma_f32_16x16x32_bf16 v[28:31], a[252:255], v[176:179], v[28:31]\n"
"v_fma_mix_f32 v226, v143, v198, v206 op_sel:[0,0,0] op_sel_hi:[1,0,0]\n"
"v_fma_mix_f32 v227, v143, v199, v207 op_sel:[1,0,0] op_sel_hi:[1,0,0]\n"
"v_mfma_f32_16x16x32_bf16 v[60:63], a[252:255], v[180:183], v[60:63]\n"
"v_cvt_pk_bf16_f32 v143, v226, v227\n"
"v_mfma_f32_16x16x32_bf16 v[92:95], a[252:255], v[184:187], v[92:95]\n"
"v_mfma_f32_16x16x32_bf16 v[124:127], a[252:255], v[188:191], v[124:127]\n"
"s_waitcnt lgkmcnt(0)\n"
"s_waitcnt vmcnt(4)\n"
"v_mfma_f32_16x16x32_bf16 v[0:3], a[192:195], v[128:131], v[0:3]\n"
"ds_read_b64_tr_b16 a[224:225], v235 offset:40960\n"
"v_fma_mix_f32 v224, v144, v208, v216 op_sel:[0,0,0] op_sel_hi:[1,0,0]\n"
"v_fma_mix_f32 v225, v144, v209, v217 op_sel:[1,0,0] op_sel_hi:[1,0,0]\n"
"v_mfma_f32_16x16x32_bf16 v[32:35], a[192:195], v[132:135], v[32:35]\n"
"ds_read_b64_tr_b16 a[226:227], v235 offset:45056\n"
"v_cvt_pk_bf16_f32 v144, v224, v225\n"
"v_fma_mix_f32 v226, v145, v210, v218 op_sel:[0,0,0] op_sel_hi:[1,0,0]\n"
"v_mfma_f32_16x16x32_bf16 v[64:67], a[192:195], v[136:139], v[64:67]\n"
"ds_read_b128 v[192:195], v247 offset:3584\n"
"v_fma_mix_f32 v227, v145, v211, v219 op_sel:[1,0,0] op_sel_hi:[1,0,0]\n"
"v_mfma_f32_16x16x32_bf16 v[96:99], a[192:195], v[140:143], v[96:99]\n"
"ds_read_b64_tr_b16 a[228:229], v235 offset:40968\n"
"v_cvt_pk_bf16_f32 v145, v226, v227\n"
"v_fma_mix_f32 v224, v146, v212, v220 op_sel:[0,0,0] op_sel_hi:[1,0,0]\n"
"v_mfma_f32_16x16x32_bf16 v[4:7], a[196:199], v[128:131], v[4:7]\n"
"ds_read_b64_tr_b16 a[230:231], v235 offset:45064\n"
"v_fma_mix_f32 v225, v146, v213, v221 op_sel:[1,0,0] op_sel_hi:[1,0,0]\n"
"v_mfma_f32_16x16x32_bf16 v[36:39], a[196:199], v[132:135], v[36:39]\n"
"ds_read_b128 v[196:199], v247 offset:3600\n"
"buffer_load_dwordx4 v[176:179], v246, s[64:67], s56 offen offset:3072\n"
"v_cvt_pk_bf16_f32 v146, v224, v225\n"
"v_fma_mix_f32 v226, v147, v214, v222 op_sel:[0,0,0] op_sel_hi:[1,0,0]\n"
"v_mfma_f32_16x16x32_bf16 v[68:71], a[196:199], v[136:139], v[68:71]\n"
"ds_read_b64_tr_b16 a[232:233], v235 offset:41472\n"
"v_fma_mix_f32 v227, v147, v215, v223 op_sel:[1,0,0] op_sel_hi:[1,0,0]\n"
"v_cvt_pk_bf16_f32 v147, v226, v227\n"
"v_mfma_f32_16x16x32_bf16 v[100:103], a[196:199], v[140:143], v[100:103]\n"
"ds_read_b64_tr_b16 a[234:235], v235 offset:45568\n"
"v_fma_mix_f32 v224, v148, v208, v216 op_sel:[0,0,0] op_sel_hi:[1,0,0]\n"
"v_mfma_f32_16x16x32_bf16 v[8:11], a[200:203], v[128:131], v[8:11]\n"
"ds_read_b128 v[200:203], v247 offset:3616\n"
"v_fma_mix_f32 v225, v148, v209, v217 op_sel:[1,0,0] op_sel_hi:[1,0,0]\n"
"v_cvt_pk_bf16_f32 v148, v224, v225\n"
"v_mfma_f32_16x16x32_bf16 v[40:43], a[200:203], v[132:135], v[40:43]\n"
"ds_read_b64_tr_b16 a[236:237], v235 offset:41480\n"
"v_fma_mix_f32 v226, v149, v210, v218 op_sel:[0,0,0] op_sel_hi:[1,0,0]\n"
"v_mfma_f32_16x16x32_bf16 v[72:75], a[200:203], v[136:139], v[72:75]\n"
"ds_read_b64_tr_b16 a[238:239], v235 offset:45576\n"
"v_fma_mix_f32 v227, v149, v211, v219 op_sel:[1,0,0] op_sel_hi:[1,0,0]\n"
"v_cvt_pk_bf16_f32 v149, v226, v227\n"
"v_mfma_f32_16x16x32_bf16 v[104:107], a[200:203], v[140:143], v[104:107]\n"
"ds_read_b128 v[204:207], v247 offset:3632\n"
"buffer_load_dwordx4 v[180:183], v248, s[64:67], s56 offen offset:3072\n"
"v_fma_mix_f32 v224, v150, v212, v220 op_sel:[0,0,0] op_sel_hi:[1,0,0]\n"
"v_fma_mix_f32 v225, v150, v213, v221 op_sel:[1,0,0] op_sel_hi:[1,0,0]\n"
"v_mfma_f32_16x16x32_bf16 v[12:15], a[204:207], v[128:131], v[12:15]\n"
"ds_read_b64_tr_b16 a[240:241], v235 offset:41984\n"
"v_cvt_pk_bf16_f32 v150, v224, v225\n"
"v_mfma_f32_16x16x32_bf16 v[44:47], a[204:207], v[132:135], v[44:47]\n"
"ds_read_b64_tr_b16 a[242:243], v235 offset:46080\n"
"v_fma_mix_f32 v226, v151, v214, v222 op_sel:[0,0,0] op_sel_hi:[1,0,0]\n"
"v_fma_mix_f32 v227, v151, v215, v223 op_sel:[1,0,0] op_sel_hi:[1,0,0]\n"
"v_mfma_f32_16x16x32_bf16 v[76:79], a[204:207], v[136:139], v[76:79]\n"
"v_cvt_pk_bf16_f32 v151, v226, v227\n"
"v_mfma_f32_16x16x32_bf16 v[108:111], a[204:207], v[140:143], v[108:111]\n"
"ds_read_b64_tr_b16 a[244:245], v235 offset:41992\n"
"v_fma_mix_f32 v224, v152, v208, v216 op_sel:[0,0,0] op_sel_hi:[1,0,0]\n"
"v_fma_mix_f32 v225, v152, v209, v217 op_sel:[1,0,0] op_sel_hi:[1,0,0]\n"
"v_mfma_f32_16x16x32_bf16 v[16:19], a[208:211], v[128:131], v[16:19]\n"
"ds_read_b64_tr_b16 a[246:247], v235 offset:46088\n"
"v_cvt_pk_bf16_f32 v152, v224, v225\n"
"v_fma_mix_f32 v226, v153, v210, v218 op_sel:[0,0,0] op_sel_hi:[1,0,0]\n"
"v_mfma_f32_16x16x32_bf16 v[48:51], a[208:211], v[132:135], v[48:51]\n"
"buffer_load_dwordx4 v[184:187], v249, s[64:67], s56 offen offset:3072\n"
"v_fma_mix_f32 v227, v153, v211, v219 op_sel:[1,0,0] op_sel_hi:[1,0,0]\n"
"v_mfma_f32_16x16x32_bf16 v[80:83], a[208:211], v[136:139], v[80:83]\n"
"ds_read_b64_tr_b16 a[248:249], v235 offset:42496\n"
"v_cvt_pk_bf16_f32 v153, v226, v227\n"
"v_fma_mix_f32 v224, v154, v212, v220 op_sel:[0,0,0] op_sel_hi:[1,0,0]\n"
"v_mfma_f32_16x16x32_bf16 v[112:115], a[208:211], v[140:143], v[112:115]\n"
"ds_read_b64_tr_b16 a[250:251], v235 offset:46592\n"
"v_fma_mix_f32 v225, v154, v213, v221 op_sel:[1,0,0] op_sel_hi:[1,0,0]\n"
"v_mfma_f32_16x16x32_bf16 v[20:23], a[212:215], v[128:131], v[20:23]\n"
"v_cvt_pk_bf16_f32 v154, v224, v225\n"
"v_fma_mix_f32 v226, v155, v214, v222 op_sel:[0,0,0] op_sel_hi:[1,0,0]\n"
"v_mfma_f32_16x16x32_bf16 v[52:55], a[212:215], v[132:135], v[52:55]\n"
"ds_read_b64_tr_b16 a[252:253], v235 offset:42504\n"
"v_fma_mix_f32 v227, v155, v215, v223 op_sel:[1,0,0] op_sel_hi:[1,0,0]\n"
"v_cvt_pk_bf16_f32 v155, v226, v227\n"
"v_mfma_f32_16x16x32_bf16 v[84:87], a[212:215], v[136:139], v[84:87]\n"
"ds_read_b64_tr_b16 a[254:255], v235 offset:46600\n"
"v_fma_mix_f32 v224, v156, v208, v216 op_sel:[0,0,0] op_sel_hi:[1,0,0]\n"
"v_mfma_f32_16x16x32_bf16 v[116:119], a[212:215], v[140:143], v[116:119]\n"
"buffer_load_dwordx4 v[188:191], v250, s[64:67], s56 offen offset:3072\n"
"v_fma_mix_f32 v225, v156, v209, v217 op_sel:[1,0,0] op_sel_hi:[1,0,0]\n"
"v_cvt_pk_bf16_f32 v156, v224, v225\n"
"v_mfma_f32_16x16x32_bf16 v[24:27], a[216:219], v[128:131], v[24:27]\n"
"s_add_u32 m0, s50, 0x4000\n"
"v_fma_mix_f32 v226, v157, v210, v218 op_sel:[0,0,0] op_sel_hi:[1,0,0]\n"
"v_mfma_f32_16x16x32_bf16 v[56:59], a[216:219], v[132:135], v[56:59]\n"
"buffer_load_dwordx4 v236, s[20:23], s47 offen offset:0 lds\n"
"v_fma_mix_f32 v227, v157, v211, v219 op_sel:[1,0,0] op_sel_hi:[1,0,0]\n"
"v_cvt_pk_bf16_f32 v157, v226, v227\n"
"v_mfma_f32_16x16x32_bf16 v[88:91], a[216:219], v[136:139], v[88:91]\n"
"buffer_load_dwordx4 v237, s[20:23], s47 offen offset:1024 lds\n"
"v_fma_mix_f32 v224, v158, v212, v220 op_sel:[0,0,0] op_sel_hi:[1,0,0]\n"
"v_fma_mix_f32 v225, v158, v213, v221 op_sel:[1,0,0] op_sel_hi:[1,0,0]\n"
"v_mfma_f32_16x16x32_bf16 v[120:123], a[216:219], v[140:143], v[120:123]\n"
"buffer_load_dwordx4 v238, s[20:23], s47 offen offset:2048 lds\n"
"v_cvt_pk_bf16_f32 v158, v224, v225\n"
"v_mfma_f32_16x16x32_bf16 v[28:31], a[220:223], v[128:131], v[28:31]\n"
"buffer_load_dwordx4 v239, s[20:23], s47 offen offset:3072 lds\n"
"v_fma_mix_f32 v226, v159, v214, v222 op_sel:[0,0,0] op_sel_hi:[1,0,0]\n"
"v_fma_mix_f32 v227, v159, v215, v223 op_sel:[1,0,0] op_sel_hi:[1,0,0]\n"
"v_mfma_f32_16x16x32_bf16 v[60:63], a[220:223], v[132:135], v[60:63]\n"
"s_add_u32 s47, s47, s55\n"
"s_add_u32 m0, s51, 0\n"
"v_cvt_pk_bf16_f32 v159, v226, v227\n"
"v_mfma_f32_16x16x32_bf16 v[92:95], a[220:223], v[136:139], v[92:95]\n"
"buffer_load_dwordx4 v240, s[24:27], s48 offen offset:0 lds\n"
"buffer_load_dwordx4 v241, s[24:27], s48 offen offset:1024 lds\n"
"s_add_u32 s56, s56, 0x1000\n"
"v_mfma_f32_16x16x32_bf16 v[124:127], a[220:223], v[140:143], v[124:127]\n"
"buffer_load_dwordx4 v240, s[24:27], s48 offen offset:2048 lds\n"
"buffer_load_dwordx4 v241, s[24:27], s48 offen offset:3072 lds\n"
"s_add_u32 s48, s48, s61\n"
"s_waitcnt lgkmcnt(0)\n"
"s_waitcnt vmcnt(12)\n"
"v_mfma_f32_16x16x32_bf16 v[0:3], a[224:227], v[144:147], v[0:3]\n"
"s_barrier\n"
"ds_read_b64_tr_b16 a[192:193], v235 offset:49152\n"
"v_fma_mix_f32 v224, v160, v192, v200 op_sel:[0,0,0] op_sel_hi:[1,0,0]\n"
"v_fma_mix_f32 v225, v160, v193, v201 op_sel:[1,0,0] op_sel_hi:[1,0,0]\n"
"v_mfma_f32_16x16x32_bf16 v[32:35], a[224:227], v[148:151], v[32:35]\n"
"ds_read_b64_tr_b16 a[194:195], v235 offset:53248\n"
"v_cvt_pk_bf16_f32 v160, v224, v225\n"
"v_fma_mix_f32 v226, v161, v194, v202 op_sel:[0,0,0] op_sel_hi:[1,0,0]\n"
"v_mfma_f32_16x16x32_bf16 v[64:67], a[224:227], v[152:155], v[64:67]\n"
"ds_read_b128 v[208:211], v247 offset:3840\n"
"v_fma_mix_f32 v227, v161, v195, v203 op_sel:[1,0,0] op_sel_hi:[1,0,0]\n"
"v_mfma_f32_16x16x32_bf16 v[96:99], a[224:227], v[156:159], v[96:99]\n"
"s_add_u32 m0, s50, 0x8000\n"
"ds_read_b64_tr_b16 a[196:197], v235 offset:49160\n"
"v_cvt_pk_bf16_f32 v161, v226, v227\n"
"v_fma_mix_f32 v224, v162, v196, v204 op_sel:[0,0,0] op_sel_hi:[1,0,0]\n"
"v_mfma_f32_16x16x32_bf16 v[4:7], a[228:231], v[144:147], v[4:7]\n"
"buffer_load_dwordx4 v236, s[20:23], s47 offen offset:0 lds\n"
"ds_read_b64_tr_b16 a[198:199], v235 offset:53256\n"
"v_fma_mix_f32 v225, v162, v197, v205 op_sel:[1,0,0] op_sel_hi:[1,0,0]\n"
"v_mfma_f32_16x16x32_bf16 v[36:39], a[228:231], v[148:151], v[36:39]\n"
"ds_read_b128 v[212:215], v247 offset:3856\n"
"v_cvt_pk_bf16_f32 v162, v224, v225\n"
"v_fma_mix_f32 v226, v163, v198, v206 op_sel:[0,0,0] op_sel_hi:[1,0,0]\n"
"v_mfma_f32_16x16x32_bf16 v[68:71], a[228:231], v[152:155], v[68:71]\n"
"ds_read_b64_tr_b16 a[200:201], v235 offset:49664\n"
"v_fma_mix_f32 v227, v163, v199, v207 op_sel:[1,0,0] op_sel_hi:[1,0,0]\n"
"v_cvt_pk_bf16_f32 v163, v226, v227\n"
"v_mfma_f32_16x16x32_bf16 v[100:103], a[228:231], v[156:159], v[100:103]\n"
"buffer_load_dwordx4 v237, s[20:23], s47 offen offset:1024 lds\n"
"ds_read_b64_tr_b16 a[202:203], v235 offset:53760\n"
"v_fma_mix_f32 v224, v164, v192, v200 op_sel:[0,0,0] op_sel_hi:[1,0,0]\n"
"v_mfma_f32_16x16x32_bf16 v[8:11], a[232:235], v[144:147], v[8:11]\n"
"ds_read_b128 v[216:219], v247 offset:3872\n"
"v_fma_mix_f32 v225, v164, v193, v201 op_sel:[1,0,0] op_sel_hi:[1,0,0]\n"
"v_cvt_pk_bf16_f32 v164, v224, v225\n"
"v_mfma_f32_16x16x32_bf16 v[40:43], a[232:235], v[148:151], v[40:43]\n"
"ds_read_b64_tr_b16 a[204:205], v235 offset:49672\n"
"v_fma_mix_f32 v226, v165, v194, v202 op_sel:[0,0,0] op_sel_hi:[1,0,0]\n"
"v_mfma_f32_16x16x32_bf16 v[72:75], a[232:235], v[152:155], v[72:75]\n"
"buffer_load_dwordx4 v238, s[20:23], s47 offen offset:2048 lds\n"
"ds_read_b64_tr_b16 a[206:207], v235 offset:53768\n"
"v_fma_mix_f32 v227, v165, v195, v203 op_sel:[1,0,0] op_sel_hi:[1,0,0]\n"
"v_cvt_pk_bf16_f32 v165, v226, v227\n"
"v_mfma_f32_16x16x32_bf16 v[104:107], a[232:235], v[156:159], v[104:107]\n"
"ds_read_b128 v[220:223], v247 offset:3888\n"
"v_fma_mix_f32 v224, v166, v196, v204 op_sel:[0,0,0] op_sel_hi:[1,0,0]\n"
"v_fma_mix_f32 v225, v166, v197, v205 op_sel:[1,0,0] op_sel_hi:[1,0,0]\n"
"v_mfma_f32_16x16x32_bf16 v[12:15], a[236:239], v[144:147], v[12:15]\n"
"ds_read_b64_tr_b16 a[208:209], v235 offset:50176\n"
"v_cvt_pk_bf16_f32 v166, v224, v225\n"
"v_mfma_f32_16x16x32_bf16 v[44:47], a[236:239], v[148:151], v[44:47]\n"
"buffer_load_dwordx4 v239, s[20:23], s47 offen offset:3072 lds\n"
"ds_read_b64_tr_b16 a[210:211], v235 offset:54272\n"
"v_fma_mix_f32 v226, v167, v198, v206 op_sel:[0,0,0] op_sel_hi:[1,0,0]\n"
"v_fma_mix_f32 v227, v167, v199, v207 op_sel:[1,0,0] op_sel_hi:[1,0,0]\n"
"v_mfma_f32_16x16x32_bf16 v[76:79], a[236:239], v[152:155], v[76:79]\n"
"s_add_u32 s47, s47, s55\n"
"v_cvt_pk_bf16_f32 v167, v226, v227\n"
"v_mfma_f32_16x16x32_bf16 v[108:111], a[236:239], v[156:159], v[108:111]\n"
"ds_read_b64_tr_b16 a[212:213], v235 offset:50184\n"
"v_fma_mix_f32 v224, v168, v192, v200 op_sel:[0,0,0] op_sel_hi:[1,0,0]\n"
"v_fma_mix_f32 v225, v168, v193, v201 op_sel:[1,0,0] op_sel_hi:[1,0,0]\n"
"v_mfma_f32_16x16x32_bf16 v[16:19], a[240:243], v[144:147], v[16:19]\n"
"ds_read_b64_tr_b16 a[214:215], v235 offset:54280\n"
"v_cvt_pk_bf16_f32 v168, v224, v225\n"
"v_fma_mix_f32 v226, v169, v194, v202 op_sel:[0,0,0] op_sel_hi:[1,0,0]\n"
"v_mfma_f32_16x16x32_bf16 v[48:51], a[240:243], v[148:151], v[48:51]\n"
"s_add_u32 m0, s51, 0x4000\n"
"v_fma_mix_f32 v227, v169, v195, v203 op_sel:[1,0,0] op_sel_hi:[1,0,0]\n"
"v_mfma_f32_16x16x32_bf16 v[80:83], a[240:243], v[152:155], v[80:83]\n"
"buffer_load_dwordx4 v240, s[24:27], s48 offen offset:0 lds\n"
"ds_read_b64_tr_b16 a[216:217], v235 offset:50688\n"
"v_cvt_pk_bf16_f32 v169, v226, v227\n"
"v_fma_mix_f32 v224, v170, v196, v204 op_sel:[0,0,0] op_sel_hi:[1,0,0]\n"
"v_mfma_f32_16x16x32_bf16 v[112:115], a[240:243], v[156:159], v[112:115]\n"
"ds_read_b64_tr_b16 a[218:219], v235 offset:54784\n"
"v_fma_mix_f32 v225, v170, v197, v205 op_sel:[1,0,0] op_sel_hi:[1,0,0]\n"
"v_mfma_f32_16x16x32_bf16 v[20:23], a[244:247], v[144:147], v[20:23]\n"
"v_cvt_pk_bf16_f32 v170, v224, v225\n"
"v_fma_mix_f32 v226, v171, v198, v206 op_sel:[0,0,0] op_sel_hi:[1,0,0]\n"
"v_mfma_f32_16x16x32_bf16 v[52:55], a[244:247], v[148:151], v[52:55]\n"
"buffer_load_dwordx4 v241, s[24:27], s48 offen offset:1024 lds\n"
"ds_read_b64_tr_b16 a[220:221], v235 offset:50696\n"
"v_fma_mix_f32 v227, v171, v199, v207 op_sel:[1,0,0] op_sel_hi:[1,0,0]\n"
"v_cvt_pk_bf16_f32 v171, v226, v227\n"
"v_mfma_f32_16x16x32_bf16 v[84:87], a[244:247], v[152:155], v[84:87]\n"
"ds_read_b64_tr_b16 a[222:223], v235 offset:54792\n"
"v_fma_mix_f32 v224, v172, v192, v200 op_sel:[0,0,0] op_sel_hi:[1,0,0]\n"
"v_mfma_f32_16x16x32_bf16 v[116:119], a[244:247], v[156:159], v[116:119]\n"
"v_fma_mix_f32 v225, v172, v193, v201 op_sel:[1,0,0] op_sel_hi:[1,0,0]\n"
"v_cvt_pk_bf16_f32 v172, v224, v225\n"
"v_mfma_f32_16x16x32_bf16 v[24:27], a[248:251], v[144:147], v[24:27]\n"
"buffer_load_dwordx4 v240, s[24:27], s48 offen offset:2048 lds\n"
"v_fma_mix_f32 v226, v173, v194, v202 op_sel:[0,0,0] op_sel_hi:[1,0,0]\n"
"v_mfma_f32_16x16x32_bf16 v[56:59], a[248:251], v[148:151], v[56:59]\n"
"v_fma_mix_f32 v227, v173, v195, v203 op_sel:[1,0,0] op_sel_hi:[1,0,0]\n"
"v_cvt_pk_bf16_f32 v173, v226, v227\n"
"v_mfma_f32_16x16x32_bf16 v[88:91], a[248:251], v[152:155], v[88:91]\n"
"v_fma_mix_f32 v224, v174, v196, v204 op_sel:[0,0,0] op_sel_hi:[1,0,0]\n"
"v_fma_mix_f32 v225, v174, v197, v205 op_sel:[1,0,0] op_sel_hi:[1,0,0]\n"
"v_mfma_f32_16x16x32_bf16 v[120:123], a[248:251], v[156:159], v[120:123]\n"
"buffer_load_dwordx4 v241, s[24:27], s48 offen offset:3072 lds\n"
"v_cvt_pk_bf16_f32 v174, v224, v225\n"
"v_mfma_f32_16x16x32_bf16 v[28:31], a[252:255], v[144:147], v[28:31]\n"
"s_add_u32 s48, s48, s61\n"
"v_fma_mix_f32 v226, v175, v198, v206 op_sel:[0,0,0] op_sel_hi:[1,0,0]\n"
"v_fma_mix_f32 v227, v175, v199, v207 op_sel:[1,0,0] op_sel_hi:[1,0,0]\n"
"v_mfma_f32_16x16x32_bf16 v[60:63], a[252:255], v[148:151], v[60:63]\n"
"v_cvt_pk_bf16_f32 v175, v226, v227\n"
"v_mfma_f32_16x16x32_bf16 v[92:95], a[252:255], v[152:155], v[92:95]\n"
"v_mfma_f32_16x16x32_bf16 v[124:127], a[252:255], v[156:159], v[124:127]\n"
"s_waitcnt lgkmcnt(0)\n"
"s_waitcnt vmcnt(16)\n"
"v_mfma_f32_16x16x32_bf16 v[0:3], a[192:195], v[160:163], v[0:3]\n"
"ds_read_b64_tr_b16 a[224:225], v235 offset:57344\n"
"v_fma_mix_f32 v224, v176, v208, v216 op_sel:[0,0,0] op_sel_hi:[1,0,0]\n"
"v_fma_mix_f32 v225, v176, v209, v217 op_sel:[1,0,0] op_sel_hi:[1,0,0]\n"
"v_mfma_f32_16x16x32_bf16 v[32:35], a[192:195], v[164:167], v[32:35]\n"
"ds_read_b64_tr_b16 a[226:227], v235 offset:61440\n"
"v_cvt_pk_bf16_f32 v176, v224, v225\n"
"v_fma_mix_f32 v226, v177, v210, v218 op_sel:[0,0,0] op_sel_hi:[1,0,0]\n"
"v_mfma_f32_16x16x32_bf16 v[64:67], a[192:195], v[168:171], v[64:67]\n"
"ds_read_b128 a[128:131], v232 offset:0\n"
"v_fma_mix_f32 v227, v177, v211, v219 op_sel:[1,0,0] op_sel_hi:[1,0,0]\n"
"v_mfma_f32_16x16x32_bf16 v[96:99], a[192:195], v[172:175], v[96:99]\n"
"s_add_u32 m0, s50, 0xc000\n"
"ds_read_b64_tr_b16 a[228:229], v235 offset:57352\n"
"v_cvt_pk_bf16_f32 v177, v226, v227\n"
"v_fma_mix_f32 v224, v178, v212, v220 op_sel:[0,0,0] op_sel_hi:[1,0,0]\n"
"v_mfma_f32_16x16x32_bf16 v[4:7], a[196:199], v[160:163], v[4:7]\n"
"buffer_load_dwordx4 v236, s[20:23], s47 offen offset:0 lds\n"
"ds_read_b64_tr_b16 a[230:231], v235 offset:61448\n"
"v_fma_mix_f32 v225, v178, v213, v221 op_sel:[1,0,0] op_sel_hi:[1,0,0]\n"
"v_mfma_f32_16x16x32_bf16 v[36:39], a[196:199], v[164:167], v[36:39]\n"
"ds_read_b128 a[132:135], v232 offset:512\n"
"v_cvt_pk_bf16_f32 v178, v224, v225\n"
"v_fma_mix_f32 v226, v179, v214, v222 op_sel:[0,0,0] op_sel_hi:[1,0,0]\n"
"v_mfma_f32_16x16x32_bf16 v[68:71], a[196:199], v[168:171], v[68:71]\n"
"ds_read_b64_tr_b16 a[232:233], v235 offset:57856\n"
"v_fma_mix_f32 v227, v179, v215, v223 op_sel:[1,0,0] op_sel_hi:[1,0,0]\n"
"v_cvt_pk_bf16_f32 v179, v226, v227\n"
"v_mfma_f32_16x16x32_bf16 v[100:103], a[196:199], v[172:175], v[100:103]\n"
"buffer_load_dwordx4 v237, s[20:23], s47 offen offset:1024 lds\n"
"ds_read_b64_tr_b16 a[234:235], v235 offset:61952\n"
"v_fma_mix_f32 v224, v180, v208, v216 op_sel:[0,0,0] op_sel_hi:[1,0,0]\n"
"v_mfma_f32_16x16x32_bf16 v[8:11], a[200:203], v[160:163], v[8:11]\n"
"ds_read_b128 a[136:139], v232 offset:1024\n"
"v_fma_mix_f32 v225, v180, v209, v217 op_sel:[1,0,0] op_sel_hi:[1,0,0]\n"
"v_cvt_pk_bf16_f32 v180, v224, v225\n"
"v_mfma_f32_16x16x32_bf16 v[40:43], a[200:203], v[164:167], v[40:43]\n"
"ds_read_b64_tr_b16 a[236:237], v235 offset:57864\n"
"v_fma_mix_f32 v226, v181, v210, v218 op_sel:[0,0,0] op_sel_hi:[1,0,0]\n"
"v_mfma_f32_16x16x32_bf16 v[72:75], a[200:203], v[168:171], v[72:75]\n"
"buffer_load_dwordx4 v238, s[20:23], s47 offen offset:2048 lds\n"
"ds_read_b64_tr_b16 a[238:239], v235 offset:61960\n"
"v_fma_mix_f32 v227, v181, v211, v219 op_sel:[1,0,0] op_sel_hi:[1,0,0]\n"
"v_cvt_pk_bf16_f32 v181, v226, v227\n"
"v_mfma_f32_16x16x32_bf16 v[104:107], a[200:203], v[172:175], v[104:107]\n"
"ds_read_b128 a[140:143], v232 offset:1536\n"
"v_fma_mix_f32 v224, v182, v212, v220 op_sel:[0,0,0] op_sel_hi:[1,0,0]\n"
"v_fma_mix_f32 v225, v182, v213, v221 op_sel:[1,0,0] op_sel_hi:[1,0,0]\n"
"v_mfma_f32_16x16x32_bf16 v[12:15], a[204:207], v[160:163], v[12:15]\n"
"ds_read_b64_tr_b16 a[240:241], v235 offset:58368\n"
"v_cvt_pk_bf16_f32 v182, v224, v225\n"
"v_mfma_f32_16x16x32_bf16 v[44:47], a[204:207], v[164:167], v[44:47]\n"
"buffer_load_dwordx4 v239, s[20:23], s47 offen offset:3072 lds\n"
"ds_read_b64_tr_b16 a[242:243], v235 offset:62464\n"
"v_fma_mix_f32 v226, v183, v214, v222 op_sel:[0,0,0] op_sel_hi:[1,0,0]\n"
"v_fma_mix_f32 v227, v183, v215, v223 op_sel:[1,0,0] op_sel_hi:[1,0,0]\n"
"v_mfma_f32_16x16x32_bf16 v[76:79], a[204:207], v[168:171], v[76:79]\n"
"s_add_u32 s47, s47, s55\n"
"ds_read_b128 a[144:147], v232 offset:4096\n"
"v_cvt_pk_bf16_f32 v183, v226, v227\n"
"v_mfma_f32_16x16x32_bf16 v[108:111], a[204:207], v[172:175], v[108:111]\n"
"ds_read_b64_tr_b16 a[244:245], v235 offset:58376\n"
"v_fma_mix_f32 v224, v184, v208, v216 op_sel:[0,0,0] op_sel_hi:[1,0,0]\n"
"v_fma_mix_f32 v225, v184, v209, v217 op_sel:[1,0,0] op_sel_hi:[1,0,0]\n"
"v_mfma_f32_16x16x32_bf16 v[16:19], a[208:211], v[160:163], v[16:19]\n"
"ds_read_b64_tr_b16 a[246:247], v235 offset:62472\n"
"v_cvt_pk_bf16_f32 v184, v224, v225\n"
"v_fma_mix_f32 v226, v185, v210, v218 op_sel:[0,0,0] op_sel_hi:[1,0,0]\n"
"v_mfma_f32_16x16x32_bf16 v[48:51], a[208:211], v[164:167], v[48:51]\n"
"s_add_u32 m0, s51, 0x8000\n"
"ds_read_b128 a[148:151], v232 offset:4608\n"
"v_fma_mix_f32 v227, v185, v211, v219 op_sel:[1,0,0] op_sel_hi:[1,0,0]\n"
"v_mfma_f32_16x16x32_bf16 v[80:83], a[208:211], v[168:171], v[80:83]\n"
"buffer_load_dwordx4 v240, s[24:27], s48 offen offset:0 lds\n"
"ds_read_b64_tr_b16 a[248:249], v235 offset:58880\n"
"v_cvt_pk_bf16_f32 v185, v226, v227\n"
"v_fma_mix_f32 v224, v186, v212, v220 op_sel:[0,0,0] op_sel_hi:[1,0,0]\n"
"v_mfma_f32_16x16x32_bf16 v[112:115], a[208:211], v[172:175], v[112:115]\n"
"ds_read_b64_tr_b16 a[250:251], v235 offset:62976\n"
"v_fma_mix_f32 v225, v186, v213, v221 op_sel:[1,0,0] op_sel_hi:[1,0,0]\n"
"v_mfma_f32_16x16x32_bf16 v[20:23], a[212:215], v[160:163], v[20:23]\n"
"ds_read_b128 a[152:155], v232 offset:5120\n"
"v_cvt_pk_bf16_f32 v186, v224, v225\n"
"v_fma_mix_f32 v226, v187, v214, v222 op_sel:[0,0,0] op_sel_hi:[1,0,0]\n"
"v_mfma_f32_16x16x32_bf16 v[52:55], a[212:215], v[164:167], v[52:55]\n"
"buffer_load_dwordx4 v241, s[24:27], s48 offen offset:1024 lds\n"
"ds_read_b64_tr_b16 a[252:253], v235 offset:58888\n"
"v_fma_mix_f32 v227, v187, v215, v223 op_sel:[1,0,0] op_sel_hi:[1,0,0]\n"
"v_cvt_pk_bf16_f32 v187, v226, v227\n"
"v_mfma_f32_16x16x32_bf16 v[84:87], a[212:215], v[168:171], v[84:87]\n"
"ds_read_b64_tr_b16 a[254:255], v235 offset:62984\n"
"v_fma_mix_f32 v224, v188, v208, v216 op_sel:[0,0,0] op_sel_hi:[1,0,0]\n"
"v_mfma_f32_16x16x32_bf16 v[116:119], a[212:215], v[172:175], v[116:119]\n"
"ds_read_b128 a[156:159], v232 offset:5632\n"
"v_fma_mix_f32 v225, v188, v209, v217 op_sel:[1,0,0] op_sel_hi:[1,0,0]\n"
"v_cvt_pk_bf16_f32 v188, v224, v225\n"
"v_mfma_f32_16x16x32_bf16 v[24:27], a[216:219], v[160:163], v[24:27]\n"
"buffer_load_dwordx4 v240, s[24:27], s48 offen offset:2048 lds\n"
"v_fma_mix_f32 v226, v189, v210, v218 op_sel:[0,0,0] op_sel_hi:[1,0,0]\n"
"v_mfma_f32_16x16x32_bf16 v[56:59], a[216:219], v[164:167], v[56:59]\n"
"v_fma_mix_f32 v227, v189, v211, v219 op_sel:[1,0,0] op_sel_hi:[1,0,0]\n"
"v_cvt_pk_bf16_f32 v189, v226, v227\n"
"v_mfma_f32_16x16x32_bf16 v[88:91], a[216:219], v[168:171], v[88:91]\n"
"v_fma_mix_f32 v224, v190, v212, v220 op_sel:[0,0,0] op_sel_hi:[1,0,0]\n"
"v_fma_mix_f32 v225, v190, v213, v221 op_sel:[1,0,0] op_sel_hi:[1,0,0]\n"
"v_mfma_f32_16x16x32_bf16 v[120:123], a[216:219], v[172:175], v[120:123]\n"
"buffer_load_dwordx4 v241, s[24:27], s48 offen offset:3072 lds\n"
"v_cvt_pk_bf16_f32 v190, v224, v225\n"
"v_mfma_f32_16x16x32_bf16 v[28:31], a[220:223], v[160:163], v[28:31]\n"
"s_add_u32 s48, s48, s61\n"
"v_fma_mix_f32 v226, v191, v214, v222 op_sel:[0,0,0] op_sel_hi:[1,0,0]\n"
"v_fma_mix_f32 v227, v191, v215, v223 op_sel:[1,0,0] op_sel_hi:[1,0,0]\n"
"v_mfma_f32_16x16x32_bf16 v[60:63], a[220:223], v[164:167], v[60:63]\n"
"v_cvt_pk_bf16_f32 v191, v226, v227\n"
"v_mfma_f32_16x16x32_bf16 v[92:95], a[220:223], v[168:171], v[92:95]\n"
"v_mfma_f32_16x16x32_bf16 v[124:127], a[220:223], v[172:175], v[124:127]\n"
"s_waitcnt lgkmcnt(1)\n"
"v_mfma_f32_16x16x32_bf16 v[0:3], a[224:227], v[176:179], v[0:3]\n"
"v_mfma_f32_16x16x32_bf16 v[32:35], a[224:227], v[180:183], v[32:35]\n"
"v_mfma_f32_16x16x32_bf16 v[64:67], a[224:227], v[184:187], v[64:67]\n"
"ds_read_b128 a[160:163], v232 offset:8192\n"
"v_mfma_f32_16x16x32_bf16 v[96:99], a[224:227], v[188:191], v[96:99]\n"
"v_mfma_f32_16x16x32_bf16 v[4:7], a[228:231], v[176:179], v[4:7]\n"
"v_mfma_f32_16x16x32_bf16 v[36:39], a[228:231], v[180:183], v[36:39]\n"
"ds_read_b128 a[164:167], v232 offset:8704\n"
"v_mfma_f32_16x16x32_bf16 v[68:71], a[228:231], v[184:187], v[68:71]\n"
"v_mfma_f32_16x16x32_bf16 v[100:103], a[228:231], v[188:191], v[100:103]\n"
"v_mfma_f32_16x16x32_bf16 v[8:11], a[232:235], v[176:179], v[8:11]\n"
"ds_read_b128 a[168:171], v232 offset:9216\n"
"v_mfma_f32_16x16x32_bf16 v[40:43], a[232:235], v[180:183], v[40:43]\n"
"v_mfma_f32_16x16x32_bf16 v[72:75], a[232:235], v[184:187], v[72:75]\n"
"v_mfma_f32_16x16x32_bf16 v[104:107], a[232:235], v[188:191], v[104:107]\n"
"ds_read_b128 a[172:175], v232 offset:9728\n"
"v_mfma_f32_16x16x32_bf16 v[12:15], a[236:239], v[176:179], v[12:15]\n"
"v_mfma_f32_16x16x32_bf16 v[44:47], a[236:239], v[180:183], v[44:47]\n"
"v_mfma_f32_16x16x32_bf16 v[76:79], a[236:239], v[184:187], v[76:79]\n"
"ds_read_b128 a[176:179], v232 offset:12288\n"
"v_mfma_f32_16x16x32_bf16 v[108:111], a[236:239], v[188:191], v[108:111]\n"
"v_mfma_f32_16x16x32_bf16 v[16:19], a[240:243], v[176:179], v[16:19]\n"
"v_mfma_f32_16x16x32_bf16 v[48:51], a[240:243], v[180:183], v[48:51]\n"
"ds_read_b128 a[180:183], v232 offset:12800\n"
"v_mfma_f32_16x16x32_bf16 v[80:83], a[240:243], v[184:187], v[80:83]\n"
"v_mfma_f32_16x16x32_bf16 v[112:115], a[240:243], v[188:191], v[112:115]\n"
"v_mfma_f32_16x16x32_bf16 v[20:23], a[244:247], v[176:179], v[20:23]\n"
"ds_read_b128 a[184:187], v232 offset:13312\n"
"v_mfma_f32_16x16x32_bf16 v[52:55], a[244:247], v[180:183], v[52:55]\n"
"v_mfma_f32_16x16x32_bf16 v[84:87], a[244:247], v[184:187], v[84:87]\n"
"v_mfma_f32_16x16x32_bf16 v[116:119], a[244:247], v[188:191], v[116:119]\n"
"ds_read_b128 a[188:191], v232 offset:13824\n"
"v_mfma_f32_16x16x32_bf16 v[24:27], a[248:251], v[176:179], v[24:27]\n"
"v_mfma_f32_16x16x32_bf16 v[56:59], a[248:251], v[180:183], v[56:59]\n"
"v_mfma_f32_16x16x32_bf16 v[88:91], a[248:251], v[184:187], v[88:91]\n"
"v_mfma_f32_16x16x32_bf16 v[120:123], a[248:251], v[188:191], v[120:123]\n"
"v_mfma_f32_16x16x32_bf16 v[28:31], a[252:255], v[176:179], v[28:31]\n"
"v_mfma_f32_16x16x32_bf16 v[60:63], a[252:255], v[180:183], v[60:63]\n"
"v_mfma_f32_16x16x32_bf16 v[92:95], a[252:255], v[184:187], v[92:95]\n"
"v_mfma_f32_16x16x32_bf16 v[124:127], a[252:255], v[188:191], v[124:127]\n"
"s_nop 7\n"
"v_cvt_pk_bf16_f32 v0, v0, v1\n"
"v_cvt_pk_bf16_f32 v1, v2, v3\n"
"v_cvt_pk_bf16_f32 v2, v4, v5\n"
"v_cvt_pk_bf16_f32 v3, v6, v7\n"
"v_cvt_pk_bf16_f32 v32, v32, v33\n"
"v_cvt_pk_bf16_f32 v33, v34, v35\n"
"v_cvt_pk_bf16_f32 v34, v36, v37\n"
"v_cvt_pk_bf16_f32 v35, v38, v39\n"
"v_cvt_pk_bf16_f32 v64, v64, v65\n"
"v_cvt_pk_bf16_f32 v65, v66, v67\n"
"v_cvt_pk_bf16_f32 v66, v68, v69\n"
"v_cvt_pk_bf16_f32 v67, v70, v71\n"
"v_cvt_pk_bf16_f32 v96, v96, v97\n"
"v_cvt_pk_bf16_f32 v97, v98, v99\n"
"v_cvt_pk_bf16_f32 v98, v100, v101\n"
"v_cvt_pk_bf16_f32 v99, v102, v103\n"
"v_cvt_pk_bf16_f32 v8, v8, v9\n"
"v_cvt_pk_bf16_f32 v9, v10, v11\n"
"v_cvt_pk_bf16_f32 v10, v12, v13\n"
"v_cvt_pk_bf16_f32 v11, v14, v15\n"
"v_cvt_pk_bf16_f32 v40, v40, v41\n"
"v_cvt_pk_bf16_f32 v41, v42, v43\n"
"v_cvt_pk_bf16_f32 v42, v44, v45\n"
"v_cvt_pk_bf16_f32 v43, v46, v47\n"
"v_cvt_pk_bf16_f32 v72, v72, v73\n"
"v_cvt_pk_bf16_f32 v73, v74, v75\n"
"v_cvt_pk_bf16_f32 v74, v76, v77\n"
"v_cvt_pk_bf16_f32 v75, v78, v79\n"
"v_cvt_pk_bf16_f32 v104, v104, v105\n"
"v_cvt_pk_bf16_f32 v105, v106, v107\n"
"v_cvt_pk_bf16_f32 v106, v108, v109\n"
"v_cvt_pk_bf16_f32 v107, v110, v111\n"
"v_cvt_pk_bf16_f32 v16, v16, v17\n"
"v_cvt_pk_bf16_f32 v17, v18, v19\n"
"v_cvt_pk_bf16_f32 v18, v20, v21\n"
"v_cvt_pk_bf16_f32 v19, v22, v23\n"
"v_cvt_pk_bf16_f32 v48, v48, v49\n"
"v_cvt_pk_bf16_f32 v49, v50, v51\n"
"v_cvt_pk_bf16_f32 v50, v52, v53\n"
"v_cvt_pk_bf16_f32 v51, v54, v55\n"
"v_cvt_pk_bf16_f32 v80, v80, v81\n"
"v_cvt_pk_bf16_f32 v81, v82, v83\n"
"v_cvt_pk_bf16_f32 v82, v84, v85\n"
"v_cvt_pk_bf16_f32 v83, v86, v87\n"
"v_cvt_pk_bf16_f32 v112, v112, v113\n"
"v_cvt_pk_bf16_f32 v113, v114, v115\n"
"v_cvt_pk_bf16_f32 v114, v116, v117\n"
"v_cvt_pk_bf16_f32 v115, v118, v119\n"
"v_cvt_pk_bf16_f32 v24, v24, v25\n"
"v_cvt_pk_bf16_f32 v25, v26, v27\n"
"v_cvt_pk_bf16_f32 v26, v28, v29\n"
"v_cvt_pk_bf16_f32 v27, v30, v31\n"
"v_cvt_pk_bf16_f32 v56, v56, v57\n"
"v_cvt_pk_bf16_f32 v57, v58, v59\n"
"v_cvt_pk_bf16_f32 v58, v60, v61\n"
"v_cvt_pk_bf16_f32 v59, v62, v63\n"
"v_cvt_pk_bf16_f32 v88, v88, v89\n"
"v_cvt_pk_bf16_f32 v89, v90, v91\n"
"v_cvt_pk_bf16_f32 v90, v92, v93\n"
"v_cvt_pk_bf16_f32 v91, v94, v95\n"
"v_cvt_pk_bf16_f32 v120, v120, v121\n"
"v_cvt_pk_bf16_f32 v121, v122, v123\n"
"v_cvt_pk_bf16_f32 v122, v124, v125\n"
"v_cvt_pk_bf16_f32 v123, v126, v127\n"
"s_nop 1\n"
"s_waitcnt lgkmcnt(8)\n"
"v_mfma_f32_16x16x32_bf16 v[128:131], a[128:131], v[0:3], 0\n"
"s_cmp_eq_u32 s44, 2\n"
"s_cselect_b32 s62, s94, s96\n"
"s_cselect_b32 s63, s95, s97\n"
"s_mul_i32 s57, s16, 0x1c00\n"
"v_mfma_f32_16x16x32_bf16 v[128:131], a[132:135], v[8:11], v[128:131]\n"
"s_mov_b32 s58, 0x1c00\n"
"s_lshl_b32 s59, s16, 14\n"
"v_mfma_f32_16x16x32_bf16 v[128:131], a[136:139], v[16:19], v[128:131]\n"
"s_lshl_b32 s60, s44, 13\n"
"s_add_u32 s59, s59, s60\n"
"v_mfma_f32_16x16x32_bf16 v[128:131], a[140:143], v[24:27], v[128:131]\n"
"s_cmp_lt_u32 s44, 2\n"
"s_cselect_b32 s62, s92, s62\n"
"s_cselect_b32 s63, s93, s63\n"
"s_cselect_b32 s57, s59, s57\n"
"s_cselect_b32 s58, 0x2000, s58\n"
"s_add_u32 s68, s62, s57\n"
"s_addc_u32 s69, s63, 0\n"
"v_mfma_f32_16x16x32_bf16 v[132:135], a[144:147], v[0:3], 0\n"
"s_and_b32 s69, s69, 0xffff\n"
"s_mov_b32 s70, s58\n"
"v_mfma_f32_16x16x32_bf16 v[132:135], a[148:151], v[8:11], v[132:135]\n"
"s_mov_b32 s71, 0x20000\n"
"s_lshl_b32 s57, s16, 2\n"
"v_mfma_f32_16x16x32_bf16 v[132:135], a[152:155], v[16:19], v[132:135]\n"
"s_add_u32 s57, s57, s44\n"
"s_mul_i32 s57, s57, 0x700\n"
"v_mfma_f32_16x16x32_bf16 v[132:135], a[156:159], v[24:27], v[132:135]\n"
"s_add_u32 s40, s98, s57\n"
"s_addc_u32 s41, s99, 0\n"
"s_and_b32 s41, s41, 0xffff\n"
"v_mfma_f32_16x16x32_bf16 v[136:139], a[128:131], v[32:35], 0\n"
"s_mov_b32 s42, 0x700\n"
"s_mov_b32 s43, 0x20000\n"
"v_mfma_f32_16x16x32_bf16 v[136:139], a[132:135], v[40:43], v[136:139]\n"
"v_mov_b32 v253, v246\n"
"v_mov_b32 v217, v240\n"
"v_mfma_f32_16x16x32_bf16 v[136:139], a[136:139], v[48:51], v[136:139]\n"
"v_mov_b32 v218, v241\n"
"v_mbcnt_lo_u32_b32 v226, -1, 0\n"
"v_mfma_f32_16x16x32_bf16 v[136:139], a[140:143], v[56:59], v[136:139]\n"
"v_mbcnt_hi_u32_b32 v226, -1, v226\n"
"v_bfe_u32 v224, v226, 4, 1\n"
"v_mfma_f32_16x16x32_bf16 v[140:143], a[144:147], v[32:35], 0\n"
"v_lshlrev_b32 v224, 1, v224\n"
"v_and_b32 v225, 3, v226\n"
"v_mfma_f32_16x16x32_bf16 v[140:143], a[148:151], v[40:43], v[140:143]\n"
"v_xor_b32 v224, v224, v225\n"
"v_lshlrev_b32 v224, 4, v224\n"
"v_mfma_f32_16x16x32_bf16 v[140:143], a[152:155], v[48:51], v[140:143]\n"
"v_lshrrev_b32 v225, 5, v226\n"
"v_lshl_add_u32 v224, v225, 6, v224\n"
"v_mfma_f32_16x16x32_bf16 v[140:143], a[156:159], v[56:59], v[140:143]\n"
"v_bfe_u32 v225, v226, 2, 3\n"
"v_lshl_add_u32 v224, v225, 10, v224\n"
"v_mfma_f32_16x16x32_bf16 v[144:147], a[128:131], v[64:67], 0\n"
"v_add_u32 v242, 0x400, v224\n"
"v_add_u32 v243, 0x80, v224\n"
"v_mfma_f32_16x16x32_bf16 v[144:147], a[132:135], v[72:75], v[144:147]\n"
"v_add_u32 v244, 0x1c00, v224\n"
"v_add_u32 v245, 0x1880, v224\n"
"v_mfma_f32_16x16x32_bf16 v[144:147], a[136:139], v[80:83], v[144:147]\n"
"s_bfe_u32 s59, s16, 0x20001\n"
"s_lshl_b32 s59, s59, 20\n"
"v_mfma_f32_16x16x32_bf16 v[144:147], a[140:143], v[88:91], v[144:147]\n"
"s_lshr_b32 s60, s16, 6\n"
"s_lshl_b32 s60, s60, 8\n"
"v_mfma_f32_16x16x32_bf16 v[148:151], a[144:147], v[64:67], 0\n"
"s_add_u32 s59, s59, s60\n"
"s_sub_u32 s60, 0x100400, s60\n"
"v_mfma_f32_16x16x32_bf16 v[148:151], a[148:151], v[72:75], v[148:151]\n"
"s_add_u32 s64, s92, s59\n"
"s_addc_u32 s65, s93, 0\n"
"s_sub_u32 s64, s64, 0x400\n"
"s_subb_u32 s65, s65, 0\n"
"v_mfma_f32_16x16x32_bf16 v[148:151], a[152:155], v[80:83], v[148:151]\n"
"s_and_b32 s65, s65, 0xffff\n"
"s_mov_b32 s66, s60\n"
"v_mfma_f32_16x16x32_bf16 v[148:151], a[156:159], v[88:91], v[148:151]\n"
"s_mov_b32 s67, 0x20000\n"
"s_lshl_b32 s49, s44, 14\n"
"v_mfma_f32_16x16x32_bf16 v[152:155], a[128:131], v[96:99], 0\n"
"s_lshl_b32 s59, s44, 12\n"
"s_add_u32 s56, s17, s59\n"
"v_mfma_f32_16x16x32_bf16 v[152:155], a[132:135], v[104:107], v[152:155]\n"
"s_getpc_b64 s[28:29]\n"
"L_cpc_%=:\n"
"s_add_u32 s28, s28, L_cold_%=-L_cpc_%=\n"
"s_addc_u32 s29, s29, 0\n"
"v_mfma_f32_16x16x32_bf16 v[152:155], a[136:139], v[112:115], v[152:155]\n"
"s_and_b32 s29, s29, 0xffff\n"
"s_mov_b32 s30, L_end_%=-L_cold_%=\n"
"v_mfma_f32_16x16x32_bf16 v[152:155], a[140:143], v[120:123], v[152:155]\n"
"s_mov_b32 s31, 0x20000\n"
"s_bfe_u32 s57, s16, 0x20003\n"
"v_mfma_f32_16x16x32_bf16 v[156:159], a[144:147], v[96:99], 0\n"
"s_lshl_b32 s57, s57, 2\n"
"s_add_u32 s57, s57, s44\n"
"v_mfma_f32_16x16x32_bf16 v[156:159], a[148:151], v[104:107], v[156:159]\n"
"s_lshl_b32 s57, s57, 10\n"
"v_add_u32 v251, s57, v246\n"
"v_mfma_f32_16x16x32_bf16 v[156:159], a[152:155], v[112:115], v[156:159]\n"
"v_mov_b32 v116, 0x3f803f80\n"
"v_mov_b32 v117, 0x3f803f80\n"
"v_mfma_f32_16x16x32_bf16 v[156:159], a[156:159], v[120:123], v[156:159]\n"
"v_mov_b32 v118, 0x3f803f80\n"
"v_mov_b32 v119, 0x3f803f80\n"
"s_waitcnt lgkmcnt(0)\n"
"v_mfma_f32_16x16x32_bf16 v[160:163], a[160:163], v[0:3], 0\n"
"s_waitcnt vmcnt(16)\n"
"s_barrier\n"
"v_mfma_f32_16x16x32_bf16 v[160:163], a[164:167], v[8:11], v[160:163]\n"
"ds_read_b64_tr_b16 a[192:193], v233 offset:0\n"
"v_mfma_f32_16x16x32_bf16 v[160:163], a[168:171], v[16:19], v[160:163]\n"
"ds_read_b64_tr_b16 a[194:195], v233 offset:4096\n"
"v_max3_f32 v224, v128, v129, v130\n"
"v_max3_f32 v224, v224, v131, v132\n"
"v_mfma_f32_16x16x32_bf16 v[160:163], a[172:175], v[24:27], v[160:163]\n"
"ds_read_b64_tr_b16 a[196:197], v234 offset:0\n"
"v_max3_f32 v224, v224, v133, v134\n"
"v_max_f32 v224, v224, v135\n"
"v_mfma_f32_16x16x32_bf16 v[164:167], a[176:179], v[0:3], 0\n"
"ds_read_b64_tr_b16 a[198:199], v234 offset:4096\n"
"v_max3_f32 v225, v136, v137, v138\n"
"v_max3_f32 v225, v225, v139, v140\n"
"v_mfma_f32_16x16x32_bf16 v[164:167], a[180:183], v[8:11], v[164:167]\n"
"ds_read_b64_tr_b16 a[200:201], v233 offset:512\n"
"v_max3_f32 v225, v225, v141, v142\n"
"v_max_f32 v225, v225, v143\n"
"v_mfma_f32_16x16x32_bf16 v[164:167], a[184:187], v[16:19], v[164:167]\n"
"ds_read_b64_tr_b16 a[202:203], v233 offset:4608\n"
"v_max3_f32 v226, v144, v145, v146\n"
"v_max3_f32 v226, v226, v147, v148\n"
"v_mfma_f32_16x16x32_bf16 v[164:167], a[188:191], v[24:27], v[164:167]\n"
"ds_read_b64_tr_b16 a[204:205], v234 offset:512\n"
"v_max3_f32 v226, v226, v149, v150\n"
"v_max_f32 v226, v226, v151\n"
"v_mfma_f32_16x16x32_bf16 v[168:171], a[160:163], v[32:35], 0\n"
"ds_read_b64_tr_b16 a[206:207], v234 offset:4608\n"
"v_max3_f32 v227, v152, v153, v154\n"
"v_max3_f32 v227, v227, v155, v156\n"
"v_mfma_f32_16x16x32_bf16 v[168:171], a[164:167], v[40:43], v[168:171]\n"
"ds_read_b64_tr_b16 a[208:209], v233 offset:1024\n"
"v_max3_f32 v227, v227, v157, v158\n"
"v_max_f32 v227, v227, v159\n"
"v_mfma_f32_16x16x32_bf16 v[168:171], a[168:171], v[48:51], v[168:171]\n"
"ds_read_b64_tr_b16 a[210:211], v233 offset:5120\n"
"v_mfma_f32_16x16x32_bf16 v[168:171], a[172:175], v[56:59], v[168:171]\n"
"ds_read_b64_tr_b16 a[212:213], v234 offset:1024\n"
"v_mfma_f32_16x16x32_bf16 v[172:175], a[176:179], v[32:35], 0\n"
"ds_read_b64_tr_b16 a[214:215], v234 offset:5120\n"
"v_max3_f32 v224, v224, v160, v161\n"
"v_max3_f32 v224, v224, v162, v163\n"
"v_max3_f32 v224, v224, v164, v165\n"
"v_mfma_f32_16x16x32_bf16 v[172:175], a[180:183], v[40:43], v[172:175]\n"
"ds_read_b64_tr_b16 a[216:217], v233 offset:1536\n"
"v_max3_f32 v224, v224, v166, v167\n"
"v_mov_b32 v228, v224\n"
"v_mfma_f32_16x16x32_bf16 v[172:175], a[184:187], v[48:51], v[172:175]\n"
"ds_read_b64_tr_b16 a[218:219], v233 offset:5632\n"
"s_nop 1\n"
"v_permlane32_swap_b32 v224, v228\n"
"v_mfma_f32_16x16x32_bf16 v[172:175], a[188:191], v[56:59], v[172:175]\n"
"ds_read_b64_tr_b16 a[220:221], v234 offset:1536\n"
"v_max_f32 v224, v224, v228\n"
"v_mov_b32 v228, v224\n"
"v_mfma_f32_16x16x32_bf16 v[176:179], a[160:163], v[64:67], 0\n"
"ds_read_b64_tr_b16 a[222:223], v234 offset:5632\n"
"s_nop 1\n"
"v_permlane16_swap_b32 v224, v228\n"
"v_max_f32 v224, v224, v228\n"
"v_mfma_f32_16x16x32_bf16 v[176:179], a[164:167], v[72:75], v[176:179]\n"
"v_add_f32 v224, s54, v224\n"
"v_sub_f32 v128, v128, v224\n"
"v_mfma_f32_16x16x32_bf16 v[176:179], a[168:171], v[80:83], v[176:179]\n"
"v_sub_f32 v129, v129, v224\n"
"v_sub_f32 v130, v130, v224\n"
"v_mfma_f32_16x16x32_bf16 v[176:179], a[172:175], v[88:91], v[176:179]\n"
"v_sub_f32 v131, v131, v224\n"
"v_sub_f32 v132, v132, v224\n"
"v_mfma_f32_16x16x32_bf16 v[180:183], a[176:179], v[64:67], 0\n"
"v_sub_f32 v133, v133, v224\n"
"v_sub_f32 v134, v134, v224\n"
"v_sub_f32 v135, v135, v224\n"
"v_mfma_f32_16x16x32_bf16 v[180:183], a[180:183], v[72:75], v[180:183]\n"
"v_exp_f32 v128, v128\n"
"v_exp_f32 v129, v129\n"
"v_mfma_f32_16x16x32_bf16 v[180:183], a[184:187], v[80:83], v[180:183]\n"
"v_exp_f32 v130, v130\n"
"v_exp_f32 v131, v131\n"
"v_mfma_f32_16x16x32_bf16 v[180:183], a[188:191], v[88:91], v[180:183]\n"
"v_exp_f32 v132, v132\n"
"v_exp_f32 v133, v133\n"
"v_mfma_f32_16x16x32_bf16 v[184:187], a[160:163], v[96:99], 0\n"
"v_exp_f32 v134, v134\n"
"v_exp_f32 v135, v135\n"
"v_sub_f32 v160, v160, v224\n"
"v_mfma_f32_16x16x32_bf16 v[184:187], a[164:167], v[104:107], v[184:187]\n"
"v_sub_f32 v161, v161, v224\n"
"v_sub_f32 v162, v162, v224\n"
"v_mfma_f32_16x16x32_bf16 v[184:187], a[168:171], v[112:115], v[184:187]\n"
"v_sub_f32 v163, v163, v224\n"
"v_sub_f32 v164, v164, v224\n"
"v_mfma_f32_16x16x32_bf16 v[184:187], a[172:175], v[120:123], v[184:187]\n"
"v_sub_f32 v165, v165, v224\n"
"v_sub_f32 v166, v166, v224\n"
"v_mfma_f32_16x16x32_bf16 v[188:191], a[176:179], v[96:99], 0\n"
"v_sub_f32 v167, v167, v224\n"
"v_sub_f32 v192, 0, v224\n"
"v_sub_f32 v193, 0, v224\n"
"v_mfma_f32_16x16x32_bf16 v[188:191], a[180:183], v[104:107], v[188:191]\n"
"v_sub_f32 v194, 0, v224\n"
"v_sub_f32 v195, 0, v224\n"
"v_mfma_f32_16x16x32_bf16 v[188:191], a[184:187], v[112:115], v[188:191]\n"
"v_cvt_pk_bf16_f32 v4, v128, v129\n"
"v_cvt_pk_bf16_f32 v5, v130, v131\n"
"v_mfma_f32_16x16x32_bf16 v[188:191], a[188:191], v[120:123], v[188:191]\n"
"v_cvt_pk_bf16_f32 v6, v132, v133\n"
"v_cvt_pk_bf16_f32 v7, v134, v135\n"
"v_max3_f32 v225, v225, v168, v169\n"
"v_max3_f32 v225, v225, v170, v171\n"
"v_max3_f32 v225, v225, v172, v173\n"
"v_max3_f32 v225, v225, v174, v175\n"
"v_mov_b32 v228, v225\n"
"s_nop 1\n"
"v_permlane32_swap_b32 v225, v228\n"
"v_max_f32 v225, v225, v228\n"
"v_mov_b32 v228, v225\n"
"s_nop 1\n"
"v_permlane16_swap_b32 v225, v228\n"
"v_max_f32 v225, v225, v228\n"
"v_add_f32 v225, s54, v225\n"
"v_sub_f32 v136, v136, v225\n"
"v_sub_f32 v137, v137, v225\n"
"v_sub_f32 v138, v138, v225\n"
"v_sub_f32 v139, v139, v225\n"
"v_sub_f32 v140, v140, v225\n"
"v_sub_f32 v141, v141, v225\n"
"v_sub_f32 v142, v142, v225\n"
"v_sub_f32 v143, v143, v225\n"
"v_exp_f32 v136, v136\n"
"v_exp_f32 v137, v137\n"
"v_exp_f32 v138, v138\n"
"v_exp_f32 v139, v139\n"
"v_exp_f32 v140, v140\n"
"v_exp_f32 v141, v141\n"
"v_exp_f32 v142, v142\n"
"v_exp_f32 v143, v143\n"
"v_sub_f32 v168, v168, v225\n"
"v_sub_f32 v169, v169, v225\n"
"v_sub_f32 v170, v170, v225\n"
"v_sub_f32 v171, v171, v225\n"
"v_sub_f32 v172, v172, v225\n"
"v_sub_f32 v173, v173, v225\n"
"v_sub_f32 v174, v174, v225\n"
"v_sub_f32 v175, v175, v225\n"
"v_sub_f32 v196, 0, v225\n"
"v_sub_f32 v197, 0, v225\n"
"v_sub_f32 v198, 0, v225\n"
"v_sub_f32 v199, 0, v225\n"
"v_cvt_pk_bf16_f32 v12, v136, v137\n"
"v_cvt_pk_bf16_f32 v13, v138, v139\n"
"v_cvt_pk_bf16_f32 v14, v140, v141\n"
"v_cvt_pk_bf16_f32 v15, v142, v143\n"
"v_max3_f32 v226, v226, v176, v177\n"
"v_max3_f32 v226, v226, v178, v179\n"
"v_max3_f32 v226, v226, v180, v181\n"
"v_max3_f32 v226, v226, v182, v183\n"
"v_mov_b32 v228, v226\n"
"s_nop 1\n"
"v_permlane32_swap_b32 v226, v228\n"
"v_max_f32 v226, v226, v228\n"
"v_mov_b32 v228, v226\n"
"s_nop 1\n"
"v_permlane16_swap_b32 v226, v228\n"
"v_max_f32 v226, v226, v228\n"
"v_add_f32 v226, s54, v226\n"
"v_sub_f32 v144, v144, v226\n"
"v_sub_f32 v145, v145, v226\n"
"v_sub_f32 v146, v146, v226\n"
"v_sub_f32 v147, v147, v226\n"
"v_sub_f32 v148, v148, v226\n"
"v_sub_f32 v149, v149, v226\n"
"v_sub_f32 v150, v150, v226\n"
"v_sub_f32 v151, v151, v226\n"
"v_exp_f32 v144, v144\n"
"v_exp_f32 v145, v145\n"
"v_exp_f32 v146, v146\n"
"v_exp_f32 v147, v147\n"
"v_exp_f32 v148, v148\n"
"v_exp_f32 v149, v149\n"
"v_exp_f32 v150, v150\n"
"v_exp_f32 v151, v151\n"
"v_sub_f32 v176, v176, v226\n"
"v_sub_f32 v177, v177, v226\n"
"v_sub_f32 v178, v178, v226\n"
"v_sub_f32 v179, v179, v226\n"
"v_sub_f32 v180, v180, v226\n"
"v_sub_f32 v181, v181, v226\n"
"v_sub_f32 v182, v182, v226\n"
"v_sub_f32 v183, v183, v226\n"
"v_sub_f32 v200, 0, v226\n"
"v_sub_f32 v201, 0, v226\n"
"v_sub_f32 v202, 0, v226\n"
"v_sub_f32 v203, 0, v226\n"
"v_cvt_pk_bf16_f32 v20, v144, v145\n"
"v_cvt_pk_bf16_f32 v21, v146, v147\n"
"v_cvt_pk_bf16_f32 v22, v148, v149\n"
"v_cvt_pk_bf16_f32 v23, v150, v151\n"
"v_max3_f32 v227, v227, v184, v185\n"
"v_max3_f32 v227, v227, v186, v187\n"
"v_max3_f32 v227, v227, v188, v189\n"
"v_max3_f32 v227, v227, v190, v191\n"
"v_mov_b32 v228, v227\n"
"s_nop 1\n"
"v_permlane32_swap_b32 v227, v228\n"
"v_max_f32 v227, v227, v228\n"
"v_mov_b32 v228, v227\n"
"s_nop 1\n"
"v_permlane16_swap_b32 v227, v228\n"
"v_max_f32 v227, v227, v228\n"
"v_add_f32 v227, s54, v227\n"
"v_sub_f32 v152, v152, v227\n"
"v_sub_f32 v153, v153, v227\n"
"v_sub_f32 v154, v154, v227\n"
"v_sub_f32 v155, v155, v227\n"
"v_sub_f32 v156, v156, v227\n"
"v_sub_f32 v157, v157, v227\n"
"v_sub_f32 v158, v158, v227\n"
"v_sub_f32 v159, v159, v227\n"
"v_exp_f32 v152, v152\n"
"v_exp_f32 v153, v153\n"
"v_exp_f32 v154, v154\n"
"v_exp_f32 v155, v155\n"
"v_exp_f32 v156, v156\n"
"v_exp_f32 v157, v157\n"
"v_exp_f32 v158, v158\n"
"v_exp_f32 v159, v159\n"
"v_sub_f32 v184, v184, v227\n"
"v_sub_f32 v185, v185, v227\n"
"v_sub_f32 v186, v186, v227\n"
"v_sub_f32 v187, v187, v227\n"
"v_sub_f32 v188, v188, v227\n"
"v_sub_f32 v189, v189, v227\n"
"v_sub_f32 v190, v190, v227\n"
"v_sub_f32 v191, v191, v227\n"
"v_sub_f32 v204, 0, v227\n"
"v_sub_f32 v205, 0, v227\n"
"v_sub_f32 v206, 0, v227\n"
"v_sub_f32 v207, 0, v227\n"
"v_cvt_pk_bf16_f32 v28, v152, v153\n"
"v_cvt_pk_bf16_f32 v29, v154, v155\n"
"v_cvt_pk_bf16_f32 v30, v156, v157\n"
"v_cvt_pk_bf16_f32 v31, v158, v159\n"
"s_nop 1\n"
"v_mfma_f32_16x16x32_bf16 v[128:131], v[116:119], v[4:7], 0\n"
"v_mfma_f32_16x16x32_bf16 v[136:139], v[116:119], v[12:15], 0\n"
"v_mfma_f32_16x16x32_bf16 v[144:147], v[116:119], v[20:23], 0\n"
"v_mfma_f32_16x16x32_bf16 v[152:155], v[116:119], v[28:31], 0\n"
"s_waitcnt lgkmcnt(8)\n"
"v_mfma_f32_16x16x32_bf16 a[0:3], a[192:195], v[4:7], 0\n"
"v_exp_f32 v68, v160\n"
"v_mfma_f32_16x16x32_bf16 a[4:7], a[196:199], v[4:7], 0\n"
"ds_read_b128 a[128:131], v232 offset:16384\n"
"ds_read_b128 a[132:135], v232 offset:16896\n"
"v_mfma_f32_16x16x32_bf16 a[8:11], a[200:203], v[4:7], 0\n"
"v_exp_f32 v69, v161\n"
"v_mfma_f32_16x16x32_bf16 a[12:15], a[204:207], v[4:7], 0\n"
"v_cvt_pk_bf16_f32 v36, v68, v69\n"
"s_waitcnt lgkmcnt(2)\n"
"v_mfma_f32_16x16x32_bf16 a[16:19], a[208:211], v[4:7], 0\n"
"v_exp_f32 v70, v162\n"
"v_mfma_f32_16x16x32_bf16 a[20:23], a[212:215], v[4:7], 0\n"
"ds_read_b128 a[136:139], v232 offset:17408\n"
"ds_read_b128 a[140:143], v232 offset:17920\n"
"v_mfma_f32_16x16x32_bf16 a[24:27], a[216:219], v[4:7], 0\n"
"v_exp_f32 v71, v163\n"
"v_mfma_f32_16x16x32_bf16 a[28:31], a[220:223], v[4:7], 0\n"
"ds_read_b128 a[144:147], v232 offset:20480\n"
"v_cvt_pk_bf16_f32 v37, v70, v71\n"
"v_mfma_f32_16x16x32_bf16 a[32:35], a[192:195], v[12:15], 0\n"
"v_exp_f32 v76, v164\n"
"v_mfma_f32_16x16x32_bf16 a[36:39], a[196:199], v[12:15], 0\n"
"v_mov_b32 v212, v128\n"
"v_mfma_f32_16x16x32_bf16 a[40:43], a[200:203], v[12:15], 0\n"
"v_exp_f32 v77, v165\n"
"v_mfma_f32_16x16x32_bf16 a[44:47], a[204:207], v[12:15], 0\n"
"v_cvt_pk_bf16_f32 v38, v76, v77\n"
"v_mfma_f32_16x16x32_bf16 a[48:51], a[208:211], v[12:15], 0\n"
"v_exp_f32 v78, v166\n"
"v_mfma_f32_16x16x32_bf16 a[52:55], a[212:215], v[12:15], 0\n"
"ds_read_b128 a[148:151], v232 offset:20992\n"
"v_mov_b32 v213, v136\n"
"v_mfma_f32_16x16x32_bf16 a[56:59], a[216:219], v[12:15], 0\n"
"v_exp_f32 v79, v167\n"
"v_mfma_f32_16x16x32_bf16 a[60:63], a[220:223], v[12:15], 0\n"
"ds_read_b128 a[152:155], v232 offset:21504\n"
"v_cvt_pk_bf16_f32 v39, v78, v79\n"
"v_mfma_f32_16x16x32_bf16 a[64:67], a[192:195], v[20:23], 0\n"
"v_exp_f32 v68, v168\n"
"v_mfma_f32_16x16x32_bf16 a[68:71], a[196:199], v[20:23], 0\n"
"ds_read_b128 a[156:159], v232 offset:22016\n"
"v_mov_b32 v214, v144\n"
"v_mfma_f32_16x16x32_bf16 a[72:75], a[200:203], v[20:23], 0\n"
"v_exp_f32 v69, v169\n"
"v_mfma_f32_16x16x32_bf16 a[76:79], a[204:207], v[20:23], 0\n"
"s_add_u32 m0, s50, 0\n"
"v_cvt_pk_bf16_f32 v44, v68, v69\n"
"v_mfma_f32_16x16x32_bf16 a[80:83], a[208:211], v[20:23], 0\n"
"v_exp_f32 v70, v170\n"
"v_mfma_f32_16x16x32_bf16 a[84:87], a[212:215], v[20:23], 0\n"
"buffer_load_dwordx4 v236, s[20:23], s47 offen offset:0 lds\n"
"v_mov_b32 v215, v152\n"
"v_mfma_f32_16x16x32_bf16 a[88:91], a[216:219], v[20:23], 0\n"
"v_exp_f32 v71, v171\n"
"v_mfma_f32_16x16x32_bf16 a[92:95], a[220:223], v[20:23], 0\n"
"buffer_load_dwordx4 v237, s[20:23], s47 offen offset:1024 lds\n"
"v_cvt_pk_bf16_f32 v45, v70, v71\n"
"v_mfma_f32_16x16x32_bf16 a[96:99], a[192:195], v[28:31], 0\n"
"v_exp_f32 v76, v172\n"
"v_mfma_f32_16x16x32_bf16 a[100:103], a[196:199], v[28:31], 0\n"
"buffer_load_dwordx4 v238, s[20:23], s47 offen offset:2048 lds\n"
"v_mfma_f32_16x16x32_bf16 a[104:107], a[200:203], v[28:31], 0\n"
"v_exp_f32 v77, v173\n"
"v_mfma_f32_16x16x32_bf16 a[108:111], a[204:207], v[28:31], 0\n"
"buffer_load_dwordx4 v239, s[20:23], s47 offen offset:3072 lds\n"
"v_cvt_pk_bf16_f32 v46, v76, v77\n"
"v_mfma_f32_16x16x32_bf16 a[112:115], a[208:211], v[28:31], 0\n"
"v_exp_f32 v78, v174\n"
"v_mfma_f32_16x16x32_bf16 a[116:119], a[212:215], v[28:31], 0\n"
"s_add_u32 s47, s47, s55\n"
"v_mfma_f32_16x16x32_bf16 a[120:123], a[216:219], v[28:31], 0\n"
"v_exp_f32 v79, v175\n"
"v_mfma_f32_16x16x32_bf16 a[124:127], a[220:223], v[28:31], 0\n"
"v_cvt_pk_bf16_f32 v47, v78, v79\n"
"L_loop_%=:\n"
"s_waitcnt lgkmcnt(0)\n"
"v_mfma_f32_16x16x32_bf16 v[128:131], a[128:131], v[0:3], v[192:195]\n"
"v_exp_f32 v68, v176\n"
"v_mfma_f32_16x16x32_bf16 v[100:103], v[116:119], v[36:39], 0\n"
"ds_read_b64_tr_b16 a[224:225], v233 offset:8192\n"
"ds_read_b64_tr_b16 a[226:227], v233 offset:12288\n"
"v_mfma_f32_16x16x32_bf16 v[128:131], a[132:135], v[8:11], v[128:131]\n"
"v_exp_f32 v69, v177\n"
"v_mfma_f32_16x16x32_bf16 v[108:111], v[116:119], v[44:47], 0\n"
"v_cvt_pk_bf16_f32 v52, v68, v69\n"
"v_mfma_f32_16x16x32_bf16 v[128:131], a[136:139], v[16:19], v[128:131]\n"
"v_exp_f32 v70, v178\n"
"v_mfma_f32_16x16x32_bf16 v[128:131], a[140:143], v[24:27], v[128:131]\n"
"ds_read_b64_tr_b16 a[228:229], v234 offset:8192\n"
"ds_read_b64_tr_b16 a[230:231], v234 offset:12288\n"
"v_mfma_f32_16x16x32_bf16 v[132:135], a[144:147], v[0:3], v[192:195]\n"
"v_exp_f32 v71, v179\n"
"v_mfma_f32_16x16x32_bf16 v[132:135], a[148:151], v[8:11], v[132:135]\n"
"ds_read_b64_tr_b16 a[232:233], v233 offset:8704\n"
"v_cvt_pk_bf16_f32 v53, v70, v71\n"
"v_mfma_f32_16x16x32_bf16 v[132:135], a[152:155], v[16:19], v[132:135]\n"
"v_exp_f32 v76, v180\n"
"v_mfma_f32_16x16x32_bf16 v[132:135], a[156:159], v[24:27], v[132:135]\n"
"v_max_f32 v216, v100, v108\n"
"v_cmp_lt_f32 vcc, s53, v216\n"
"v_mfma_f32_16x16x32_bf16 v[136:139], a[128:131], v[32:35], v[196:199]\n"
"v_exp_f32 v77, v181\n"
"v_mfma_f32_16x16x32_bf16 v[136:139], a[132:135], v[40:43], v[136:139]\n"
"s_cbranch_vccnz L_rare0_%=\n"
"L_ret0_%=:\n"
"v_cvt_pk_bf16_f32 v54, v76, v77\n"
"v_mfma_f32_16x16x32_bf16 v[136:139], a[136:139], v[48:51], v[136:139]\n"
"v_exp_f32 v78, v182\n"
"v_mfma_f32_16x16x32_bf16 v[136:139], a[140:143], v[56:59], v[136:139]\n"
"ds_read_b64_tr_b16 a[234:235], v233 offset:12800\n"
"v_add_f32 v212, v212, v100\n"
"v_mfma_f32_16x16x32_bf16 v[140:143], a[144:147], v[32:35], v[196:199]\n"
"v_exp_f32 v79, v183\n"
"v_mfma_f32_16x16x32_bf16 v[140:143], a[148:151], v[40:43], v[140:143]\n"
"ds_read_b64_tr_b16 a[236:237], v234 offset:8704\n"
"v_cvt_pk_bf16_f32 v55, v78, v79\n"
"v_mfma_f32_16x16x32_bf16 v[140:143], a[152:155], v[48:51], v[140:143]\n"
"v_exp_f32 v68, v184\n"
"v_mfma_f32_16x16x32_bf16 v[140:143], a[156:159], v[56:59], v[140:143]\n"
"ds_read_b64_tr_b16 a[238:239], v234 offset:12800\n"
"v_add_f32 v213, v213, v108\n"
"v_mfma_f32_16x16x32_bf16 v[144:147], a[128:131], v[64:67], v[200:203]\n"
"v_exp_f32 v69, v185\n"
"v_mfma_f32_16x16x32_bf16 v[144:147], a[132:135], v[72:75], v[144:147]\n"
"ds_read_b64_tr_b16 a[240:241], v233 offset:9216\n"
"v_cvt_pk_bf16_f32 v60, v68, v69\n"
"v_mfma_f32_16x16x32_bf16 v[144:147], a[136:139], v[80:83], v[144:147]\n"
"v_exp_f32 v70, v186\n"
"v_mfma_f32_16x16x32_bf16 v[144:147], a[140:143], v[88:91], v[144:147]\n"
"ds_read_b64_tr_b16 a[242:243], v233 offset:13312\n"
"ds_read_b64_tr_b16 a[244:245], v234 offset:9216\n"
"v_mfma_f32_16x16x32_bf16 v[148:151], a[144:147], v[64:67], v[200:203]\n"
"v_exp_f32 v71, v187\n"
"v_mfma_f32_16x16x32_bf16 v[148:151], a[148:151], v[72:75], v[148:151]\n"
"ds_read_b64_tr_b16 a[246:247], v234 offset:13312\n"
"v_cvt_pk_bf16_f32 v61, v70, v71\n"
"v_mfma_f32_16x16x32_bf16 v[148:151], a[152:155], v[80:83], v[148:151]\n"
"v_exp_f32 v76, v188\n"
"v_mfma_f32_16x16x32_bf16 v[148:151], a[156:159], v[88:91], v[148:151]\n"
"ds_read_b64_tr_b16 a[248:249], v233 offset:9728\n"
"ds_read_b64_tr_b16 a[250:251], v233 offset:13824\n"
"v_mfma_f32_16x16x32_bf16 v[152:155], a[128:131], v[96:99], v[204:207]\n"
"v_exp_f32 v77, v189\n"
"v_mfma_f32_16x16x32_bf16 v[152:155], a[132:135], v[104:107], v[152:155]\n"
"v_cvt_pk_bf16_f32 v62, v76, v77\n"
"v_mfma_f32_16x16x32_bf16 v[152:155], a[136:139], v[112:115], v[152:155]\n"
"v_exp_f32 v78, v190\n"
"v_mfma_f32_16x16x32_bf16 v[152:155], a[140:143], v[120:123], v[152:155]\n"
"ds_read_b64_tr_b16 a[252:253], v234 offset:9728\n"
"ds_read_b64_tr_b16 a[254:255], v234 offset:13824\n"
"v_mfma_f32_16x16x32_bf16 v[156:159], a[144:147], v[96:99], v[204:207]\n"
"v_exp_f32 v79, v191\n"
"v_mfma_f32_16x16x32_bf16 v[156:159], a[148:151], v[104:107], v[156:159]\n"
"v_cvt_pk_bf16_f32 v63, v78, v79\n"
"v_mfma_f32_16x16x32_bf16 v[156:159], a[152:155], v[112:115], v[156:159]\n"
"v_mfma_f32_16x16x32_bf16 v[156:159], a[156:159], v[120:123], v[156:159]\n"
"s_waitcnt lgkmcnt(8)\n"
"v_mfma_f32_16x16x32_bf16 a[0:3], a[224:227], v[36:39], a[0:3]\n"
"v_exp_f32 v68, v128\n"
"v_mfma_f32_16x16x32_bf16 v[100:103], v[116:119], v[52:55], 0\n"
"ds_read_b128 a[160:163], v232 offset:24576\n"
"ds_read_b128 a[164:167], v232 offset:25088\n"
"v_mfma_f32_16x16x32_bf16 a[4:7], a[228:231], v[36:39], a[4:7]\n"
"v_exp_f32 v69, v129\n"
"v_mfma_f32_16x16x32_bf16 v[108:111], v[116:119], v[60:63], 0\n"
"s_waitcnt lgkmcnt(2)\n"
"v_cvt_pk_bf16_f32 v4, v68, v69\n"
"v_mfma_f32_16x16x32_bf16 a[8:11], a[232:235], v[36:39], a[8:11]\n"
"v_exp_f32 v70, v130\n"
"v_mfma_f32_16x16x32_bf16 a[12:15], a[236:239], v[36:39], a[12:15]\n"
"ds_read_b128 a[168:171], v232 offset:25600\n"
"ds_read_b128 a[172:175], v232 offset:26112\n"
"v_mfma_f32_16x16x32_bf16 a[16:19], a[240:243], v[36:39], a[16:19]\n"
"v_exp_f32 v71, v131\n"
"v_mfma_f32_16x16x32_bf16 a[20:23], a[244:247], v[36:39], a[20:23]\n"
"ds_read_b128 a[176:179], v232 offset:28672\n"
"v_cvt_pk_bf16_f32 v5, v70, v71\n"
"v_mfma_f32_16x16x32_bf16 a[24:27], a[248:251], v[36:39], a[24:27]\n"
"v_exp_f32 v76, v132\n"
"v_mfma_f32_16x16x32_bf16 a[28:31], a[252:255], v[36:39], a[28:31]\n"
"v_max_f32 v216, v100, v108\n"
"v_cmp_lt_f32 vcc, s53, v216\n"
"v_mfma_f32_16x16x32_bf16 a[32:35], a[224:227], v[44:47], a[32:35]\n"
"v_exp_f32 v77, v133\n"
"v_mfma_f32_16x16x32_bf16 a[36:39], a[228:231], v[44:47], a[36:39]\n"
"s_cbranch_vccnz L_rare1_%=\n"
"L_ret1_%=:\n"
"v_cvt_pk_bf16_f32 v6, v76, v77\n"
"v_mfma_f32_16x16x32_bf16 a[40:43], a[232:235], v[44:47], a[40:43]\n"
"v_exp_f32 v78, v134\n"
"v_mfma_f32_16x16x32_bf16 a[44:47], a[236:239], v[44:47], a[44:47]\n"
"ds_read_b128 a[180:183], v232 offset:29184\n"
"v_add_f32 v214, v214, v100\n"
"v_mfma_f32_16x16x32_bf16 a[48:51], a[240:243], v[44:47], a[48:51]\n"
"v_exp_f32 v79, v135\n"
"v_mfma_f32_16x16x32_bf16 a[52:55], a[244:247], v[44:47], a[52:55]\n"
"ds_read_b128 a[184:187], v232 offset:29696\n"
"v_cvt_pk_bf16_f32 v7, v78, v79\n"
"v_mfma_f32_16x16x32_bf16 a[56:59], a[248:251], v[44:47], a[56:59]\n"
"v_exp_f32 v68, v136\n"
"v_mfma_f32_16x16x32_bf16 a[60:63], a[252:255], v[44:47], a[60:63]\n"
"ds_read_b128 a[188:191], v232 offset:30208\n"
"v_add_f32 v215, v215, v108\n"
"v_mfma_f32_16x16x32_bf16 a[64:67], a[224:227], v[52:55], a[64:67]\n"
"v_exp_f32 v69, v137\n"
"v_mfma_f32_16x16x32_bf16 a[68:71], a[228:231], v[52:55], a[68:71]\n"
"s_add_u32 m0, s51, 0xc000\n"
"v_cvt_pk_bf16_f32 v12, v68, v69\n"
"v_mfma_f32_16x16x32_bf16 a[72:75], a[232:235], v[52:55], a[72:75]\n"
"v_exp_f32 v70, v138\n"
"v_mfma_f32_16x16x32_bf16 a[76:79], a[236:239], v[52:55], a[76:79]\n"
"buffer_load_dwordx4 v240, s[24:27], s48 offen offset:0 lds\n"
"v_mfma_f32_16x16x32_bf16 a[80:83], a[240:243], v[52:55], a[80:83]\n"
"v_exp_f32 v71, v139\n"
"v_mfma_f32_16x16x32_bf16 a[84:87], a[244:247], v[52:55], a[84:87]\n"
"buffer_load_dwordx4 v241, s[24:27], s48 offen offset:1024 lds\n"
"v_cvt_pk_bf16_f32 v13, v70, v71\n"
"v_mfma_f32_16x16x32_bf16 a[88:91], a[248:251], v[52:55], a[88:91]\n"
"v_exp_f32 v76, v140\n"
"v_mfma_f32_16x16x32_bf16 a[92:95], a[252:255], v[52:55], a[92:95]\n"
"buffer_load_dwordx4 v217, s[24:27], s48 offen offset:2048 lds\n"
"v_mfma_f32_16x16x32_bf16 a[96:99], a[224:227], v[60:63], a[96:99]\n"
"v_exp_f32 v77, v141\n"
"v_mfma_f32_16x16x32_bf16 a[100:103], a[228:231], v[60:63], a[100:103]\n"
"buffer_load_dwordx4 v218, s[24:27], s48 offen offset:3072 lds\n"
"v_cvt_pk_bf16_f32 v14, v76, v77\n"
"v_mfma_f32_16x16x32_bf16 a[104:107], a[232:235], v[60:63], a[104:107]\n"
"v_exp_f32 v78, v142\n"
"v_mfma_f32_16x16x32_bf16 a[108:111], a[236:239], v[60:63], a[108:111]\n"
"s_add_u32 s48, s48, s61\n"
"v_mfma_f32_16x16x32_bf16 a[112:115], a[240:243], v[60:63], a[112:115]\n"
"v_exp_f32 v79, v143\n"
"v_mfma_f32_16x16x32_bf16 a[116:119], a[244:247], v[60:63], a[116:119]\n"
"v_cvt_pk_bf16_f32 v15, v78, v79\n"
"v_mfma_f32_16x16x32_bf16 a[120:123], a[248:251], v[60:63], a[120:123]\n"
"v_mfma_f32_16x16x32_bf16 a[124:127], a[252:255], v[60:63], a[124:127]\n"
"s_waitcnt vmcnt(16) lgkmcnt(0)\n"
"s_barrier\n"
"v_mfma_f32_16x16x32_bf16 v[160:163], a[160:163], v[0:3], v[192:195]\n"
"v_exp_f32 v68, v144\n"
"v_mfma_f32_16x16x32_bf16 v[100:103], v[116:119], v[4:7], 0\n"
"ds_read_b64_tr_b16 a[192:193], v233 offset:16384\n"
"ds_read_b64_tr_b16 a[194:195], v233 offset:20480\n"
"v_mfma_f32_16x16x32_bf16 v[160:163], a[164:167], v[8:11], v[160:163]\n"
"v_exp_f32 v69, v145\n"
"v_mfma_f32_16x16x32_bf16 v[108:111], v[116:119], v[12:15], 0\n"
"v_cvt_pk_bf16_f32 v20, v68, v69\n"
"v_mfma_f32_16x16x32_bf16 v[160:163], a[168:171], v[16:19], v[160:163]\n"
"v_exp_f32 v70, v146\n"
"v_mfma_f32_16x16x32_bf16 v[160:163], a[172:175], v[24:27], v[160:163]\n"
"ds_read_b64_tr_b16 a[196:197], v234 offset:16384\n"
"ds_read_b64_tr_b16 a[198:199], v234 offset:20480\n"
"v_mfma_f32_16x16x32_bf16 v[164:167], a[176:179], v[0:3], v[192:195]\n"
"v_exp_f32 v71, v147\n"
"v_mfma_f32_16x16x32_bf16 v[164:167], a[180:183], v[8:11], v[164:167]\n"
"ds_read_b64_tr_b16 a[200:201], v233 offset:16896\n"
"v_cvt_pk_bf16_f32 v21, v70, v71\n"
"v_mfma_f32_16x16x32_bf16 v[164:167], a[184:187], v[16:19], v[164:167]\n"
"v_exp_f32 v76, v148\n"
"v_mfma_f32_16x16x32_bf16 v[164:167], a[188:191], v[24:27], v[164:167]\n"
"v_max_f32 v216, v100, v108\n"
"v_cmp_lt_f32 vcc, s53, v216\n"
"v_mfma_f32_16x16x32_bf16 v[168:171], a[160:163], v[32:35], v[196:199]\n"
"v_exp_f32 v77, v149\n"
"v_mfma_f32_16x16x32_bf16 v[168:171], a[164:167], v[40:43], v[168:171]\n"
"s_cbranch_vccnz L_rare2_%=\n"
"L_ret2_%=:\n"
"v_cvt_pk_bf16_f32 v22, v76, v77\n"
"v_mfma_f32_16x16x32_bf16 v[168:171], a[168:171], v[48:51], v[168:171]\n"
"v_exp_f32 v78, v150\n"
"v_mfma_f32_16x16x32_bf16 v[168:171], a[172:175], v[56:59], v[168:171]\n"
"ds_read_b64_tr_b16 a[202:203], v233 offset:20992\n"
"v_add_f32 v212, v212, v100\n"
"v_mfma_f32_16x16x32_bf16 v[172:175], a[176:179], v[32:35], v[196:199]\n"
"v_exp_f32 v79, v151\n"
"v_mfma_f32_16x16x32_bf16 v[172:175], a[180:183], v[40:43], v[172:175]\n"
"ds_read_b64_tr_b16 a[204:205], v234 offset:16896\n"
"v_cvt_pk_bf16_f32 v23, v78, v79\n"
"v_mfma_f32_16x16x32_bf16 v[172:175], a[184:187], v[48:51], v[172:175]\n"
"v_exp_f32 v68, v152\n"
"v_mfma_f32_16x16x32_bf16 v[172:175], a[188:191], v[56:59], v[172:175]\n"
"ds_read_b64_tr_b16 a[206:207], v234 offset:20992\n"
"v_add_f32 v213, v213, v108\n"
"v_mfma_f32_16x16x32_bf16 v[176:179], a[160:163], v[64:67], v[200:203]\n"
"v_exp_f32 v69, v153\n"
"v_mfma_f32_16x16x32_bf16 v[176:179], a[164:167], v[72:75], v[176:179]\n"
"ds_read_b64_tr_b16 a[208:209], v233 offset:17408\n"
"v_cvt_pk_bf16_f32 v28, v68, v69\n"
"v_mfma_f32_16x16x32_bf16 v[176:179], a[168:171], v[80:83], v[176:179]\n"
"v_exp_f32 v70, v154\n"
"v_mfma_f32_16x16x32_bf16 v[176:179], a[172:175], v[88:91], v[176:179]\n"
"ds_read_b64_tr_b16 a[210:211], v233 offset:21504\n"
"ds_read_b64_tr_b16 a[212:213], v234 offset:17408\n"
"v_mfma_f32_16x16x32_bf16 v[180:183], a[176:179], v[64:67], v[200:203]\n"
"v_exp_f32 v71, v155\n"
"v_mfma_f32_16x16x32_bf16 v[180:183], a[180:183], v[72:75], v[180:183]\n"
"ds_read_b64_tr_b16 a[214:215], v234 offset:21504\n"
"v_cvt_pk_bf16_f32 v29, v70, v71\n"
"v_mfma_f32_16x16x32_bf16 v[180:183], a[184:187], v[80:83], v[180:183]\n"
"v_exp_f32 v76, v156\n"
"v_mfma_f32_16x16x32_bf16 v[180:183], a[188:191], v[88:91], v[180:183]\n"
"ds_read_b64_tr_b16 a[216:217], v233 offset:17920\n"
"ds_read_b64_tr_b16 a[218:219], v233 offset:22016\n"
"v_mfma_f32_16x16x32_bf16 v[184:187], a[160:163], v[96:99], v[204:207]\n"
"v_exp_f32 v77, v157\n"
"v_mfma_f32_16x16x32_bf16 v[184:187], a[164:167], v[104:107], v[184:187]\n"
"v_cvt_pk_bf16_f32 v30, v76, v77\n"
"v_mfma_f32_16x16x32_bf16 v[184:187], a[168:171], v[112:115], v[184:187]\n"
"v_exp_f32 v78, v158\n"
"v_mfma_f32_16x16x32_bf16 v[184:187], a[172:175], v[120:123], v[184:187]\n"
"ds_read_b64_tr_b16 a[220:221], v234 offset:17920\n"
"ds_read_b64_tr_b16 a[222:223], v234 offset:22016\n"
"v_mfma_f32_16x16x32_bf16 v[188:191], a[176:179], v[96:99], v[204:207]\n"
"v_exp_f32 v79, v159\n"
"v_mfma_f32_16x16x32_bf16 v[188:191], a[180:183], v[104:107], v[188:191]\n"
"v_cvt_pk_bf16_f32 v31, v78, v79\n"
"v_mfma_f32_16x16x32_bf16 v[188:191], a[184:187], v[112:115], v[188:191]\n"
"v_mfma_f32_16x16x32_bf16 v[188:191], a[188:191], v[120:123], v[188:191]\n"
"s_waitcnt lgkmcnt(8)\n"
"v_mfma_f32_16x16x32_bf16 a[0:3], a[192:195], v[4:7], a[0:3]\n"
"v_exp_f32 v68, v160\n"
"v_mfma_f32_16x16x32_bf16 v[100:103], v[116:119], v[20:23], 0\n"
"ds_read_b128 a[128:131], v232 offset:32768\n"
"ds_read_b128 a[132:135], v232 offset:33280\n"
"v_mfma_f32_16x16x32_bf16 a[4:7], a[196:199], v[4:7], a[4:7]\n"
"v_exp_f32 v69, v161\n"
"v_mfma_f32_16x16x32_bf16 v[108:111], v[116:119], v[28:31], 0\n"
"s_waitcnt lgkmcnt(2)\n"
"v_cvt_pk_bf16_f32 v36, v68, v69\n"
"v_mfma_f32_16x16x32_bf16 a[8:11], a[200:203], v[4:7], a[8:11]\n"
"v_exp_f32 v70, v162\n"
"v_mfma_f32_16x16x32_bf16 a[12:15], a[204:207], v[4:7], a[12:15]\n"
"ds_read_b128 a[136:139], v232 offset:33792\n"
"ds_read_b128 a[140:143], v232 offset:34304\n"
"v_mfma_f32_16x16x32_bf16 a[16:19], a[208:211], v[4:7], a[16:19]\n"
"v_exp_f32 v71, v163\n"
"v_mfma_f32_16x16x32_bf16 a[20:23], a[212:215], v[4:7], a[20:23]\n"
"ds_read_b128 a[144:147], v232 offset:36864\n"
"v_cvt_pk_bf16_f32 v37, v70, v71\n"
"v_mfma_f32_16x16x32_bf16 a[24:27], a[216:219], v[4:7], a[24:27]\n"
"v_exp_f32 v76, v164\n"
"v_mfma_f32_16x16x32_bf16 a[28:31], a[220:223], v[4:7], a[28:31]\n"
"v_max_f32 v216, v100, v108\n"
"v_cmp_lt_f32 vcc, s53, v216\n"
"v_mfma_f32_16x16x32_bf16 a[32:35], a[192:195], v[12:15], a[32:35]\n"
"v_exp_f32 v77, v165\n"
"v_mfma_f32_16x16x32_bf16 a[36:39], a[196:199], v[12:15], a[36:39]\n"
"s_cbranch_vccnz L_rare3_%=\n"
"L_ret3_%=:\n"
"v_cvt_pk_bf16_f32 v38, v76, v77\n"
"v_mfma_f32_16x16x32_bf16 a[40:43], a[200:203], v[12:15], a[40:43]\n"
"v_exp_f32 v78, v166\n"
"v_mfma_f32_16x16x32_bf16 a[44:47], a[204:207], v[12:15], a[44:47]\n"
"ds_read_b128 a[148:151], v232 offset:37376\n"
"v_add_f32 v214, v214, v100\n"
"v_mfma_f32_16x16x32_bf16 a[48:51], a[208:211], v[12:15], a[48:51]\n"
"v_exp_f32 v79, v167\n"
"v_mfma_f32_16x16x32_bf16 a[52:55], a[212:215], v[12:15], a[52:55]\n"
"ds_read_b128 a[152:155], v232 offset:37888\n"
"v_cvt_pk_bf16_f32 v39, v78, v79\n"
"v_mfma_f32_16x16x32_bf16 a[56:59], a[216:219], v[12:15], a[56:59]\n"
"v_exp_f32 v68, v168\n"
"v_mfma_f32_16x16x32_bf16 a[60:63], a[220:223], v[12:15], a[60:63]\n"
"ds_read_b128 a[156:159], v232 offset:38400\n"
"v_add_f32 v215, v215, v108\n"
"v_mfma_f32_16x16x32_bf16 a[64:67], a[192:195], v[20:23], a[64:67]\n"
"v_exp_f32 v69, v169\n"
"v_mfma_f32_16x16x32_bf16 a[68:71], a[196:199], v[20:23], a[68:71]\n"
"s_add_u32 m0, s50, 0x4000\n"
"v_cvt_pk_bf16_f32 v44, v68, v69\n"
"v_mfma_f32_16x16x32_bf16 a[72:75], a[200:203], v[20:23], a[72:75]\n"
"v_exp_f32 v70, v170\n"
"v_mfma_f32_16x16x32_bf16 a[76:79], a[204:207], v[20:23], a[76:79]\n"
"buffer_load_dwordx4 v236, s[20:23], s47 offen offset:0 lds\n"
"v_mfma_f32_16x16x32_bf16 a[80:83], a[208:211], v[20:23], a[80:83]\n"
"v_exp_f32 v71, v171\n"
"v_mfma_f32_16x16x32_bf16 a[84:87], a[212:215], v[20:23], a[84:87]\n"
"buffer_load_dwordx4 v237, s[20:23], s47 offen offset:1024 lds\n"
"v_cvt_pk_bf16_f32 v45, v70, v71\n"
"v_mfma_f32_16x16x32_bf16 a[88:91], a[216:219], v[20:23], a[88:91]\n"
"v_exp_f32 v76, v172\n"
"v_mfma_f32_16x16x32_bf16 a[92:95], a[220:223], v[20:23], a[92:95]\n"
"buffer_load_dwordx4 v238, s[20:23], s47 offen offset:2048 lds\n"
"v_mfma_f32_16x16x32_bf16 a[96:99], a[192:195], v[28:31], a[96:99]\n"
"v_exp_f32 v77, v173\n"
"v_mfma_f32_16x16x32_bf16 a[100:103], a[196:199], v[28:31], a[100:103]\n"
"buffer_load_dwordx4 v239, s[20:23], s47 offen offset:3072 lds\n"
"v_cvt_pk_bf16_f32 v46, v76, v77\n"
"v_mfma_f32_16x16x32_bf16 a[104:107], a[200:203], v[28:31], a[104:107]\n"
"v_exp_f32 v78, v174\n"
"v_mfma_f32_16x16x32_bf16 a[108:111], a[204:207], v[28:31], a[108:111]\n"
"s_add_u32 s47, s47, s55\n"
"v_mfma_f32_16x16x32_bf16 a[112:115], a[208:211], v[28:31], a[112:115]\n"
"v_exp_f32 v79, v175\n"
"v_mfma_f32_16x16x32_bf16 a[116:119], a[212:215], v[28:31], a[116:119]\n"
"v_cvt_pk_bf16_f32 v47, v78, v79\n"
"v_mfma_f32_16x16x32_bf16 a[120:123], a[216:219], v[28:31], a[120:123]\n"
"v_mfma_f32_16x16x32_bf16 a[124:127], a[220:223], v[28:31], a[124:127]\n"
"s_waitcnt lgkmcnt(0)\n"
"v_mfma_f32_16x16x32_bf16 v[128:131], a[128:131], v[0:3], v[192:195]\n"
"v_exp_f32 v68, v176\n"
"v_mfma_f32_16x16x32_bf16 v[100:103], v[116:119], v[36:39], 0\n"
"ds_read_b64_tr_b16 a[224:225], v233 offset:24576\n"
"ds_read_b64_tr_b16 a[226:227], v233 offset:28672\n"
"v_mfma_f32_16x16x32_bf16 v[128:131], a[132:135], v[8:11], v[128:131]\n"
"v_exp_f32 v69, v177\n"
"v_mfma_f32_16x16x32_bf16 v[108:111], v[116:119], v[44:47], 0\n"
"v_cvt_pk_bf16_f32 v52, v68, v69\n"
"v_mfma_f32_16x16x32_bf16 v[128:131], a[136:139], v[16:19], v[128:131]\n"
"v_exp_f32 v70, v178\n"
"v_mfma_f32_16x16x32_bf16 v[128:131], a[140:143], v[24:27], v[128:131]\n"
"ds_read_b64_tr_b16 a[228:229], v234 offset:24576\n"
"ds_read_b64_tr_b16 a[230:231], v234 offset:28672\n"
"v_mfma_f32_16x16x32_bf16 v[132:135], a[144:147], v[0:3], v[192:195]\n"
"v_exp_f32 v71, v179\n"
"v_mfma_f32_16x16x32_bf16 v[132:135], a[148:151], v[8:11], v[132:135]\n"
"ds_read_b64_tr_b16 a[232:233], v233 offset:25088\n"
"v_cvt_pk_bf16_f32 v53, v70, v71\n"
"v_mfma_f32_16x16x32_bf16 v[132:135], a[152:155], v[16:19], v[132:135]\n"
"v_exp_f32 v76, v180\n"
"v_mfma_f32_16x16x32_bf16 v[132:135], a[156:159], v[24:27], v[132:135]\n"
"v_max_f32 v216, v100, v108\n"
"v_cmp_lt_f32 vcc, s53, v216\n"
"v_mfma_f32_16x16x32_bf16 v[136:139], a[128:131], v[32:35], v[196:199]\n"
"v_exp_f32 v77, v181\n"
"v_mfma_f32_16x16x32_bf16 v[136:139], a[132:135], v[40:43], v[136:139]\n"
"s_cbranch_vccnz L_rare4_%=\n"
"L_ret4_%=:\n"
"v_cvt_pk_bf16_f32 v54, v76, v77\n"
"v_mfma_f32_16x16x32_bf16 v[136:139], a[136:139], v[48:51], v[136:139]\n"
"v_exp_f32 v78, v182\n"
"v_mfma_f32_16x16x32_bf16 v[136:139], a[140:143], v[56:59], v[136:139]\n"
"ds_read_b64_tr_b16 a[234:235], v233 offset:29184\n"
"v_add_f32 v212, v212, v100\n"
"v_mfma_f32_16x16x32_bf16 v[140:143], a[144:147], v[32:35], v[196:199]\n"
"v_exp_f32 v79, v183\n"
"v_mfma_f32_16x16x32_bf16 v[140:143], a[148:151], v[40:43], v[140:143]\n"
"ds_read_b64_tr_b16 a[236:237], v234 offset:25088\n"
"v_cvt_pk_bf16_f32 v55, v78, v79\n"
"v_mfma_f32_16x16x32_bf16 v[140:143], a[152:155], v[48:51], v[140:143]\n"
"v_exp_f32 v68, v184\n"
"v_mfma_f32_16x16x32_bf16 v[140:143], a[156:159], v[56:59], v[140:143]\n"
"ds_read_b64_tr_b16 a[238:239], v234 offset:29184\n"
"v_add_f32 v213, v213, v108\n"
"v_mfma_f32_16x16x32_bf16 v[144:147], a[128:131], v[64:67], v[200:203]\n"
"v_exp_f32 v69, v185\n"
"v_mfma_f32_16x16x32_bf16 v[144:147], a[132:135], v[72:75], v[144:147]\n"
"ds_read_b64_tr_b16 a[240:241], v233 offset:25600\n"
"v_cvt_pk_bf16_f32 v60, v68, v69\n"
"v_mfma_f32_16x16x32_bf16 v[144:147], a[136:139], v[80:83], v[144:147]\n"
"v_exp_f32 v70, v186\n"
"v_mfma_f32_16x16x32_bf16 v[144:147], a[140:143], v[88:91], v[144:147]\n"
"ds_read_b64_tr_b16 a[242:243], v233 offset:29696\n"
"ds_read_b64_tr_b16 a[244:245], v234 offset:25600\n"
"v_mfma_f32_16x16x32_bf16 v[148:151], a[144:147], v[64:67], v[200:203]\n"
"v_exp_f32 v71, v187\n"
"v_mfma_f32_16x16x32_bf16 v[148:151], a[148:151], v[72:75], v[148:151]\n"
"ds_read_b64_tr_b16 a[246:247], v234 offset:29696\n"
"v_cvt_pk_bf16_f32 v61, v70, v71\n"
"v_mfma_f32_16x16x32_bf16 v[148:151], a[152:155], v[80:83], v[148:151]\n"
"v_exp_f32 v76, v188\n"
"v_mfma_f32_16x16x32_bf16 v[148:151], a[156:159], v[88:91], v[148:151]\n"
"ds_read_b64_tr_b16 a[248:249], v233 offset:26112\n"
"ds_read_b64_tr_b16 a[250:251], v233 offset:30208\n"
"v_mfma_f32_16x16x32_bf16 v[152:155], a[128:131], v[96:99], v[204:207]\n"
"v_exp_f32 v77, v189\n"
"v_mfma_f32_16x16x32_bf16 v[152:155], a[132:135], v[104:107], v[152:155]\n"
"v_cvt_pk_bf16_f32 v62, v76, v77\n"
"v_mfma_f32_16x16x32_bf16 v[152:155], a[136:139], v[112:115], v[152:155]\n"
"v_exp_f32 v78, v190\n"
"v_mfma_f32_16x16x32_bf16 v[152:155], a[140:143], v[120:123], v[152:155]\n"
"ds_read_b64_tr_b16 a[252:253], v234 offset:26112\n"
"ds_read_b64_tr_b16 a[254:255], v234 offset:30208\n"
"v_mfma_f32_16x16x32_bf16 v[156:159], a[144:147], v[96:99], v[204:207]\n"
"v_exp_f32 v79, v191\n"
"v_mfma_f32_16x16x32_bf16 v[156:159], a[148:151], v[104:107], v[156:159]\n"
"s_cmp_eq_u32 s52, 0\n"
"s_cbranch_scc1 L_swv_%=\n"
"L_swvr_%=:\n"
"v_cvt_pk_bf16_f32 v63, v78, v79\n"
"v_mfma_f32_16x16x32_bf16 v[156:159], a[152:155], v[112:115], v[156:159]\n"
"v_mfma_f32_16x16x32_bf16 v[156:159], a[156:159], v[120:123], v[156:159]\n"
"s_waitcnt lgkmcnt(8)\n"
"v_mfma_f32_16x16x32_bf16 a[0:3], a[224:227], v[36:39], a[0:3]\n"
"v_exp_f32 v68, v128\n"
"v_mfma_f32_16x16x32_bf16 v[100:103], v[116:119], v[52:55], 0\n"
"ds_read_b128 a[160:163], v232 offset:40960\n"
"ds_read_b128 a[164:167], v232 offset:41472\n"
"v_mfma_f32_16x16x32_bf16 a[4:7], a[228:231], v[36:39], a[4:7]\n"
"v_exp_f32 v69, v129\n"
"v_mfma_f32_16x16x32_bf16 v[108:111], v[116:119], v[60:63], 0\n"
"s_waitcnt lgkmcnt(2)\n"
"v_cvt_pk_bf16_f32 v4, v68, v69\n"
"v_mfma_f32_16x16x32_bf16 a[8:11], a[232:235], v[36:39], a[8:11]\n"
"v_exp_f32 v70, v130\n"
"v_mfma_f32_16x16x32_bf16 a[12:15], a[236:239], v[36:39], a[12:15]\n"
"ds_read_b128 a[168:171], v232 offset:41984\n"
"ds_read_b128 a[172:175], v232 offset:42496\n"
"v_mfma_f32_16x16x32_bf16 a[16:19], a[240:243], v[36:39], a[16:19]\n"
"v_exp_f32 v71, v131\n"
"v_mfma_f32_16x16x32_bf16 a[20:23], a[244:247], v[36:39], a[20:23]\n"
"ds_read_b128 a[176:179], v232 offset:45056\n"
"v_cvt_pk_bf16_f32 v5, v70, v71\n"
"v_mfma_f32_16x16x32_bf16 a[24:27], a[248:251], v[36:39], a[24:27]\n"
"v_exp_f32 v76, v132\n"
"v_mfma_f32_16x16x32_bf16 a[28:31], a[252:255], v[36:39], a[28:31]\n"
"v_max_f32 v216, v100, v108\n"
"v_cmp_lt_f32 vcc, s53, v216\n"
"v_mfma_f32_16x16x32_bf16 a[32:35], a[224:227], v[44:47], a[32:35]\n"
"v_exp_f32 v77, v133\n"
"v_mfma_f32_16x16x32_bf16 a[36:39], a[228:231], v[44:47], a[36:39]\n"
"s_cbranch_vccnz L_rare5_%=\n"
"L_ret5_%=:\n"
"v_cvt_pk_bf16_f32 v6, v76, v77\n"
"v_mfma_f32_16x16x32_bf16 a[40:43], a[232:235], v[44:47], a[40:43]\n"
"v_exp_f32 v78, v134\n"
"v_mfma_f32_16x16x32_bf16 a[44:47], a[236:239], v[44:47], a[44:47]\n"
"ds_read_b128 a[180:183], v232 offset:45568\n"
"v_add_f32 v214, v214, v100\n"
"v_mfma_f32_16x16x32_bf16 a[48:51], a[240:243], v[44:47], a[48:51]\n"
"v_exp_f32 v79, v135\n"
"v_mfma_f32_16x16x32_bf16 a[52:55], a[244:247], v[44:47], a[52:55]\n"
"ds_read_b128 a[184:187], v232 offset:46080\n"
"v_cvt_pk_bf16_f32 v7, v78, v79\n"
"v_mfma_f32_16x16x32_bf16 a[56:59], a[248:251], v[44:47], a[56:59]\n"
"v_exp_f32 v68, v136\n"
"v_mfma_f32_16x16x32_bf16 a[60:63], a[252:255], v[44:47], a[60:63]\n"
"ds_read_b128 a[188:191], v232 offset:46592\n"
"v_add_f32 v215, v215, v108\n"
"v_mfma_f32_16x16x32_bf16 a[64:67], a[224:227], v[52:55], a[64:67]\n"
"v_exp_f32 v69, v137\n"
"v_mfma_f32_16x16x32_bf16 a[68:71], a[228:231], v[52:55], a[68:71]\n"
"s_add_u32 m0, s51, 0\n"
"v_cvt_pk_bf16_f32 v12, v68, v69\n"
"v_mfma_f32_16x16x32_bf16 a[72:75], a[232:235], v[52:55], a[72:75]\n"
"v_exp_f32 v70, v138\n"
"v_mfma_f32_16x16x32_bf16 a[76:79], a[236:239], v[52:55], a[76:79]\n"
"buffer_load_dwordx4 v240, s[24:27], s48 offen offset:0 lds\n"
"v_mfma_f32_16x16x32_bf16 a[80:83], a[240:243], v[52:55], a[80:83]\n"
"v_exp_f32 v71, v139\n"
"v_mfma_f32_16x16x32_bf16 a[84:87], a[244:247], v[52:55], a[84:87]\n"
"buffer_load_dwordx4 v241, s[24:27], s48 offen offset:1024 lds\n"
"v_cvt_pk_bf16_f32 v13, v70, v71\n"
"v_mfma_f32_16x16x32_bf16 a[88:91], a[248:251], v[52:55], a[88:91]\n"
"v_exp_f32 v76, v140\n"
"v_mfma_f32_16x16x32_bf16 a[92:95], a[252:255], v[52:55], a[92:95]\n"
"buffer_load_dwordx4 v217, s[24:27], s48 offen offset:2048 lds\n"
"v_mfma_f32_16x16x32_bf16 a[96:99], a[224:227], v[60:63], a[96:99]\n"
"v_exp_f32 v77, v141\n"
"v_mfma_f32_16x16x32_bf16 a[100:103], a[228:231], v[60:63], a[100:103]\n"
"buffer_load_dwordx4 v218, s[24:27], s48 offen offset:3072 lds\n"
"v_cvt_pk_bf16_f32 v14, v76, v77\n"
"v_mfma_f32_16x16x32_bf16 a[104:107], a[232:235], v[60:63], a[104:107]\n"
"v_exp_f32 v78, v142\n"
"v_mfma_f32_16x16x32_bf16 a[108:111], a[236:239], v[60:63], a[108:111]\n"
"s_add_u32 s48, s48, s61\n"
"v_mfma_f32_16x16x32_bf16 a[112:115], a[240:243], v[60:63], a[112:115]\n"
"v_exp_f32 v79, v143\n"
"v_mfma_f32_16x16x32_bf16 a[116:119], a[244:247], v[60:63], a[116:119]\n"
"buffer_load_dwordx4 v[84:87], v253, s[68:71], 0 offen offset:0\n"
"v_cvt_pk_bf16_f32 v15, v78, v79\n"
"v_mfma_f32_16x16x32_bf16 a[120:123], a[248:251], v[60:63], a[120:123]\n"
"buffer_load_dwordx4 v[92:95], v253, s[40:43], 0 offen offset:0\n"
"v_mfma_f32_16x16x32_bf16 a[124:127], a[252:255], v[60:63], a[124:127]\n"
"s_waitcnt vmcnt(18) lgkmcnt(0)\n"
"s_barrier\n"
"v_mfma_f32_16x16x32_bf16 v[160:163], a[160:163], v[0:3], v[192:195]\n"
"v_exp_f32 v68, v144\n"
"v_mfma_f32_16x16x32_bf16 v[100:103], v[116:119], v[4:7], 0\n"
"ds_read_b64_tr_b16 a[192:193], v233 offset:32768\n"
"ds_read_b64_tr_b16 a[194:195], v233 offset:36864\n"
"v_mfma_f32_16x16x32_bf16 v[160:163], a[164:167], v[8:11], v[160:163]\n"
"v_exp_f32 v69, v145\n"
"v_mfma_f32_16x16x32_bf16 v[108:111], v[116:119], v[12:15], 0\n"
"v_cvt_pk_bf16_f32 v20, v68, v69\n"
"v_mfma_f32_16x16x32_bf16 v[160:163], a[168:171], v[16:19], v[160:163]\n"
"v_exp_f32 v70, v146\n"
"v_mfma_f32_16x16x32_bf16 v[160:163], a[172:175], v[24:27], v[160:163]\n"
"ds_read_b64_tr_b16 a[196:197], v234 offset:32768\n"
"ds_read_b64_tr_b16 a[198:199], v234 offset:36864\n"
"v_mfma_f32_16x16x32_bf16 v[164:167], a[176:179], v[0:3], v[192:195]\n"
"v_exp_f32 v71, v147\n"
"v_mfma_f32_16x16x32_bf16 v[164:167], a[180:183], v[8:11], v[164:167]\n"
"ds_read_b64_tr_b16 a[200:201], v233 offset:33280\n"
"v_cvt_pk_bf16_f32 v21, v70, v71\n"
"v_mfma_f32_16x16x32_bf16 v[164:167], a[184:187], v[16:19], v[164:167]\n"
"v_exp_f32 v76, v148\n"
"v_mfma_f32_16x16x32_bf16 v[164:167], a[188:191], v[24:27], v[164:167]\n"
"v_max_f32 v216, v100, v108\n"
"v_cmp_lt_f32 vcc, s53, v216\n"
"v_mfma_f32_16x16x32_bf16 v[168:171], a[160:163], v[32:35], v[196:199]\n"
"v_exp_f32 v77, v149\n"
"v_mfma_f32_16x16x32_bf16 v[168:171], a[164:167], v[40:43], v[168:171]\n"
"s_cbranch_vccnz L_rare6_%=\n"
"L_ret6_%=:\n"
"v_cvt_pk_bf16_f32 v22, v76, v77\n"
"v_mfma_f32_16x16x32_bf16 v[168:171], a[168:171], v[48:51], v[168:171]\n"
"v_exp_f32 v78, v150\n"
"v_mfma_f32_16x16x32_bf16 v[168:171], a[172:175], v[56:59], v[168:171]\n"
"ds_read_b64_tr_b16 a[202:203], v233 offset:37376\n"
"v_add_f32 v212, v212, v100\n"
"v_mfma_f32_16x16x32_bf16 v[172:175], a[176:179], v[32:35], v[196:199]\n"
"v_exp_f32 v79, v151\n"
"v_mfma_f32_16x16x32_bf16 v[172:175], a[180:183], v[40:43], v[172:175]\n"
"ds_read_b64_tr_b16 a[204:205], v234 offset:33280\n"
"v_cvt_pk_bf16_f32 v23, v78, v79\n"
"v_mfma_f32_16x16x32_bf16 v[172:175], a[184:187], v[48:51], v[172:175]\n"
"v_exp_f32 v68, v152\n"
"v_mfma_f32_16x16x32_bf16 v[172:175], a[188:191], v[56:59], v[172:175]\n"
"ds_read_b64_tr_b16 a[206:207], v234 offset:37376\n"
"v_add_f32 v213, v213, v108\n"
"v_mfma_f32_16x16x32_bf16 v[176:179], a[160:163], v[64:67], v[200:203]\n"
"v_exp_f32 v69, v153\n"
"v_mfma_f32_16x16x32_bf16 v[176:179], a[164:167], v[72:75], v[176:179]\n"
"ds_read_b64_tr_b16 a[208:209], v233 offset:33792\n"
"v_cvt_pk_bf16_f32 v28, v68, v69\n"
"v_mfma_f32_16x16x32_bf16 v[176:179], a[168:171], v[80:83], v[176:179]\n"
"v_exp_f32 v70, v154\n"
"v_mfma_f32_16x16x32_bf16 v[176:179], a[172:175], v[88:91], v[176:179]\n"
"ds_read_b64_tr_b16 a[210:211], v233 offset:37888\n"
"ds_read_b64_tr_b16 a[212:213], v234 offset:33792\n"
"v_mfma_f32_16x16x32_bf16 v[180:183], a[176:179], v[64:67], v[200:203]\n"
"v_exp_f32 v71, v155\n"
"v_mfma_f32_16x16x32_bf16 v[180:183], a[180:183], v[72:75], v[180:183]\n"
"ds_read_b64_tr_b16 a[214:215], v234 offset:37888\n"
"v_cvt_pk_bf16_f32 v29, v70, v71\n"
"v_mfma_f32_16x16x32_bf16 v[180:183], a[184:187], v[80:83], v[180:183]\n"
"v_exp_f32 v76, v156\n"
"v_mfma_f32_16x16x32_bf16 v[180:183], a[188:191], v[88:91], v[180:183]\n"
"ds_read_b64_tr_b16 a[216:217], v233 offset:34304\n"
"ds_read_b64_tr_b16 a[218:219], v233 offset:38400\n"
"v_mfma_f32_16x16x32_bf16 v[184:187], a[160:163], v[96:99], v[204:207]\n"
"v_exp_f32 v77, v157\n"
"v_mfma_f32_16x16x32_bf16 v[184:187], a[164:167], v[104:107], v[184:187]\n"
"v_cvt_pk_bf16_f32 v30, v76, v77\n"
"v_mfma_f32_16x16x32_bf16 v[184:187], a[168:171], v[112:115], v[184:187]\n"
"v_exp_f32 v78, v158\n"
"v_mfma_f32_16x16x32_bf16 v[184:187], a[172:175], v[120:123], v[184:187]\n"
"ds_read_b64_tr_b16 a[220:221], v234 offset:34304\n"
"ds_read_b64_tr_b16 a[222:223], v234 offset:38400\n"
"v_add_u32 v253, 0x400, v253\n"
"v_mfma_f32_16x16x32_bf16 v[188:191], a[176:179], v[96:99], v[204:207]\n"
"v_exp_f32 v79, v159\n"
"v_mfma_f32_16x16x32_bf16 v[188:191], a[180:183], v[104:107], v[188:191]\n"
"v_cvt_pk_bf16_f32 v31, v78, v79\n"
"v_mfma_f32_16x16x32_bf16 v[188:191], a[184:187], v[112:115], v[188:191]\n"
"v_mfma_f32_16x16x32_bf16 v[188:191], a[188:191], v[120:123], v[188:191]\n"
"s_waitcnt lgkmcnt(8)\n"
"v_mfma_f32_16x16x32_bf16 a[0:3], a[192:195], v[4:7], a[0:3]\n"
"v_exp_f32 v68, v160\n"
"v_mfma_f32_16x16x32_bf16 v[100:103], v[116:119], v[20:23], 0\n"
"ds_read_b128 a[128:131], v232 offset:49152\n"
"ds_read_b128 a[132:135], v232 offset:49664\n"
"v_mfma_f32_16x16x32_bf16 a[4:7], a[196:199], v[4:7], a[4:7]\n"
"v_exp_f32 v69, v161\n"
"v_mfma_f32_16x16x32_bf16 v[108:111], v[116:119], v[28:31], 0\n"
"s_waitcnt lgkmcnt(2)\n"
"v_cvt_pk_bf16_f32 v36, v68, v69\n"
"v_mfma_f32_16x16x32_bf16 a[8:11], a[200:203], v[4:7], a[8:11]\n"
"v_exp_f32 v70, v162\n"
"v_mfma_f32_16x16x32_bf16 a[12:15], a[204:207], v[4:7], a[12:15]\n"
"ds_read_b128 a[136:139], v232 offset:50176\n"
"ds_read_b128 a[140:143], v232 offset:50688\n"
"v_mfma_f32_16x16x32_bf16 a[16:19], a[208:211], v[4:7], a[16:19]\n"
"v_exp_f32 v71, v163\n"
"v_mfma_f32_16x16x32_bf16 a[20:23], a[212:215], v[4:7], a[20:23]\n"
"ds_read_b128 a[144:147], v232 offset:53248\n"
"v_cvt_pk_bf16_f32 v37, v70, v71\n"
"v_mfma_f32_16x16x32_bf16 a[24:27], a[216:219], v[4:7], a[24:27]\n"
"v_exp_f32 v76, v164\n"
"v_mfma_f32_16x16x32_bf16 a[28:31], a[220:223], v[4:7], a[28:31]\n"
"v_max_f32 v216, v100, v108\n"
"v_cmp_lt_f32 vcc, s53, v216\n"
"v_mfma_f32_16x16x32_bf16 a[32:35], a[192:195], v[12:15], a[32:35]\n"
"v_exp_f32 v77, v165\n"
"v_mfma_f32_16x16x32_bf16 a[36:39], a[196:199], v[12:15], a[36:39]\n"
"s_cbranch_vccnz L_rare7_%=\n"
"L_ret7_%=:\n"
"v_cvt_pk_bf16_f32 v38, v76, v77\n"
"v_mfma_f32_16x16x32_bf16 a[40:43], a[200:203], v[12:15], a[40:43]\n"
"v_exp_f32 v78, v166\n"
"v_mfma_f32_16x16x32_bf16 a[44:47], a[204:207], v[12:15], a[44:47]\n"
"ds_read_b128 a[148:151], v232 offset:53760\n"
"v_add_f32 v214, v214, v100\n"
"v_mfma_f32_16x16x32_bf16 a[48:51], a[208:211], v[12:15], a[48:51]\n"
"v_exp_f32 v79, v167\n"
"v_mfma_f32_16x16x32_bf16 a[52:55], a[212:215], v[12:15], a[52:55]\n"
"ds_read_b128 a[152:155], v232 offset:54272\n"
"v_cvt_pk_bf16_f32 v39, v78, v79\n"
"v_mfma_f32_16x16x32_bf16 a[56:59], a[216:219], v[12:15], a[56:59]\n"
"v_exp_f32 v68, v168\n"
"v_mfma_f32_16x16x32_bf16 a[60:63], a[220:223], v[12:15], a[60:63]\n"
"ds_read_b128 a[156:159], v232 offset:54784\n"
"v_add_f32 v215, v215, v108\n"
"v_mfma_f32_16x16x32_bf16 a[64:67], a[192:195], v[20:23], a[64:67]\n"
"v_exp_f32 v69, v169\n"
"v_mfma_f32_16x16x32_bf16 a[68:71], a[196:199], v[20:23], a[68:71]\n"
"s_add_u32 m0, s50, 0x8000\n"
"v_cvt_pk_bf16_f32 v44, v68, v69\n"
"v_mfma_f32_16x16x32_bf16 a[72:75], a[200:203], v[20:23], a[72:75]\n"
"v_exp_f32 v70, v170\n"
"v_mfma_f32_16x16x32_bf16 a[76:79], a[204:207], v[20:23], a[76:79]\n"
"buffer_load_dwordx4 v236, s[20:23], s47 offen offset:0 lds\n"
"v_mfma_f32_16x16x32_bf16 a[80:83], a[208:211], v[20:23], a[80:83]\n"
"v_exp_f32 v71, v171\n"
"v_mfma_f32_16x16x32_bf16 a[84:87], a[212:215], v[20:23], a[84:87]\n"
"buffer_load_dwordx4 v237, s[20:23], s47 offen offset:1024 lds\n"
"v_cvt_pk_bf16_f32 v45, v70, v71\n"
"v_mfma_f32_16x16x32_bf16 a[88:91], a[216:219], v[20:23], a[88:91]\n"
"v_exp_f32 v76, v172\n"
"v_mfma_f32_16x16x32_bf16 a[92:95], a[220:223], v[20:23], a[92:95]\n"
"buffer_load_dwordx4 v238, s[20:23], s47 offen offset:2048 lds\n"
"v_mfma_f32_16x16x32_bf16 a[96:99], a[192:195], v[28:31], a[96:99]\n"
"v_exp_f32 v77, v173\n"
"v_mfma_f32_16x16x32_bf16 a[100:103], a[196:199], v[28:31], a[100:103]\n"
"buffer_load_dwordx4 v239, s[20:23], s47 offen offset:3072 lds\n"
"v_cvt_pk_bf16_f32 v46, v76, v77\n"
"v_mfma_f32_16x16x32_bf16 a[104:107], a[200:203], v[28:31], a[104:107]\n"
"v_exp_f32 v78, v174\n"
"v_mfma_f32_16x16x32_bf16 a[108:111], a[204:207], v[28:31], a[108:111]\n"
"s_add_u32 s47, s47, s55\n"
"v_mfma_f32_16x16x32_bf16 a[112:115], a[208:211], v[28:31], a[112:115]\n"
"v_exp_f32 v79, v175\n"
"v_mfma_f32_16x16x32_bf16 a[116:119], a[212:215], v[28:31], a[116:119]\n"
"v_cvt_pk_bf16_f32 v47, v78, v79\n"
"v_mfma_f32_16x16x32_bf16 a[120:123], a[216:219], v[28:31], a[120:123]\n"
"v_mfma_f32_16x16x32_bf16 a[124:127], a[220:223], v[28:31], a[124:127]\n"
"s_waitcnt lgkmcnt(0)\n"
"v_mfma_f32_16x16x32_bf16 v[128:131], a[128:131], v[0:3], v[192:195]\n"
"v_exp_f32 v68, v176\n"
"v_mfma_f32_16x16x32_bf16 v[100:103], v[116:119], v[36:39], 0\n"
"ds_read_b64_tr_b16 a[224:225], v233 offset:40960\n"
"ds_read_b64_tr_b16 a[226:227], v233 offset:45056\n"
"v_mfma_f32_16x16x32_bf16 v[128:131], a[132:135], v[8:11], v[128:131]\n"
"v_exp_f32 v69, v177\n"
"v_mfma_f32_16x16x32_bf16 v[108:111], v[116:119], v[44:47], 0\n"
"v_cvt_pk_bf16_f32 v52, v68, v69\n"
"v_mfma_f32_16x16x32_bf16 v[128:131], a[136:139], v[16:19], v[128:131]\n"
"v_exp_f32 v70, v178\n"
"v_mfma_f32_16x16x32_bf16 v[128:131], a[140:143], v[24:27], v[128:131]\n"
"ds_read_b64_tr_b16 a[228:229], v234 offset:40960\n"
"ds_read_b64_tr_b16 a[230:231], v234 offset:45056\n"
"v_mfma_f32_16x16x32_bf16 v[132:135], a[144:147], v[0:3], v[192:195]\n"
"v_exp_f32 v71, v179\n"
"v_mfma_f32_16x16x32_bf16 v[132:135], a[148:151], v[8:11], v[132:135]\n"
"ds_read_b64_tr_b16 a[232:233], v233 offset:41472\n"
"v_cvt_pk_bf16_f32 v53, v70, v71\n"
"v_mfma_f32_16x16x32_bf16 v[132:135], a[152:155], v[16:19], v[132:135]\n"
"v_exp_f32 v76, v180\n"
"v_mfma_f32_16x16x32_bf16 v[132:135], a[156:159], v[24:27], v[132:135]\n"
"v_max_f32 v216, v100, v108\n"
"v_cmp_lt_f32 vcc, s53, v216\n"
"v_mfma_f32_16x16x32_bf16 v[136:139], a[128:131], v[32:35], v[196:199]\n"
"v_exp_f32 v77, v181\n"
"v_mfma_f32_16x16x32_bf16 v[136:139], a[132:135], v[40:43], v[136:139]\n"
"s_cbranch_vccnz L_rare8_%=\n"
"L_ret8_%=:\n"
"v_cvt_pk_bf16_f32 v54, v76, v77\n"
"v_mfma_f32_16x16x32_bf16 v[136:139], a[136:139], v[48:51], v[136:139]\n"
"v_exp_f32 v78, v182\n"
"v_mfma_f32_16x16x32_bf16 v[136:139], a[140:143], v[56:59], v[136:139]\n"
"ds_read_b64_tr_b16 a[234:235], v233 offset:45568\n"
"v_add_f32 v212, v212, v100\n"
"v_mfma_f32_16x16x32_bf16 v[140:143], a[144:147], v[32:35], v[196:199]\n"
"v_exp_f32 v79, v183\n"
"v_mfma_f32_16x16x32_bf16 v[140:143], a[148:151], v[40:43], v[140:143]\n"
"ds_read_b64_tr_b16 a[236:237], v234 offset:41472\n"
"v_cvt_pk_bf16_f32 v55, v78, v79\n"
"v_mfma_f32_16x16x32_bf16 v[140:143], a[152:155], v[48:51], v[140:143]\n"
"v_exp_f32 v68, v184\n"
"v_mfma_f32_16x16x32_bf16 v[140:143], a[156:159], v[56:59], v[140:143]\n"
"ds_read_b64_tr_b16 a[238:239], v234 offset:45568\n"
"v_add_f32 v213, v213, v108\n"
"v_mfma_f32_16x16x32_bf16 v[144:147], a[128:131], v[64:67], v[200:203]\n"
"v_exp_f32 v69, v185\n"
"v_mfma_f32_16x16x32_bf16 v[144:147], a[132:135], v[72:75], v[144:147]\n"
"ds_read_b64_tr_b16 a[240:241], v233 offset:41984\n"
"v_cvt_pk_bf16_f32 v60, v68, v69\n"
"v_mfma_f32_16x16x32_bf16 v[144:147], a[136:139], v[80:83], v[144:147]\n"
"v_exp_f32 v70, v186\n"
"v_mfma_f32_16x16x32_bf16 v[144:147], a[140:143], v[88:91], v[144:147]\n"
"ds_read_b64_tr_b16 a[242:243], v233 offset:46080\n"
"ds_read_b64_tr_b16 a[244:245], v234 offset:41984\n"
"v_mfma_f32_16x16x32_bf16 v[148:151], a[144:147], v[64:67], v[200:203]\n"
"v_exp_f32 v71, v187\n"
"v_mfma_f32_16x16x32_bf16 v[148:151], a[148:151], v[72:75], v[148:151]\n"
"ds_read_b64_tr_b16 a[246:247], v234 offset:46080\n"
"v_cvt_pk_bf16_f32 v61, v70, v71\n"
"v_mfma_f32_16x16x32_bf16 v[148:151], a[152:155], v[80:83], v[148:151]\n"
"v_exp_f32 v76, v188\n"
"v_mfma_f32_16x16x32_bf16 v[148:151], a[156:159], v[88:91], v[148:151]\n"
"ds_read_b64_tr_b16 a[248:249], v233 offset:42496\n"
"ds_read_b64_tr_b16 a[250:251], v233 offset:46592\n"
"v_mfma_f32_16x16x32_bf16 v[152:155], a[128:131], v[96:99], v[204:207]\n"
"v_exp_f32 v77, v189\n"
"v_mfma_f32_16x16x32_bf16 v[152:155], a[132:135], v[104:107], v[152:155]\n"
"v_cvt_pk_bf16_f32 v62, v76, v77\n"
"v_mfma_f32_16x16x32_bf16 v[152:155], a[136:139], v[112:115], v[152:155]\n"
"v_exp_f32 v78, v190\n"
"v_mfma_f32_16x16x32_bf16 v[152:155], a[140:143], v[120:123], v[152:155]\n"
"ds_read_b64_tr_b16 a[252:253], v234 offset:42496\n"
"ds_read_b64_tr_b16 a[254:255], v234 offset:46592\n"
"v_mfma_f32_16x16x32_bf16 v[156:159], a[144:147], v[96:99], v[204:207]\n"
"v_exp_f32 v79, v191\n"
"v_mfma_f32_16x16x32_bf16 v[156:159], a[148:151], v[104:107], v[156:159]\n"
"v_cvt_pk_bf16_f32 v63, v78, v79\n"
"v_mfma_f32_16x16x32_bf16 v[156:159], a[152:155], v[112:115], v[156:159]\n"
"v_mfma_f32_16x16x32_bf16 v[156:159], a[156:159], v[120:123], v[156:159]\n"
"s_waitcnt lgkmcnt(8)\n"
"v_mfma_f32_16x16x32_bf16 a[0:3], a[224:227], v[36:39], a[0:3]\n"
"v_exp_f32 v68, v128\n"
"v_mfma_f32_16x16x32_bf16 v[100:103], v[116:119], v[52:55], 0\n"
"ds_read_b128 a[160:163], v232 offset:57344\n"
"ds_read_b128 a[164:167], v232 offset:57856\n"
"v_mfma_f32_16x16x32_bf16 a[4:7], a[228:231], v[36:39], a[4:7]\n"
"v_exp_f32 v69, v129\n"
"v_mfma_f32_16x16x32_bf16 v[108:111], v[116:119], v[60:63], 0\n"
"s_waitcnt lgkmcnt(2)\n"
"v_cvt_pk_bf16_f32 v4, v68, v69\n"
"v_mfma_f32_16x16x32_bf16 a[8:11], a[232:235], v[36:39], a[8:11]\n"
"v_exp_f32 v70, v130\n"
"v_mfma_f32_16x16x32_bf16 a[12:15], a[236:239], v[36:39], a[12:15]\n"
"ds_read_b128 a[168:171], v232 offset:58368\n"
"ds_read_b128 a[172:175], v232 offset:58880\n"
"v_mfma_f32_16x16x32_bf16 a[16:19], a[240:243], v[36:39], a[16:19]\n"
"v_exp_f32 v71, v131\n"
"v_mfma_f32_16x16x32_bf16 a[20:23], a[244:247], v[36:39], a[20:23]\n"
"ds_read_b128 a[176:179], v232 offset:61440\n"
"v_cvt_pk_bf16_f32 v5, v70, v71\n"
"v_mfma_f32_16x16x32_bf16 a[24:27], a[248:251], v[36:39], a[24:27]\n"
"v_exp_f32 v76, v132\n"
"v_mfma_f32_16x16x32_bf16 a[28:31], a[252:255], v[36:39], a[28:31]\n"
"v_max_f32 v216, v100, v108\n"
"v_cmp_lt_f32 vcc, s53, v216\n"
"v_mfma_f32_16x16x32_bf16 a[32:35], a[224:227], v[44:47], a[32:35]\n"
"v_exp_f32 v77, v133\n"
"v_mfma_f32_16x16x32_bf16 a[36:39], a[228:231], v[44:47], a[36:39]\n"
"s_cbranch_vccnz L_rare9_%=\n"
"L_ret9_%=:\n"
"v_cvt_pk_bf16_f32 v6, v76, v77\n"
"v_mfma_f32_16x16x32_bf16 a[40:43], a[232:235], v[44:47], a[40:43]\n"
"v_exp_f32 v78, v134\n"
"v_mfma_f32_16x16x32_bf16 a[44:47], a[236:239], v[44:47], a[44:47]\n"
"ds_read_b128 a[180:183], v232 offset:61952\n"
"v_add_f32 v214, v214, v100\n"
"v_mfma_f32_16x16x32_bf16 a[48:51], a[240:243], v[44:47], a[48:51]\n"
"v_exp_f32 v79, v135\n"
"v_mfma_f32_16x16x32_bf16 a[52:55], a[244:247], v[44:47], a[52:55]\n"
"ds_read_b128 a[184:187], v232 offset:62464\n"
"v_cvt_pk_bf16_f32 v7, v78, v79\n"
"v_mfma_f32_16x16x32_bf16 a[56:59], a[248:251], v[44:47], a[56:59]\n"
"v_exp_f32 v68, v136\n"
"v_mfma_f32_16x16x32_bf16 a[60:63], a[252:255], v[44:47], a[60:63]\n"
"ds_read_b128 a[188:191], v232 offset:62976\n"
"v_add_f32 v215, v215, v108\n"
"v_mfma_f32_16x16x32_bf16 a[64:67], a[224:227], v[52:55], a[64:67]\n"
"v_exp_f32 v69, v137\n"
"v_mfma_f32_16x16x32_bf16 a[68:71], a[228:231], v[52:55], a[68:71]\n"
"s_add_u32 m0, s51, 0x4000\n"
"v_cvt_pk_bf16_f32 v12, v68, v69\n"
"v_mfma_f32_16x16x32_bf16 a[72:75], a[232:235], v[52:55], a[72:75]\n"
"v_exp_f32 v70, v138\n"
"v_mfma_f32_16x16x32_bf16 a[76:79], a[236:239], v[52:55], a[76:79]\n"
"buffer_load_dwordx4 v240, s[24:27], s48 offen offset:0 lds\n"
"v_mfma_f32_16x16x32_bf16 a[80:83], a[240:243], v[52:55], a[80:83]\n"
"v_exp_f32 v71, v139\n"
"v_mfma_f32_16x16x32_bf16 a[84:87], a[244:247], v[52:55], a[84:87]\n"
"buffer_load_dwordx4 v241, s[24:27], s48 offen offset:1024 lds\n"
"v_cvt_pk_bf16_f32 v13, v70, v71\n"
"v_mfma_f32_16x16x32_bf16 a[88:91], a[248:251], v[52:55], a[88:91]\n"
"v_exp_f32 v76, v140\n"
"v_mfma_f32_16x16x32_bf16 a[92:95], a[252:255], v[52:55], a[92:95]\n"
"buffer_load_dwordx4 v217, s[24:27], s48 offen offset:2048 lds\n"
"v_mfma_f32_16x16x32_bf16 a[96:99], a[224:227], v[60:63], a[96:99]\n"
"v_exp_f32 v77, v141\n"
"v_mfma_f32_16x16x32_bf16 a[100:103], a[228:231], v[60:63], a[100:103]\n"
"buffer_load_dwordx4 v218, s[24:27], s48 offen offset:3072 lds\n"
"v_cvt_pk_bf16_f32 v14, v76, v77\n"
"v_mfma_f32_16x16x32_bf16 a[104:107], a[232:235], v[60:63], a[104:107]\n"
"v_exp_f32 v78, v142\n"
"v_mfma_f32_16x16x32_bf16 a[108:111], a[236:239], v[60:63], a[108:111]\n"
"s_add_u32 s48, s48, s61\n"
"v_mfma_f32_16x16x32_bf16 a[112:115], a[240:243], v[60:63], a[112:115]\n"
"v_exp_f32 v79, v143\n"
"v_mfma_f32_16x16x32_bf16 a[116:119], a[244:247], v[60:63], a[116:119]\n"
"v_cvt_pk_bf16_f32 v15, v78, v79\n"
"v_mfma_f32_16x16x32_bf16 a[120:123], a[248:251], v[60:63], a[120:123]\n"
"v_mfma_f32_16x16x32_bf16 a[124:127], a[252:255], v[60:63], a[124:127]\n"
"s_waitcnt vmcnt(18) lgkmcnt(0)\n"
"s_barrier\n"
"v_mfma_f32_16x16x32_bf16 v[160:163], a[160:163], v[0:3], v[192:195]\n"
"v_exp_f32 v68, v144\n"
"v_mfma_f32_16x16x32_bf16 v[100:103], v[116:119], v[4:7], 0\n"
"ds_read_b64_tr_b16 a[192:193], v233 offset:49152\n"
"ds_read_b64_tr_b16 a[194:195], v233 offset:53248\n"
"v_mfma_f32_16x16x32_bf16 v[160:163], a[164:167], v[8:11], v[160:163]\n"
"v_exp_f32 v69, v145\n"
"v_mfma_f32_16x16x32_bf16 v[108:111], v[116:119], v[12:15], 0\n"
"v_cvt_pk_bf16_f32 v20, v68, v69\n"
"v_mfma_f32_16x16x32_bf16 v[160:163], a[168:171], v[16:19], v[160:163]\n"
"v_exp_f32 v70, v146\n"
"v_mfma_f32_16x16x32_bf16 v[160:163], a[172:175], v[24:27], v[160:163]\n"
"ds_read_b64_tr_b16 a[196:197], v234 offset:49152\n"
"ds_read_b64_tr_b16 a[198:199], v234 offset:53248\n"
"v_mfma_f32_16x16x32_bf16 v[164:167], a[176:179], v[0:3], v[192:195]\n"
"v_exp_f32 v71, v147\n"
"v_mfma_f32_16x16x32_bf16 v[164:167], a[180:183], v[8:11], v[164:167]\n"
"ds_read_b64_tr_b16 a[200:201], v233 offset:49664\n"
"v_cvt_pk_bf16_f32 v21, v70, v71\n"
"v_mfma_f32_16x16x32_bf16 v[164:167], a[184:187], v[16:19], v[164:167]\n"
"v_exp_f32 v76, v148\n"
"v_mfma_f32_16x16x32_bf16 v[164:167], a[188:191], v[24:27], v[164:167]\n"
"v_max_f32 v216, v100, v108\n"
"v_cmp_lt_f32 vcc, s53, v216\n"
"v_mfma_f32_16x16x32_bf16 v[168:171], a[160:163], v[32:35], v[196:199]\n"
"v_exp_f32 v77, v149\n"
"v_mfma_f32_16x16x32_bf16 v[168:171], a[164:167], v[40:43], v[168:171]\n"
"s_cbranch_vccnz L_rare10_%=\n"
"L_ret10_%=:\n"
"v_cvt_pk_bf16_f32 v22, v76, v77\n"
"v_mfma_f32_16x16x32_bf16 v[168:171], a[168:171], v[48:51], v[168:171]\n"
"v_exp_f32 v78, v150\n"
"v_mfma_f32_16x16x32_bf16 v[168:171], a[172:175], v[56:59], v[168:171]\n"
"ds_read_b64_tr_b16 a[202:203], v233 offset:53760\n"
"v_add_f32 v212, v212, v100\n"
"v_mfma_f32_16x16x32_bf16 v[172:175], a[176:179], v[32:35], v[196:199]\n"
"v_exp_f32 v79, v151\n"
"v_mfma_f32_16x16x32_bf16 v[172:175], a[180:183], v[40:43], v[172:175]\n"
"ds_read_b64_tr_b16 a[204:205], v234 offset:49664\n"
"v_cvt_pk_bf16_f32 v23, v78, v79\n"
"v_mfma_f32_16x16x32_bf16 v[172:175], a[184:187], v[48:51], v[172:175]\n"
"v_exp_f32 v68, v152\n"
"v_mfma_f32_16x16x32_bf16 v[172:175], a[188:191], v[56:59], v[172:175]\n"
"ds_read_b64_tr_b16 a[206:207], v234 offset:53760\n"
"v_add_f32 v213, v213, v108\n"
"v_mfma_f32_16x16x32_bf16 v[176:179], a[160:163], v[64:67], v[200:203]\n"
"v_exp_f32 v69, v153\n"
"v_mfma_f32_16x16x32_bf16 v[176:179], a[164:167], v[72:75], v[176:179]\n"
"ds_read_b64_tr_b16 a[208:209], v233 offset:50176\n"
"v_cvt_pk_bf16_f32 v28, v68, v69\n"
"v_mfma_f32_16x16x32_bf16 v[176:179], a[168:171], v[80:83], v[176:179]\n"
"v_exp_f32 v70, v154\n"
"v_mfma_f32_16x16x32_bf16 v[176:179], a[172:175], v[88:91], v[176:179]\n"
"ds_read_b64_tr_b16 a[210:211], v233 offset:54272\n"
"ds_read_b64_tr_b16 a[212:213], v234 offset:50176\n"
"v_mfma_f32_16x16x32_bf16 v[180:183], a[176:179], v[64:67], v[200:203]\n"
"v_exp_f32 v71, v155\n"
"v_mfma_f32_16x16x32_bf16 v[180:183], a[180:183], v[72:75], v[180:183]\n"
"ds_read_b64_tr_b16 a[214:215], v234 offset:54272\n"
"v_cvt_pk_bf16_f32 v29, v70, v71\n"
"v_mfma_f32_16x16x32_bf16 v[180:183], a[184:187], v[80:83], v[180:183]\n"
"v_exp_f32 v76, v156\n"
"v_mfma_f32_16x16x32_bf16 v[180:183], a[188:191], v[88:91], v[180:183]\n"
"ds_read_b64_tr_b16 a[216:217], v233 offset:50688\n"
"ds_read_b64_tr_b16 a[218:219], v233 offset:54784\n"
"v_mfma_f32_16x16x32_bf16 v[184:187], a[160:163], v[96:99], v[204:207]\n"
"v_exp_f32 v77, v157\n"
"v_mfma_f32_16x16x32_bf16 v[184:187], a[164:167], v[104:107], v[184:187]\n"
"v_cvt_pk_bf16_f32 v30, v76, v77\n"
"v_mfma_f32_16x16x32_bf16 v[184:187], a[168:171], v[112:115], v[184:187]\n"
"v_exp_f32 v78, v158\n"
"v_mfma_f32_16x16x32_bf16 v[184:187], a[172:175], v[120:123], v[184:187]\n"
"ds_read_b64_tr_b16 a[220:221], v234 offset:50688\n"
"ds_read_b64_tr_b16 a[222:223], v234 offset:54784\n"
"v_mfma_f32_16x16x32_bf16 v[188:191], a[176:179], v[96:99], v[204:207]\n"
"v_exp_f32 v79, v159\n"
"v_mfma_f32_16x16x32_bf16 v[188:191], a[180:183], v[104:107], v[188:191]\n"
"v_cvt_pk_bf16_f32 v31, v78, v79\n"
"v_mfma_f32_16x16x32_bf16 v[188:191], a[184:187], v[112:115], v[188:191]\n"
"v_mfma_f32_16x16x32_bf16 v[188:191], a[188:191], v[120:123], v[188:191]\n"
"s_waitcnt lgkmcnt(8)\n"
"v_mfma_f32_16x16x32_bf16 a[0:3], a[192:195], v[4:7], a[0:3]\n"
"v_exp_f32 v68, v160\n"
"v_mfma_f32_16x16x32_bf16 v[100:103], v[116:119], v[20:23], 0\n"
"ds_read_b128 a[128:131], v232 offset:0\n"
"ds_read_b128 a[132:135], v232 offset:512\n"
"v_mfma_f32_16x16x32_bf16 a[4:7], a[196:199], v[4:7], a[4:7]\n"
"v_exp_f32 v69, v161\n"
"v_mfma_f32_16x16x32_bf16 v[108:111], v[116:119], v[28:31], 0\n"
"s_waitcnt lgkmcnt(2)\n"
"v_cvt_pk_bf16_f32 v36, v68, v69\n"
"v_mfma_f32_16x16x32_bf16 a[8:11], a[200:203], v[4:7], a[8:11]\n"
"v_exp_f32 v70, v162\n"
"v_mfma_f32_16x16x32_bf16 a[12:15], a[204:207], v[4:7], a[12:15]\n"
"ds_read_b128 a[136:139], v232 offset:1024\n"
"ds_read_b128 a[140:143], v232 offset:1536\n"
"v_mfma_f32_16x16x32_bf16 a[16:19], a[208:211], v[4:7], a[16:19]\n"
"v_exp_f32 v71, v163\n"
"v_mfma_f32_16x16x32_bf16 a[20:23], a[212:215], v[4:7], a[20:23]\n"
"ds_read_b128 a[144:147], v232 offset:4096\n"
"v_cvt_pk_bf16_f32 v37, v70, v71\n"
"v_mfma_f32_16x16x32_bf16 a[24:27], a[216:219], v[4:7], a[24:27]\n"
"v_exp_f32 v76, v164\n"
"v_mfma_f32_16x16x32_bf16 a[28:31], a[220:223], v[4:7], a[28:31]\n"
"v_max_f32 v216, v100, v108\n"
"v_cmp_lt_f32 vcc, s53, v216\n"
"v_mfma_f32_16x16x32_bf16 a[32:35], a[192:195], v[12:15], a[32:35]\n"
"v_exp_f32 v77, v165\n"
"v_mfma_f32_16x16x32_bf16 a[36:39], a[196:199], v[12:15], a[36:39]\n"
"s_cbranch_vccnz L_rare11_%=\n"
"L_ret11_%=:\n"
"v_cvt_pk_bf16_f32 v38, v76, v77\n"
"v_mfma_f32_16x16x32_bf16 a[40:43], a[200:203], v[12:15], a[40:43]\n"
"v_exp_f32 v78, v166\n"
"v_mfma_f32_16x16x32_bf16 a[44:47], a[204:207], v[12:15], a[44:47]\n"
"ds_read_b128 a[148:151], v232 offset:4608\n"
"v_add_f32 v214, v214, v100\n"
"v_mfma_f32_16x16x32_bf16 a[48:51], a[208:211], v[12:15], a[48:51]\n"
"v_exp_f32 v79, v167\n"
"v_mfma_f32_16x16x32_bf16 a[52:55], a[212:215], v[12:15], a[52:55]\n"
"ds_read_b128 a[152:155], v232 offset:5120\n"
"v_cvt_pk_bf16_f32 v39, v78, v79\n"
"v_mfma_f32_16x16x32_bf16 a[56:59], a[216:219], v[12:15], a[56:59]\n"
"v_exp_f32 v68, v168\n"
"v_mfma_f32_16x16x32_bf16 a[60:63], a[220:223], v[12:15], a[60:63]\n"
"ds_read_b128 a[156:159], v232 offset:5632\n"
"v_add_f32 v215, v215, v108\n"
"v_mfma_f32_16x16x32_bf16 a[64:67], a[192:195], v[20:23], a[64:67]\n"
"v_exp_f32 v69, v169\n"
"v_mfma_f32_16x16x32_bf16 a[68:71], a[196:199], v[20:23], a[68:71]\n"
"s_add_u32 m0, s50, 0xc000\n"
"v_cvt_pk_bf16_f32 v44, v68, v69\n"
"v_mfma_f32_16x16x32_bf16 a[72:75], a[200:203], v[20:23], a[72:75]\n"
"v_exp_f32 v70, v170\n"
"v_mfma_f32_16x16x32_bf16 a[76:79], a[204:207], v[20:23], a[76:79]\n"
"buffer_load_dwordx4 v236, s[20:23], s47 offen offset:0 lds\n"
"v_mfma_f32_16x16x32_bf16 a[80:83], a[208:211], v[20:23], a[80:83]\n"
"v_exp_f32 v71, v171\n"
"v_mfma_f32_16x16x32_bf16 a[84:87], a[212:215], v[20:23], a[84:87]\n"
"buffer_load_dwordx4 v237, s[20:23], s47 offen offset:1024 lds\n"
"v_cvt_pk_bf16_f32 v45, v70, v71\n"
"v_mfma_f32_16x16x32_bf16 a[88:91], a[216:219], v[20:23], a[88:91]\n"
"v_exp_f32 v76, v172\n"
"v_mfma_f32_16x16x32_bf16 a[92:95], a[220:223], v[20:23], a[92:95]\n"
"buffer_load_dwordx4 v238, s[20:23], s47 offen offset:2048 lds\n"
"v_mfma_f32_16x16x32_bf16 a[96:99], a[192:195], v[28:31], a[96:99]\n"
"v_exp_f32 v77, v173\n"
"v_mfma_f32_16x16x32_bf16 a[100:103], a[196:199], v[28:31], a[100:103]\n"
"buffer_load_dwordx4 v239, s[20:23], s47 offen offset:3072 lds\n"
"v_cvt_pk_bf16_f32 v46, v76, v77\n"
"v_mfma_f32_16x16x32_bf16 a[104:107], a[200:203], v[28:31], a[104:107]\n"
"v_exp_f32 v78, v174\n"
"v_mfma_f32_16x16x32_bf16 a[108:111], a[204:207], v[28:31], a[108:111]\n"
"s_add_u32 s47, s47, s55\n"
"v_mfma_f32_16x16x32_bf16 a[112:115], a[208:211], v[28:31], a[112:115]\n"
"v_exp_f32 v79, v175\n"
"v_mfma_f32_16x16x32_bf16 a[116:119], a[212:215], v[28:31], a[116:119]\n"
"v_cvt_pk_bf16_f32 v47, v78, v79\n"
"v_mfma_f32_16x16x32_bf16 a[120:123], a[216:219], v[28:31], a[120:123]\n"
"v_mfma_f32_16x16x32_bf16 a[124:127], a[220:223], v[28:31], a[124:127]\n"
"s_waitcnt lgkmcnt(0)\n"
"v_mfma_f32_16x16x32_bf16 v[128:131], a[128:131], v[0:3], v[192:195]\n"
"v_exp_f32 v68, v176\n"
"v_mfma_f32_16x16x32_bf16 v[100:103], v[116:119], v[36:39], 0\n"
"ds_read_b64_tr_b16 a[224:225], v233 offset:57344\n"
"ds_read_b64_tr_b16 a[226:227], v233 offset:61440\n"
"v_mfma_f32_16x16x32_bf16 v[128:131], a[132:135], v[8:11], v[128:131]\n"
"v_exp_f32 v69, v177\n"
"v_mfma_f32_16x16x32_bf16 v[108:111], v[116:119], v[44:47], 0\n"
"v_cvt_pk_bf16_f32 v52, v68, v69\n"
"v_mfma_f32_16x16x32_bf16 v[128:131], a[136:139], v[16:19], v[128:131]\n"
"v_exp_f32 v70, v178\n"
"v_mfma_f32_16x16x32_bf16 v[128:131], a[140:143], v[24:27], v[128:131]\n"
"ds_read_b64_tr_b16 a[228:229], v234 offset:57344\n"
"ds_read_b64_tr_b16 a[230:231], v234 offset:61440\n"
"v_mfma_f32_16x16x32_bf16 v[132:135], a[144:147], v[0:3], v[192:195]\n"
"v_exp_f32 v71, v179\n"
"v_mfma_f32_16x16x32_bf16 v[132:135], a[148:151], v[8:11], v[132:135]\n"
"ds_read_b64_tr_b16 a[232:233], v233 offset:57856\n"
"v_cvt_pk_bf16_f32 v53, v70, v71\n"
"v_mfma_f32_16x16x32_bf16 v[132:135], a[152:155], v[16:19], v[132:135]\n"
"v_exp_f32 v76, v180\n"
"v_mfma_f32_16x16x32_bf16 v[132:135], a[156:159], v[24:27], v[132:135]\n"
"v_max_f32 v216, v100, v108\n"
"v_cmp_lt_f32 vcc, s53, v216\n"
"v_mfma_f32_16x16x32_bf16 v[136:139], a[128:131], v[32:35], v[196:199]\n"
"v_exp_f32 v77, v181\n"
"v_mfma_f32_16x16x32_bf16 v[136:139], a[132:135], v[40:43], v[136:139]\n"
"s_cbranch_vccnz L_rare12_%=\n"
"L_ret12_%=:\n"
"v_cvt_pk_bf16_f32 v54, v76, v77\n"
"v_mfma_f32_16x16x32_bf16 v[136:139], a[136:139], v[48:51], v[136:139]\n"
"v_exp_f32 v78, v182\n"
"v_mfma_f32_16x16x32_bf16 v[136:139], a[140:143], v[56:59], v[136:139]\n"
"ds_read_b64_tr_b16 a[234:235], v233 offset:61952\n"
"v_add_f32 v212, v212, v100\n"
"v_mfma_f32_16x16x32_bf16 v[140:143], a[144:147], v[32:35], v[196:199]\n"
"v_exp_f32 v79, v183\n"
"v_mfma_f32_16x16x32_bf16 v[140:143], a[148:151], v[40:43], v[140:143]\n"
"ds_read_b64_tr_b16 a[236:237], v234 offset:57856\n"
"v_cvt_pk_bf16_f32 v55, v78, v79\n"
"v_mfma_f32_16x16x32_bf16 v[140:143], a[152:155], v[48:51], v[140:143]\n"
"v_exp_f32 v68, v184\n"
"v_mfma_f32_16x16x32_bf16 v[140:143], a[156:159], v[56:59], v[140:143]\n"
"ds_read_b64_tr_b16 a[238:239], v234 offset:61952\n"
"v_add_f32 v213, v213, v108\n"
"v_mfma_f32_16x16x32_bf16 v[144:147], a[128:131], v[64:67], v[200:203]\n"
"v_exp_f32 v69, v185\n"
"v_mfma_f32_16x16x32_bf16 v[144:147], a[132:135], v[72:75], v[144:147]\n"
"ds_read_b64_tr_b16 a[240:241], v233 offset:58368\n"
"v_cvt_pk_bf16_f32 v60, v68, v69\n"
"v_mfma_f32_16x16x32_bf16 v[144:147], a[136:139], v[80:83], v[144:147]\n"
"v_exp_f32 v70, v186\n"
"v_mfma_f32_16x16x32_bf16 v[144:147], a[140:143], v[88:91], v[144:147]\n"
"ds_read_b64_tr_b16 a[242:243], v233 offset:62464\n"
"ds_read_b64_tr_b16 a[244:245], v234 offset:58368\n"
"v_mfma_f32_16x16x32_bf16 v[148:151], a[144:147], v[64:67], v[200:203]\n"
"v_exp_f32 v71, v187\n"
"v_mfma_f32_16x16x32_bf16 v[148:151], a[148:151], v[72:75], v[148:151]\n"
"ds_read_b64_tr_b16 a[246:247], v234 offset:62464\n"
"v_cvt_pk_bf16_f32 v61, v70, v71\n"
"v_mfma_f32_16x16x32_bf16 v[148:151], a[152:155], v[80:83], v[148:151]\n"
"v_exp_f32 v76, v188\n"
"v_mfma_f32_16x16x32_bf16 v[148:151], a[156:159], v[88:91], v[148:151]\n"
"ds_read_b64_tr_b16 a[248:249], v233 offset:58880\n"
"ds_read_b64_tr_b16 a[250:251], v233 offset:62976\n"
"v_mfma_f32_16x16x32_bf16 v[152:155], a[128:131], v[96:99], v[204:207]\n"
"v_exp_f32 v77, v189\n"
"v_mfma_f32_16x16x32_bf16 v[152:155], a[132:135], v[104:107], v[152:155]\n"
"v_cvt_pk_bf16_f32 v62, v76, v77\n"
"v_mfma_f32_16x16x32_bf16 v[152:155], a[136:139], v[112:115], v[152:155]\n"
"v_exp_f32 v78, v190\n"
"v_mfma_f32_16x16x32_bf16 v[152:155], a[140:143], v[120:123], v[152:155]\n"
"ds_read_b64_tr_b16 a[252:253], v234 offset:58880\n"
"ds_read_b64_tr_b16 a[254:255], v234 offset:62976\n"
"v_mfma_f32_16x16x32_bf16 v[156:159], a[144:147], v[96:99], v[204:207]\n"
"v_exp_f32 v79, v191\n"
"v_mfma_f32_16x16x32_bf16 v[156:159], a[148:151], v[104:107], v[156:159]\n"
"v_cvt_pk_bf16_f32 v63, v78, v79\n"
"v_mfma_f32_16x16x32_bf16 v[156:159], a[152:155], v[112:115], v[156:159]\n"
"v_mfma_f32_16x16x32_bf16 v[156:159], a[156:159], v[120:123], v[156:159]\n"
"s_waitcnt lgkmcnt(8)\n"
"v_mfma_f32_16x16x32_bf16 a[0:3], a[224:227], v[36:39], a[0:3]\n"
"v_exp_f32 v68, v128\n"
"v_mfma_f32_16x16x32_bf16 v[100:103], v[116:119], v[52:55], 0\n"
"ds_read_b128 a[160:163], v232 offset:8192\n"
"ds_read_b128 a[164:167], v232 offset:8704\n"
"v_mfma_f32_16x16x32_bf16 a[4:7], a[228:231], v[36:39], a[4:7]\n"
"v_exp_f32 v69, v129\n"
"v_mfma_f32_16x16x32_bf16 v[108:111], v[116:119], v[60:63], 0\n"
"s_waitcnt lgkmcnt(2)\n"
"v_cvt_pk_bf16_f32 v4, v68, v69\n"
"v_mfma_f32_16x16x32_bf16 a[8:11], a[232:235], v[36:39], a[8:11]\n"
"v_exp_f32 v70, v130\n"
"v_mfma_f32_16x16x32_bf16 a[12:15], a[236:239], v[36:39], a[12:15]\n"
"ds_read_b128 a[168:171], v232 offset:9216\n"
"ds_read_b128 a[172:175], v232 offset:9728\n"
"v_mfma_f32_16x16x32_bf16 a[16:19], a[240:243], v[36:39], a[16:19]\n"
"v_exp_f32 v71, v131\n"
"v_mfma_f32_16x16x32_bf16 a[20:23], a[244:247], v[36:39], a[20:23]\n"
"ds_read_b128 a[176:179], v232 offset:12288\n"
"v_cvt_pk_bf16_f32 v5, v70, v71\n"
"v_mfma_f32_16x16x32_bf16 a[24:27], a[248:251], v[36:39], a[24:27]\n"
"v_exp_f32 v76, v132\n"
"v_mfma_f32_16x16x32_bf16 a[28:31], a[252:255], v[36:39], a[28:31]\n"
"v_max_f32 v216, v100, v108\n"
"v_cmp_lt_f32 vcc, s53, v216\n"
"v_mfma_f32_16x16x32_bf16 a[32:35], a[224:227], v[44:47], a[32:35]\n"
"v_exp_f32 v77, v133\n"
"v_mfma_f32_16x16x32_bf16 a[36:39], a[228:231], v[44:47], a[36:39]\n"
"s_cbranch_vccnz L_rare13_%=\n"
"L_ret13_%=:\n"
"v_cvt_pk_bf16_f32 v6, v76, v77\n"
"v_mfma_f32_16x16x32_bf16 a[40:43], a[232:235], v[44:47], a[40:43]\n"
"v_exp_f32 v78, v134\n"
"v_mfma_f32_16x16x32_bf16 a[44:47], a[236:239], v[44:47], a[44:47]\n"
"ds_read_b128 a[180:183], v232 offset:12800\n"
"v_add_f32 v214, v214, v100\n"
"v_mfma_f32_16x16x32_bf16 a[48:51], a[240:243], v[44:47], a[48:51]\n"
"v_exp_f32 v79, v135\n"
"v_mfma_f32_16x16x32_bf16 a[52:55], a[244:247], v[44:47], a[52:55]\n"
"ds_read_b128 a[184:187], v232 offset:13312\n"
"v_cvt_pk_bf16_f32 v7, v78, v79\n"
"v_mfma_f32_16x16x32_bf16 a[56:59], a[248:251], v[44:47], a[56:59]\n"
"v_exp_f32 v68, v136\n"
"v_mfma_f32_16x16x32_bf16 a[60:63], a[252:255], v[44:47], a[60:63]\n"
"ds_read_b128 a[188:191], v232 offset:13824\n"
"v_add_f32 v215, v215, v108\n"
"v_mfma_f32_16x16x32_bf16 a[64:67], a[224:227], v[52:55], a[64:67]\n"
"v_exp_f32 v69, v137\n"
"v_mfma_f32_16x16x32_bf16 a[68:71], a[228:231], v[52:55], a[68:71]\n"
"s_add_u32 m0, s51, 0x8000\n"
"v_cvt_pk_bf16_f32 v12, v68, v69\n"
"v_mfma_f32_16x16x32_bf16 a[72:75], a[232:235], v[52:55], a[72:75]\n"
"v_exp_f32 v70, v138\n"
"v_mfma_f32_16x16x32_bf16 a[76:79], a[236:239], v[52:55], a[76:79]\n"
"buffer_load_dwordx4 v240, s[24:27], s48 offen offset:0 lds\n"
"v_mfma_f32_16x16x32_bf16 a[80:83], a[240:243], v[52:55], a[80:83]\n"
"v_exp_f32 v71, v139\n"
"v_mfma_f32_16x16x32_bf16 a[84:87], a[244:247], v[52:55], a[84:87]\n"
"buffer_load_dwordx4 v241, s[24:27], s48 offen offset:1024 lds\n"
"v_cvt_pk_bf16_f32 v13, v70, v71\n"
"v_mfma_f32_16x16x32_bf16 a[88:91], a[248:251], v[52:55], a[88:91]\n"
"v_exp_f32 v76, v140\n"
"v_mfma_f32_16x16x32_bf16 a[92:95], a[252:255], v[52:55], a[92:95]\n"
"buffer_load_dwordx4 v217, s[24:27], s48 offen offset:2048 lds\n"
"v_mfma_f32_16x16x32_bf16 a[96:99], a[224:227], v[60:63], a[96:99]\n"
"v_exp_f32 v77, v141\n"
"v_mfma_f32_16x16x32_bf16 a[100:103], a[228:231], v[60:63], a[100:103]\n"
"buffer_load_dwordx4 v218, s[24:27], s48 offen offset:3072 lds\n"
"v_cvt_pk_bf16_f32 v14, v76, v77\n"
"v_mfma_f32_16x16x32_bf16 a[104:107], a[232:235], v[60:63], a[104:107]\n"
"v_exp_f32 v78, v142\n"
"v_mfma_f32_16x16x32_bf16 a[108:111], a[236:239], v[60:63], a[108:111]\n"
"s_add_u32 s48, s48, s61\n"
"v_mfma_f32_16x16x32_bf16 a[112:115], a[240:243], v[60:63], a[112:115]\n"
"v_exp_f32 v79, v143\n"
"v_mfma_f32_16x16x32_bf16 a[116:119], a[244:247], v[60:63], a[116:119]\n"
"v_cvt_pk_bf16_f32 v15, v78, v79\n"
"v_mfma_f32_16x16x32_bf16 a[120:123], a[248:251], v[60:63], a[120:123]\n"
"v_mfma_f32_16x16x32_bf16 a[124:127], a[252:255], v[60:63], a[124:127]\n"
"s_waitcnt vmcnt(16) lgkmcnt(0)\n"
"s_barrier\n"
"s_cmp_eq_u32 s52, 0\n"
"s_cbranch_scc1 L_epi_%=\n"
"s_sub_u32 s52, s52, 1\n"
"v_mfma_f32_16x16x32_bf16 v[160:163], a[160:163], v[0:3], v[192:195]\n"
"v_exp_f32 v68, v144\n"
"v_mfma_f32_16x16x32_bf16 v[100:103], v[116:119], v[4:7], 0\n"
"ds_read_b64_tr_b16 a[192:193], v233 offset:0\n"
"ds_read_b64_tr_b16 a[194:195], v233 offset:4096\n"
"v_mfma_f32_16x16x32_bf16 v[160:163], a[164:167], v[8:11], v[160:163]\n"
"v_exp_f32 v69, v145\n"
"v_mfma_f32_16x16x32_bf16 v[108:111], v[116:119], v[12:15], 0\n"
"v_cvt_pk_bf16_f32 v20, v68, v69\n"
"v_mfma_f32_16x16x32_bf16 v[160:163], a[168:171], v[16:19], v[160:163]\n"
"v_exp_f32 v70, v146\n"
"v_mfma_f32_16x16x32_bf16 v[160:163], a[172:175], v[24:27], v[160:163]\n"
"ds_read_b64_tr_b16 a[196:197], v234 offset:0\n"
"ds_read_b64_tr_b16 a[198:199], v234 offset:4096\n"
"v_mfma_f32_16x16x32_bf16 v[164:167], a[176:179], v[0:3], v[192:195]\n"
"v_exp_f32 v71, v147\n"
"v_mfma_f32_16x16x32_bf16 v[164:167], a[180:183], v[8:11], v[164:167]\n"
"ds_read_b64_tr_b16 a[200:201], v233 offset:512\n"
"v_cvt_pk_bf16_f32 v21, v70, v71\n"
"v_mfma_f32_16x16x32_bf16 v[164:167], a[184:187], v[16:19], v[164:167]\n"
"v_exp_f32 v76, v148\n"
"v_mfma_f32_16x16x32_bf16 v[164:167], a[188:191], v[24:27], v[164:167]\n"
"v_max_f32 v216, v100, v108\n"
"v_cmp_lt_f32 vcc, s53, v216\n"
"v_mfma_f32_16x16x32_bf16 v[168:171], a[160:163], v[32:35], v[196:199]\n"
"v_exp_f32 v77, v149\n"
"v_mfma_f32_16x16x32_bf16 v[168:171], a[164:167], v[40:43], v[168:171]\n"
"s_cbranch_vccnz L_rare14_%=\n"
"L_ret14_%=:\n"
"v_cvt_pk_bf16_f32 v22, v76, v77\n"
"v_mfma_f32_16x16x32_bf16 v[168:171], a[168:171], v[48:51], v[168:171]\n"
"v_exp_f32 v78, v150\n"
"v_mfma_f32_16x16x32_bf16 v[168:171], a[172:175], v[56:59], v[168:171]\n"
"ds_read_b64_tr_b16 a[202:203], v233 offset:4608\n"
"v_add_f32 v212, v212, v100\n"
"v_mfma_f32_16x16x32_bf16 v[172:175], a[176:179], v[32:35], v[196:199]\n"
"v_exp_f32 v79, v151\n"
"v_mfma_f32_16x16x32_bf16 v[172:175], a[180:183], v[40:43], v[172:175]\n"
"ds_read_b64_tr_b16 a[204:205], v234 offset:512\n"
"v_cvt_pk_bf16_f32 v23, v78, v79\n"
"v_mfma_f32_16x16x32_bf16 v[172:175], a[184:187], v[48:51], v[172:175]\n"
"v_exp_f32 v68, v152\n"
"v_mfma_f32_16x16x32_bf16 v[172:175], a[188:191], v[56:59], v[172:175]\n"
"ds_read_b64_tr_b16 a[206:207], v234 offset:4608\n"
"v_add_f32 v213, v213, v108\n"
"v_mfma_f32_16x16x32_bf16 v[176:179], a[160:163], v[64:67], v[200:203]\n"
"v_exp_f32 v69, v153\n"
"v_mfma_f32_16x16x32_bf16 v[176:179], a[164:167], v[72:75], v[176:179]\n"
"ds_read_b64_tr_b16 a[208:209], v233 offset:1024\n"
"v_cvt_pk_bf16_f32 v28, v68, v69\n"
"v_mfma_f32_16x16x32_bf16 v[176:179], a[168:171], v[80:83], v[176:179]\n"
"v_exp_f32 v70, v154\n"
"v_mfma_f32_16x16x32_bf16 v[176:179], a[172:175], v[88:91], v[176:179]\n"
"ds_read_b64_tr_b16 a[210:211], v233 offset:5120\n"
"ds_read_b64_tr_b16 a[212:213], v234 offset:1024\n"
"v_mfma_f32_16x16x32_bf16 v[180:183], a[176:179], v[64:67], v[200:203]\n"
"v_exp_f32 v71, v155\n"
"v_mfma_f32_16x16x32_bf16 v[180:183], a[180:183], v[72:75], v[180:183]\n"
"ds_read_b64_tr_b16 a[214:215], v234 offset:5120\n"
"v_cvt_pk_bf16_f32 v29, v70, v71\n"
"v_mfma_f32_16x16x32_bf16 v[180:183], a[184:187], v[80:83], v[180:183]\n"
"v_exp_f32 v76, v156\n"
"v_mfma_f32_16x16x32_bf16 v[180:183], a[188:191], v[88:91], v[180:183]\n"
"ds_read_b64_tr_b16 a[216:217], v233 offset:1536\n"
"ds_read_b64_tr_b16 a[218:219], v233 offset:5632\n"
"v_mfma_f32_16x16x32_bf16 v[184:187], a[160:163], v[96:99], v[204:207]\n"
"v_exp_f32 v77, v157\n"
"v_mfma_f32_16x16x32_bf16 v[184:187], a[164:167], v[104:107], v[184:187]\n"
"v_cvt_pk_bf16_f32 v30, v76, v77\n"
"v_mfma_f32_16x16x32_bf16 v[184:187], a[168:171], v[112:115], v[184:187]\n"
"v_exp_f32 v78, v158\n"
"v_mfma_f32_16x16x32_bf16 v[184:187], a[172:175], v[120:123], v[184:187]\n"
"ds_read_b64_tr_b16 a[220:221], v234 offset:1536\n"
"ds_read_b64_tr_b16 a[222:223], v234 offset:5632\n"
"v_mfma_f32_16x16x32_bf16 v[188:191], a[176:179], v[96:99], v[204:207]\n"
"v_exp_f32 v79, v159\n"
"v_mfma_f32_16x16x32_bf16 v[188:191], a[180:183], v[104:107], v[188:191]\n"
"s_cmp_eq_u32 s52, 0\n"
"s_cbranch_scc1 L_swk_%=\n"
"L_swkr_%=:\n"
"v_cvt_pk_bf16_f32 v31, v78, v79\n"
"v_mfma_f32_16x16x32_bf16 v[188:191], a[184:187], v[112:115], v[188:191]\n"
"v_mfma_f32_16x16x32_bf16 v[188:191], a[188:191], v[120:123], v[188:191]\n"
"s_waitcnt lgkmcnt(8)\n"
"v_mfma_f32_16x16x32_bf16 a[0:3], a[192:195], v[4:7], a[0:3]\n"
"v_exp_f32 v68, v160\n"
"v_mfma_f32_16x16x32_bf16 v[100:103], v[116:119], v[20:23], 0\n"
"ds_read_b128 a[128:131], v232 offset:16384\n"
"ds_read_b128 a[132:135], v232 offset:16896\n"
"v_mfma_f32_16x16x32_bf16 a[4:7], a[196:199], v[4:7], a[4:7]\n"
"v_exp_f32 v69, v161\n"
"v_mfma_f32_16x16x32_bf16 v[108:111], v[116:119], v[28:31], 0\n"
"s_waitcnt lgkmcnt(2)\n"
"v_cvt_pk_bf16_f32 v36, v68, v69\n"
"v_mfma_f32_16x16x32_bf16 a[8:11], a[200:203], v[4:7], a[8:11]\n"
"v_exp_f32 v70, v162\n"
"v_mfma_f32_16x16x32_bf16 a[12:15], a[204:207], v[4:7], a[12:15]\n"
"ds_read_b128 a[136:139], v232 offset:17408\n"
"ds_read_b128 a[140:143], v232 offset:17920\n"
"v_mfma_f32_16x16x32_bf16 a[16:19], a[208:211], v[4:7], a[16:19]\n"
"v_exp_f32 v71, v163\n"
"v_mfma_f32_16x16x32_bf16 a[20:23], a[212:215], v[4:7], a[20:23]\n"
"ds_read_b128 a[144:147], v232 offset:20480\n"
"v_cvt_pk_bf16_f32 v37, v70, v71\n"
"v_mfma_f32_16x16x32_bf16 a[24:27], a[216:219], v[4:7], a[24:27]\n"
"v_exp_f32 v76, v164\n"
"v_mfma_f32_16x16x32_bf16 a[28:31], a[220:223], v[4:7], a[28:31]\n"
"v_max_f32 v216, v100, v108\n"
"v_cmp_lt_f32 vcc, s53, v216\n"
"v_mfma_f32_16x16x32_bf16 a[32:35], a[192:195], v[12:15], a[32:35]\n"
"v_exp_f32 v77, v165\n"
"v_mfma_f32_16x16x32_bf16 a[36:39], a[196:199], v[12:15], a[36:39]\n"
"s_cbranch_vccnz L_rare15_%=\n"
"L_ret15_%=:\n"
"v_cvt_pk_bf16_f32 v38, v76, v77\n"
"v_mfma_f32_16x16x32_bf16 a[40:43], a[200:203], v[12:15], a[40:43]\n"
"v_exp_f32 v78, v166\n"
"v_mfma_f32_16x16x32_bf16 a[44:47], a[204:207], v[12:15], a[44:47]\n"
"ds_read_b128 a[148:151], v232 offset:20992\n"
"v_add_f32 v214, v214, v100\n"
"v_mfma_f32_16x16x32_bf16 a[48:51], a[208:211], v[12:15], a[48:51]\n"
"v_exp_f32 v79, v167\n"
"v_mfma_f32_16x16x32_bf16 a[52:55], a[212:215], v[12:15], a[52:55]\n"
"ds_read_b128 a[152:155], v232 offset:21504\n"
"v_cvt_pk_bf16_f32 v39, v78, v79\n"
"v_mfma_f32_16x16x32_bf16 a[56:59], a[216:219], v[12:15], a[56:59]\n"
"v_exp_f32 v68, v168\n"
"v_mfma_f32_16x16x32_bf16 a[60:63], a[220:223], v[12:15], a[60:63]\n"
"ds_read_b128 a[156:159], v232 offset:22016\n"
"v_add_f32 v215, v215, v108\n"
"v_mfma_f32_16x16x32_bf16 a[64:67], a[192:195], v[20:23], a[64:67]\n"
"v_exp_f32 v69, v169\n"
"v_mfma_f32_16x16x32_bf16 a[68:71], a[196:199], v[20:23], a[68:71]\n"
"s_add_u32 m0, s50, 0\n"
"v_cvt_pk_bf16_f32 v44, v68, v69\n"
"v_mfma_f32_16x16x32_bf16 a[72:75], a[200:203], v[20:23], a[72:75]\n"
"v_exp_f32 v70, v170\n"
"v_mfma_f32_16x16x32_bf16 a[76:79], a[204:207], v[20:23], a[76:79]\n"
"buffer_load_dwordx4 v236, s[20:23], s47 offen offset:0 lds\n"
"v_mfma_f32_16x16x32_bf16 a[80:83], a[208:211], v[20:23], a[80:83]\n"
"v_exp_f32 v71, v171\n"
"v_mfma_f32_16x16x32_bf16 a[84:87], a[212:215], v[20:23], a[84:87]\n"
"buffer_load_dwordx4 v237, s[20:23], s47 offen offset:1024 lds\n"
"v_cvt_pk_bf16_f32 v45, v70, v71\n"
"v_mfma_f32_16x16x32_bf16 a[88:91], a[216:219], v[20:23], a[88:91]\n"
"v_exp_f32 v76, v172\n"
"v_mfma_f32_16x16x32_bf16 a[92:95], a[220:223], v[20:23], a[92:95]\n"
"buffer_load_dwordx4 v238, s[20:23], s47 offen offset:2048 lds\n"
"v_mfma_f32_16x16x32_bf16 a[96:99], a[192:195], v[28:31], a[96:99]\n"
"v_exp_f32 v77, v173\n"
"v_mfma_f32_16x16x32_bf16 a[100:103], a[196:199], v[28:31], a[100:103]\n"
"buffer_load_dwordx4 v239, s[20:23], s47 offen offset:3072 lds\n"
"v_cvt_pk_bf16_f32 v46, v76, v77\n"
"v_mfma_f32_16x16x32_bf16 a[104:107], a[200:203], v[28:31], a[104:107]\n"
"v_exp_f32 v78, v174\n"
"v_mfma_f32_16x16x32_bf16 a[108:111], a[204:207], v[28:31], a[108:111]\n"
"s_add_u32 s47, s47, s55\n"
"v_mfma_f32_16x16x32_bf16 a[112:115], a[208:211], v[28:31], a[112:115]\n"
"v_exp_f32 v79, v175\n"
"v_mfma_f32_16x16x32_bf16 a[116:119], a[212:215], v[28:31], a[116:119]\n"
"v_cvt_pk_bf16_f32 v47, v78, v79\n"
"v_mfma_f32_16x16x32_bf16 a[120:123], a[216:219], v[28:31], a[120:123]\n"
"v_mfma_f32_16x16x32_bf16 a[124:127], a[220:223], v[28:31], a[124:127]\n"
"s_waitcnt lgkmcnt(0)\n"
"s_branch L_loop_%=\n"
"L_swv_%=:\n"
"v_mov_b32 v240, v242\n"
"v_mov_b32 v241, v243\n"
"v_mov_b32 v217, v244\n"
"v_mov_b32 v218, v245\n"
"s_mov_b32 s24, s64\n"
"s_mov_b32 s25, s65\n"
"s_mov_b32 s26, s66\n"
"s_add_u32 s48, s49, 0x40000\n"
"s_mov_b32 s61, 0x10000\n"
"s_branch L_swvr_%=\n"
"L_swk_%=:\n"
"v_mov_b32 v236, v242\n"
"v_mov_b32 v237, v243\n"
"v_mov_b32 v238, v244\n"
"v_mov_b32 v239, v245\n"
"s_mov_b32 s20, s64\n"
"s_mov_b32 s21, s65\n"
"s_mov_b32 s22, s66\n"
"s_add_u32 s47, s49, 0\n"
"s_mov_b32 s55, 0x10000\n"
"v_lshrrev_b32 v225, 4, v246\n"
"s_getpc_b64 s[28:29]\n"
".Lcp_at:\n"
"s_and_b32 s29, s29, 0xffff\n"
"s_mov_b32 s30, .Lkm_end-.Lcp_at\n"
"s_mov_b32 s31, 0x20000\n"
"s_lshr_b32 s59, s16, 3\n"
"s_lshl_b32 s59, s59, 2\n"
"s_add_u32 s59, s59, s44\n"
"s_lshl_b32 s59, s59, 10\n"
"v_lshlrev_b32 v224, 4, v225\n"
"v_add_u32 v224, s59, v224\n"
"buffer_load_dwordx4 v[220:223], v224, s[28:31], 0 offen offset:0\n"
"s_branch L_swkr_%=\n"
"L_epi_%=:\n"
"s_nop 7\n"
"v_rcp_f32 v224, v212\n"
"v_rcp_f32 v225, v213\n"
"v_rcp_f32 v226, v214\n"
"v_rcp_f32 v227, v215\n"
"s_lshl_b32 s57, s45, 12\n"
"s_add_u32 s58, s57, 0\n"
"v_accvgpr_read_b32 v128, a0\n"
"v_accvgpr_read_b32 v129, a1\n"
"v_accvgpr_read_b32 v130, a2\n"
"v_accvgpr_read_b32 v131, a3\n"
"v_accvgpr_read_b32 v132, a4\n"
"v_accvgpr_read_b32 v133, a5\n"
"v_accvgpr_read_b32 v134, a6\n"
"v_accvgpr_read_b32 v135, a7\n"
"v_accvgpr_read_b32 v136, a8\n"
"v_accvgpr_read_b32 v137, a9\n"
"v_accvgpr_read_b32 v138, a10\n"
"v_accvgpr_read_b32 v139, a11\n"
"v_accvgpr_read_b32 v140, a12\n"
"v_accvgpr_read_b32 v141, a13\n"
"v_accvgpr_read_b32 v142, a14\n"
"v_accvgpr_read_b32 v143, a15\n"
"v_mul_f32 v128, v128, v224\n"
"v_mul_f32 v129, v129, v224\n"
"v_mul_f32 v130, v130, v224\n"
"v_mul_f32 v131, v131, v224\n"
"v_mul_f32 v132, v132, v224\n"
"v_mul_f32 v133, v133, v224\n"
"v_mul_f32 v134, v134, v224\n"
"v_mul_f32 v135, v135, v224\n"
"v_mul_f32 v136, v136, v224\n"
"v_mul_f32 v137, v137, v224\n"
"v_mul_f32 v138, v138, v224\n"
"v_mul_f32 v139, v139, v224\n"
"v_mul_f32 v140, v140, v224\n"
"v_mul_f32 v141, v141, v224\n"
"v_mul_f32 v142, v142, v224\n"
"v_mul_f32 v143, v143, v224\n"
"v_cvt_pk_bf16_f32 v64, v128, v129\n"
"v_cvt_pk_bf16_f32 v65, v130, v131\n"
"v_cvt_pk_bf16_f32 v66, v132, v133\n"
"v_cvt_pk_bf16_f32 v67, v134, v135\n"
"v_cvt_pk_bf16_f32 v68, v136, v137\n"
"v_cvt_pk_bf16_f32 v69, v138, v139\n"
"v_cvt_pk_bf16_f32 v70, v140, v141\n"
"v_cvt_pk_bf16_f32 v71, v142, v143\n"
"buffer_store_dwordx4 v[64:67], v246, s[36:39], s58 offen offset:0 sc1\n"
"buffer_store_dwordx4 v[68:71], v246, s[36:39], s58 offen offset:1024 sc1\n"
"v_accvgpr_read_b32 v144, a16\n"
"v_accvgpr_read_b32 v145, a17\n"
"v_accvgpr_read_b32 v146, a18\n"
"v_accvgpr_read_b32 v147, a19\n"
"v_accvgpr_read_b32 v148, a20\n"
"v_accvgpr_read_b32 v149, a21\n"
"v_accvgpr_read_b32 v150, a22\n"
"v_accvgpr_read_b32 v151, a23\n"
"v_accvgpr_read_b32 v152, a24\n"
"v_accvgpr_read_b32 v153, a25\n"
"v_accvgpr_read_b32 v154, a26\n"
"v_accvgpr_read_b32 v155, a27\n"
"v_accvgpr_read_b32 v156, a28\n"
"v_accvgpr_read_b32 v157, a29\n"
"v_accvgpr_read_b32 v158, a30\n"
"v_accvgpr_read_b32 v159, a31\n"
"v_mul_f32 v144, v144, v224\n"
"v_mul_f32 v145, v145, v224\n"
"v_mul_f32 v146, v146, v224\n"
"v_mul_f32 v147, v147, v224\n"
"v_mul_f32 v148, v148, v224\n"
"v_mul_f32 v149, v149, v224\n"
"v_mul_f32 v150, v150, v224\n"
"v_mul_f32 v151, v151, v224\n"
"v_mul_f32 v152, v152, v224\n"
"v_mul_f32 v153, v153, v224\n"
"v_mul_f32 v154, v154, v224\n"
"v_mul_f32 v155, v155, v224\n"
"v_mul_f32 v156, v156, v224\n"
"v_mul_f32 v157, v157, v224\n"
"v_mul_f32 v158, v158, v224\n"
"v_mul_f32 v159, v159, v224\n"
"v_cvt_pk_bf16_f32 v72, v144, v145\n"
"v_cvt_pk_bf16_f32 v73, v146, v147\n"
"v_cvt_pk_bf16_f32 v74, v148, v149\n"
"v_cvt_pk_bf16_f32 v75, v150, v151\n"
"v_cvt_pk_bf16_f32 v76, v152, v153\n"
"v_cvt_pk_bf16_f32 v77, v154, v155\n"
"v_cvt_pk_bf16_f32 v78, v156, v157\n"
"v_cvt_pk_bf16_f32 v79, v158, v159\n"
"buffer_store_dwordx4 v[72:75], v246, s[36:39], s58 offen offset:2048 sc1\n"
"buffer_store_dwordx4 v[76:79], v246, s[36:39], s58 offen offset:3072 sc1\n"
"s_add_u32 s58, s57, 0x20000\n"
"v_accvgpr_read_b32 v160, a32\n"
"v_accvgpr_read_b32 v161, a33\n"
"v_accvgpr_read_b32 v162, a34\n"
"v_accvgpr_read_b32 v163, a35\n"
"v_accvgpr_read_b32 v164, a36\n"
"v_accvgpr_read_b32 v165, a37\n"
"v_accvgpr_read_b32 v166, a38\n"
"v_accvgpr_read_b32 v167, a39\n"
"v_accvgpr_read_b32 v168, a40\n"
"v_accvgpr_read_b32 v169, a41\n"
"v_accvgpr_read_b32 v170, a42\n"
"v_accvgpr_read_b32 v171, a43\n"
"v_accvgpr_read_b32 v172, a44\n"
"v_accvgpr_read_b32 v173, a45\n"
"v_accvgpr_read_b32 v174, a46\n"
"v_accvgpr_read_b32 v175, a47\n"
"v_mul_f32 v160, v160, v225\n"
"v_mul_f32 v161, v161, v225\n"
"v_mul_f32 v162, v162, v225\n"
"v_mul_f32 v163, v163, v225\n"
"v_mul_f32 v164, v164, v225\n"
"v_mul_f32 v165, v165, v225\n"
"v_mul_f32 v166, v166, v225\n"
"v_mul_f32 v167, v167, v225\n"
"v_mul_f32 v168, v168, v225\n"
"v_mul_f32 v169, v169, v225\n"
"v_mul_f32 v170, v170, v225\n"
"v_mul_f32 v171, v171, v225\n"
"v_mul_f32 v172, v172, v225\n"
"v_mul_f32 v173, v173, v225\n"
"v_mul_f32 v174, v174, v225\n"
"v_mul_f32 v175, v175, v225\n"
"v_cvt_pk_bf16_f32 v80, v160, v161\n"
"v_cvt_pk_bf16_f32 v81, v162, v163\n"
"v_cvt_pk_bf16_f32 v82, v164, v165\n"
"v_cvt_pk_bf16_f32 v83, v166, v167\n"
"v_cvt_pk_bf16_f32 v84, v168, v169\n"
"v_cvt_pk_bf16_f32 v85, v170, v171\n"
"v_cvt_pk_bf16_f32 v86, v172, v173\n"
"v_cvt_pk_bf16_f32 v87, v174, v175\n"
"buffer_store_dwordx4 v[80:83], v246, s[36:39], s58 offen offset:0 sc1\n"
"buffer_store_dwordx4 v[84:87], v246, s[36:39], s58 offen offset:1024 sc1\n"
"v_accvgpr_read_b32 v176, a48\n"
"v_accvgpr_read_b32 v177, a49\n"
"v_accvgpr_read_b32 v178, a50\n"
"v_accvgpr_read_b32 v179, a51\n"
"v_accvgpr_read_b32 v180, a52\n"
"v_accvgpr_read_b32 v181, a53\n"
"v_accvgpr_read_b32 v182, a54\n"
"v_accvgpr_read_b32 v183, a55\n"
"v_accvgpr_read_b32 v184, a56\n"
"v_accvgpr_read_b32 v185, a57\n"
"v_accvgpr_read_b32 v186, a58\n"
"v_accvgpr_read_b32 v187, a59\n"
"v_accvgpr_read_b32 v188, a60\n"
"v_accvgpr_read_b32 v189, a61\n"
"v_accvgpr_read_b32 v190, a62\n"
"v_accvgpr_read_b32 v191, a63\n"
"v_mul_f32 v176, v176, v225\n"
"v_mul_f32 v177, v177, v225\n"
"v_mul_f32 v178, v178, v225\n"
"v_mul_f32 v179, v179, v225\n"
"v_mul_f32 v180, v180, v225\n"
"v_mul_f32 v181, v181, v225\n"
"v_mul_f32 v182, v182, v225\n"
"v_mul_f32 v183, v183, v225\n"
"v_mul_f32 v184, v184, v225\n"
"v_mul_f32 v185, v185, v225\n"
"v_mul_f32 v186, v186, v225\n"
"v_mul_f32 v187, v187, v225\n"
"v_mul_f32 v188, v188, v225\n"
"v_mul_f32 v189, v189, v225\n"
"v_mul_f32 v190, v190, v225\n"
"v_mul_f32 v191, v191, v225\n"
"v_cvt_pk_bf16_f32 v88, v176, v177\n"
"v_cvt_pk_bf16_f32 v89, v178, v179\n"
"v_cvt_pk_bf16_f32 v90, v180, v181\n"
"v_cvt_pk_bf16_f32 v91, v182, v183\n"
"v_cvt_pk_bf16_f32 v92, v184, v185\n"
"v_cvt_pk_bf16_f32 v93, v186, v187\n"
"v_cvt_pk_bf16_f32 v94, v188, v189\n"
"v_cvt_pk_bf16_f32 v95, v190, v191\n"
"buffer_store_dwordx4 v[88:91], v246, s[36:39], s58 offen offset:2048 sc1\n"
"buffer_store_dwordx4 v[92:95], v246, s[36:39], s58 offen offset:3072 sc1\n"
"s_add_u32 s58, s57, 0x40000\n"
"v_accvgpr_read_b32 v128, a64\n"
"v_accvgpr_read_b32 v129, a65\n"
"v_accvgpr_read_b32 v130, a66\n"
"v_accvgpr_read_b32 v131, a67\n"
"v_accvgpr_read_b32 v132, a68\n"
"v_accvgpr_read_b32 v133, a69\n"
"v_accvgpr_read_b32 v134, a70\n"
"v_accvgpr_read_b32 v135, a71\n"
"v_accvgpr_read_b32 v136, a72\n"
"v_accvgpr_read_b32 v137, a73\n"
"v_accvgpr_read_b32 v138, a74\n"
"v_accvgpr_read_b32 v139, a75\n"
"v_accvgpr_read_b32 v140, a76\n"
"v_accvgpr_read_b32 v141, a77\n"
"v_accvgpr_read_b32 v142, a78\n"
"v_accvgpr_read_b32 v143, a79\n"
"v_mul_f32 v128, v128, v226\n"
"v_mul_f32 v129, v129, v226\n"
"v_mul_f32 v130, v130, v226\n"
"v_mul_f32 v131, v131, v226\n"
"v_mul_f32 v132, v132, v226\n"
"v_mul_f32 v133, v133, v226\n"
"v_mul_f32 v134, v134, v226\n"
"v_mul_f32 v135, v135, v226\n"
"v_mul_f32 v136, v136, v226\n"
"v_mul_f32 v137, v137, v226\n"
"v_mul_f32 v138, v138, v226\n"
"v_mul_f32 v139, v139, v226\n"
"v_mul_f32 v140, v140, v226\n"
"v_mul_f32 v141, v141, v226\n"
"v_mul_f32 v142, v142, v226\n"
"v_mul_f32 v143, v143, v226\n"
"v_cvt_pk_bf16_f32 v64, v128, v129\n"
"v_cvt_pk_bf16_f32 v65, v130, v131\n"
"v_cvt_pk_bf16_f32 v66, v132, v133\n"
"v_cvt_pk_bf16_f32 v67, v134, v135\n"
"v_cvt_pk_bf16_f32 v68, v136, v137\n"
"v_cvt_pk_bf16_f32 v69, v138, v139\n"
"v_cvt_pk_bf16_f32 v70, v140, v141\n"
"v_cvt_pk_bf16_f32 v71, v142, v143\n"
"buffer_store_dwordx4 v[64:67], v246, s[36:39], s58 offen offset:0 sc1\n"
"buffer_store_dwordx4 v[68:71], v246, s[36:39], s58 offen offset:1024 sc1\n"
"v_accvgpr_read_b32 v144, a80\n"
"v_accvgpr_read_b32 v145, a81\n"
"v_accvgpr_read_b32 v146, a82\n"
"v_accvgpr_read_b32 v147, a83\n"
"v_accvgpr_read_b32 v148, a84\n"
"v_accvgpr_read_b32 v149, a85\n"
"v_accvgpr_read_b32 v150, a86\n"
"v_accvgpr_read_b32 v151, a87\n"
"v_accvgpr_read_b32 v152, a88\n"
"v_accvgpr_read_b32 v153, a89\n"
"v_accvgpr_read_b32 v154, a90\n"
"v_accvgpr_read_b32 v155, a91\n"
"v_accvgpr_read_b32 v156, a92\n"
"v_accvgpr_read_b32 v157, a93\n"
"v_accvgpr_read_b32 v158, a94\n"
"v_accvgpr_read_b32 v159, a95\n"
"v_mul_f32 v144, v144, v226\n"
"v_mul_f32 v145, v145, v226\n"
"v_mul_f32 v146, v146, v226\n"
"v_mul_f32 v147, v147, v226\n"
"v_mul_f32 v148, v148, v226\n"
"v_mul_f32 v149, v149, v226\n"
"v_mul_f32 v150, v150, v226\n"
"v_mul_f32 v151, v151, v226\n"
"v_mul_f32 v152, v152, v226\n"
"v_mul_f32 v153, v153, v226\n"
"v_mul_f32 v154, v154, v226\n"
"v_mul_f32 v155, v155, v226\n"
"v_mul_f32 v156, v156, v226\n"
"v_mul_f32 v157, v157, v226\n"
"v_mul_f32 v158, v158, v226\n"
"v_mul_f32 v159, v159, v226\n"
"v_cvt_pk_bf16_f32 v72, v144, v145\n"
"v_cvt_pk_bf16_f32 v73, v146, v147\n"
"v_cvt_pk_bf16_f32 v74, v148, v149\n"
"v_cvt_pk_bf16_f32 v75, v150, v151\n"
"v_cvt_pk_bf16_f32 v76, v152, v153\n"
"v_cvt_pk_bf16_f32 v77, v154, v155\n"
"v_cvt_pk_bf16_f32 v78, v156, v157\n"
"v_cvt_pk_bf16_f32 v79, v158, v159\n"
"buffer_store_dwordx4 v[72:75], v246, s[36:39], s58 offen offset:2048 sc1\n"
"buffer_store_dwordx4 v[76:79], v246, s[36:39], s58 offen offset:3072 sc1\n"
"s_add_u32 s58, s57, 0x60000\n"
"v_accvgpr_read_b32 v160, a96\n"
"v_accvgpr_read_b32 v161, a97\n"
"v_accvgpr_read_b32 v162, a98\n"
"v_accvgpr_read_b32 v163, a99\n"
"v_accvgpr_read_b32 v164, a100\n"
"v_accvgpr_read_b32 v165, a101\n"
"v_accvgpr_read_b32 v166, a102\n"
"v_accvgpr_read_b32 v167, a103\n"
"v_accvgpr_read_b32 v168, a104\n"
"v_accvgpr_read_b32 v169, a105\n"
"v_accvgpr_read_b32 v170, a106\n"
"v_accvgpr_read_b32 v171, a107\n"
"v_accvgpr_read_b32 v172, a108\n"
"v_accvgpr_read_b32 v173, a109\n"
"v_accvgpr_read_b32 v174, a110\n"
"v_accvgpr_read_b32 v175, a111\n"
"v_mul_f32 v160, v160, v227\n"
"v_mul_f32 v161, v161, v227\n"
"v_mul_f32 v162, v162, v227\n"
"v_mul_f32 v163, v163, v227\n"
"v_mul_f32 v164, v164, v227\n"
"v_mul_f32 v165, v165, v227\n"
"v_mul_f32 v166, v166, v227\n"
"v_mul_f32 v167, v167, v227\n"
"v_mul_f32 v168, v168, v227\n"
"v_mul_f32 v169, v169, v227\n"
"v_mul_f32 v170, v170, v227\n"
"v_mul_f32 v171, v171, v227\n"
"v_mul_f32 v172, v172, v227\n"
"v_mul_f32 v173, v173, v227\n"
"v_mul_f32 v174, v174, v227\n"
"v_mul_f32 v175, v175, v227\n"
"v_cvt_pk_bf16_f32 v80, v160, v161\n"
"v_cvt_pk_bf16_f32 v81, v162, v163\n"
"v_cvt_pk_bf16_f32 v82, v164, v165\n"
"v_cvt_pk_bf16_f32 v83, v166, v167\n"
"v_cvt_pk_bf16_f32 v84, v168, v169\n"
"v_cvt_pk_bf16_f32 v85, v170, v171\n"
"v_cvt_pk_bf16_f32 v86, v172, v173\n"
"v_cvt_pk_bf16_f32 v87, v174, v175\n"
"buffer_store_dwordx4 v[80:83], v246, s[36:39], s58 offen offset:0 sc1\n"
"buffer_store_dwordx4 v[84:87], v246, s[36:39], s58 offen offset:1024 sc1\n"
"v_accvgpr_read_b32 v176, a112\n"
"v_accvgpr_read_b32 v177, a113\n"
"v_accvgpr_read_b32 v178, a114\n"
"v_accvgpr_read_b32 v179, a115\n"
"v_accvgpr_read_b32 v180, a116\n"
"v_accvgpr_read_b32 v181, a117\n"
"v_accvgpr_read_b32 v182, a118\n"
"v_accvgpr_read_b32 v183, a119\n"
"v_accvgpr_read_b32 v184, a120\n"
"v_accvgpr_read_b32 v185, a121\n"
"v_accvgpr_read_b32 v186, a122\n"
"v_accvgpr_read_b32 v187, a123\n"
"v_accvgpr_read_b32 v188, a124\n"
"v_accvgpr_read_b32 v189, a125\n"
"v_accvgpr_read_b32 v190, a126\n"
"v_accvgpr_read_b32 v191, a127\n"
"v_mul_f32 v176, v176, v227\n"
"v_mul_f32 v177, v177, v227\n"
"v_mul_f32 v178, v178, v227\n"
"v_mul_f32 v179, v179, v227\n"
"v_mul_f32 v180, v180, v227\n"
"v_mul_f32 v181, v181, v227\n"
"v_mul_f32 v182, v182, v227\n"
"v_mul_f32 v183, v183, v227\n"
"v_mul_f32 v184, v184, v227\n"
"v_mul_f32 v185, v185, v227\n"
"v_mul_f32 v186, v186, v227\n"
"v_mul_f32 v187, v187, v227\n"
"v_mul_f32 v188, v188, v227\n"
"v_mul_f32 v189, v189, v227\n"
"v_mul_f32 v190, v190, v227\n"
"v_mul_f32 v191, v191, v227\n"
"v_cvt_pk_bf16_f32 v88, v176, v177\n"
"v_cvt_pk_bf16_f32 v89, v178, v179\n"
"v_cvt_pk_bf16_f32 v90, v180, v181\n"
"v_cvt_pk_bf16_f32 v91, v182, v183\n"
"v_cvt_pk_bf16_f32 v92, v184, v185\n"
"v_cvt_pk_bf16_f32 v93, v186, v187\n"
"v_cvt_pk_bf16_f32 v94, v188, v189\n"
"v_cvt_pk_bf16_f32 v95, v190, v191\n"
"buffer_store_dwordx4 v[88:91], v246, s[36:39], s58 offen offset:2048 sc1\n"
"buffer_store_dwordx4 v[92:95], v246, s[36:39], s58 offen offset:3072 sc1\n"
"s_waitcnt vmcnt(0)\n"
"s_branch L_end_%=\n"
"L_cold_%=:\n"
"L_rare0_%=:\n"
"s_getpc_b64 s[62:63]\n"
"L_rpc0_%=:\n"
"s_add_u32 s62, s62, L_ret0_%=-L_rpc0_%=\n"
"s_addc_u32 s63, s63, -1\n"
"s_branch L_sub10_%=\n"
"L_rare1_%=:\n"
"s_getpc_b64 s[62:63]\n"
"L_rpc1_%=:\n"
"s_add_u32 s62, s62, L_ret1_%=-L_rpc1_%=\n"
"s_addc_u32 s63, s63, -1\n"
"s_branch L_sub11_%=\n"
"L_rare2_%=:\n"
"s_getpc_b64 s[62:63]\n"
"L_rpc2_%=:\n"
"s_add_u32 s62, s62, L_ret2_%=-L_rpc2_%=\n"
"s_addc_u32 s63, s63, -1\n"
"s_branch L_sub00_%=\n"
"L_rare3_%=:\n"
"s_getpc_b64 s[62:63]\n"
"L_rpc3_%=:\n"
"s_add_u32 s62, s62, L_ret3_%=-L_rpc3_%=\n"
"s_addc_u32 s63, s63, -1\n"
"s_branch L_sub01_%=\n"
"L_rare4_%=:\n"
"s_getpc_b64 s[62:63]\n"
"L_rpc4_%=:\n"
"s_add_u32 s62, s62, L_ret4_%=-L_rpc4_%=\n"
"s_addc_u32 s63, s63, -1\n"
"s_branch L_sub10_%=\n"
"L_rare5_%=:\n"
"s_getpc_b64 s[62:63]\n"
"L_rpc5_%=:\n"
"s_add_u32 s62, s62, L_ret5_%=-L_rpc5_%=\n"
"s_addc_u32 s63, s63, -1\n"
"s_branch L_sub11_%=\n"
"L_rare6_%=:\n"
"s_getpc_b64 s[62:63]\n"
"L_rpc6_%=:\n"
"s_add_u32 s62, s62, L_ret6_%=-L_rpc6_%=\n"
"s_addc_u32 s63, s63, -1\n"
"s_branch L_sub00_%=\n"
"L_rare7_%=:\n"
"s_getpc_b64 s[62:63]\n"
"L_rpc7_%=:\n"
"s_add_u32 s62, s62, L_ret7_%=-L_rpc7_%=\n"
"s_addc_u32 s63, s63, -1\n"
"s_branch L_sub01_%=\n"
"L_rare8_%=:\n"
"s_getpc_b64 s[62:63]\n"
"L_rpc8_%=:\n"
"s_add_u32 s62, s62, L_ret8_%=-L_rpc8_%=\n"
"s_addc_u32 s63, s63, -1\n"
"s_branch L_sub10_%=\n"
"L_rare9_%=:\n"
"s_getpc_b64 s[62:63]\n"
"L_rpc9_%=:\n"
"s_add_u32 s62, s62, L_ret9_%=-L_rpc9_%=\n"
"s_addc_u32 s63, s63, -1\n"
"s_branch L_sub11_%=\n"
"L_rare10_%=:\n"
"s_getpc_b64 s[62:63]\n"
"L_rpc10_%=:\n"
"s_add_u32 s62, s62, L_ret10_%=-L_rpc10_%=\n"
"s_addc_u32 s63, s63, -1\n"
"s_branch L_sub00_%=\n"
"L_rare11_%=:\n"
"s_getpc_b64 s[62:63]\n"
"L_rpc11_%=:\n"
"s_add_u32 s62, s62, L_ret11_%=-L_rpc11_%=\n"
"s_addc_u32 s63, s63, -1\n"
"s_branch L_sub01_%=\n"
"L_rare12_%=:\n"
"s_getpc_b64 s[62:63]\n"
"L_rpc12_%=:\n"
"s_add_u32 s62, s62, L_ret12_%=-L_rpc12_%=\n"
"s_addc_u32 s63, s63, -1\n"
"s_branch L_sub10_%=\n"
"L_rare13_%=:\n"
"s_getpc_b64 s[62:63]\n"
"L_rpc13_%=:\n"
"s_add_u32 s62, s62, L_ret13_%=-L_rpc13_%=\n"
"s_addc_u32 s63, s63, -1\n"
"s_branch L_sub11_%=\n"
"L_rare14_%=:\n"
"s_getpc_b64 s[62:63]\n"
"L_rpc14_%=:\n"
"s_add_u32 s62, s62, L_ret14_%=-L_rpc14_%=\n"
"s_addc_u32 s63, s63, -1\n"
"s_branch L_sub00_%=\n"
"L_rare15_%=:\n"
"s_getpc_b64 s[62:63]\n"
"L_rpc15_%=:\n"
"s_add_u32 s62, s62, L_ret15_%=-L_rpc15_%=\n"
"s_addc_u32 s63, s63, -1\n"
"s_branch L_sub01_%=\n"
"L_sub10_%=:\n"
"s_nop 15\n"
"v_max3_f32 v224, v160, v161, v162\n"
"v_max3_f32 v224, v224, v163, v164\n"
"v_max3_f32 v224, v224, v165, v166\n"
"v_max_f32 v224, v224, v167\n"
"v_mov_b32 v225, v224\n"
"s_nop 1\n"
"v_permlane32_swap_b32 v224, v225\n"
"v_max_f32 v224, v224, v225\n"
"v_mov_b32 v225, v224\n"
"s_nop 1\n"
"v_permlane16_swap_b32 v224, v225\n"
"v_max_f32 v224, v224, v225\n"
"v_add_f32 v226, s54, v224\n"
"v_max_f32 v226, 0, v226\n"
"v_exp_f32 v227, -v226\n"
"v_sub_f32 v160, v160, v226\n"
"v_sub_f32 v161, v161, v226\n"
"v_sub_f32 v162, v162, v226\n"
"v_sub_f32 v163, v163, v226\n"
"v_sub_f32 v164, v164, v226\n"
"v_sub_f32 v165, v165, v226\n"
"v_sub_f32 v166, v166, v226\n"
"v_sub_f32 v167, v167, v226\n"
"v_sub_f32 v128, v128, v226\n"
"v_sub_f32 v129, v129, v226\n"
"v_sub_f32 v130, v130, v226\n"
"v_sub_f32 v131, v131, v226\n"
"v_sub_f32 v132, v132, v226\n"
"v_sub_f32 v133, v133, v226\n"
"v_sub_f32 v134, v134, v226\n"
"v_sub_f32 v135, v135, v226\n"
"v_sub_f32 v192, v192, v226\n"
"v_sub_f32 v193, v193, v226\n"
"v_sub_f32 v194, v194, v226\n"
"v_sub_f32 v195, v195, v226\n"
"v_mul_f32 v212, v212, v227\n"
"v_accvgpr_read_b32 v228, a0\n"
"v_accvgpr_read_b32 v229, a1\n"
"v_accvgpr_read_b32 v230, a2\n"
"v_accvgpr_read_b32 v231, a3\n"
"v_mul_f32 v228, v228, v227\n"
"v_mul_f32 v229, v229, v227\n"
"v_mul_f32 v230, v230, v227\n"
"v_mul_f32 v231, v231, v227\n"
"v_accvgpr_write_b32 a0, v228\n"
"v_accvgpr_write_b32 a1, v229\n"
"v_accvgpr_write_b32 a2, v230\n"
"v_accvgpr_write_b32 a3, v231\n"
"v_accvgpr_read_b32 v228, a4\n"
"v_accvgpr_read_b32 v229, a5\n"
"v_accvgpr_read_b32 v230, a6\n"
"v_accvgpr_read_b32 v231, a7\n"
"v_mul_f32 v228, v228, v227\n"
"v_mul_f32 v229, v229, v227\n"
"v_mul_f32 v230, v230, v227\n"
"v_mul_f32 v231, v231, v227\n"
"v_accvgpr_write_b32 a4, v228\n"
"v_accvgpr_write_b32 a5, v229\n"
"v_accvgpr_write_b32 a6, v230\n"
"v_accvgpr_write_b32 a7, v231\n"
"v_accvgpr_read_b32 v228, a8\n"
"v_accvgpr_read_b32 v229, a9\n"
"v_accvgpr_read_b32 v230, a10\n"
"v_accvgpr_read_b32 v231, a11\n"
"v_mul_f32 v228, v228, v227\n"
"v_mul_f32 v229, v229, v227\n"
"v_mul_f32 v230, v230, v227\n"
"v_mul_f32 v231, v231, v227\n"
"v_accvgpr_write_b32 a8, v228\n"
"v_accvgpr_write_b32 a9, v229\n"
"v_accvgpr_write_b32 a10, v230\n"
"v_accvgpr_write_b32 a11, v231\n"
"v_accvgpr_read_b32 v228, a12\n"
"v_accvgpr_read_b32 v229, a13\n"
"v_accvgpr_read_b32 v230, a14\n"
"v_accvgpr_read_b32 v231, a15\n"
"v_mul_f32 v228, v228, v227\n"
"v_mul_f32 v229, v229, v227\n"
"v_mul_f32 v230, v230, v227\n"
"v_mul_f32 v231, v231, v227\n"
"v_accvgpr_write_b32 a12, v228\n"
"v_accvgpr_write_b32 a13, v229\n"
"v_accvgpr_write_b32 a14, v230\n"
"v_accvgpr_write_b32 a15, v231\n"
"v_accvgpr_read_b32 v228, a16\n"
"v_accvgpr_read_b32 v229, a17\n"
"v_accvgpr_read_b32 v230, a18\n"
"v_accvgpr_read_b32 v231, a19\n"
"v_mul_f32 v228, v228, v227\n"
"v_mul_f32 v229, v229, v227\n"
"v_mul_f32 v230, v230, v227\n"
"v_mul_f32 v231, v231, v227\n"
"v_accvgpr_write_b32 a16, v228\n"
"v_accvgpr_write_b32 a17, v229\n"
"v_accvgpr_write_b32 a18, v230\n"
"v_accvgpr_write_b32 a19, v231\n"
"v_accvgpr_read_b32 v228, a20\n"
"v_accvgpr_read_b32 v229, a21\n"
"v_accvgpr_read_b32 v230, a22\n"
"v_accvgpr_read_b32 v231, a23\n"
"v_mul_f32 v228, v228, v227\n"
"v_mul_f32 v229, v229, v227\n"
"v_mul_f32 v230, v230, v227\n"
"v_mul_f32 v231, v231, v227\n"
"v_accvgpr_write_b32 a20, v228\n"
"v_accvgpr_write_b32 a21, v229\n"
"v_accvgpr_write_b32 a22, v230\n"
"v_accvgpr_write_b32 a23, v231\n"
"v_accvgpr_read_b32 v228, a24\n"
"v_accvgpr_read_b32 v229, a25\n"
"v_accvgpr_read_b32 v230, a26\n"
"v_accvgpr_read_b32 v231, a27\n"
"v_mul_f32 v228, v228, v227\n"
"v_mul_f32 v229, v229, v227\n"
"v_mul_f32 v230, v230, v227\n"
"v_mul_f32 v231, v231, v227\n"
"v_accvgpr_write_b32 a24, v228\n"
"v_accvgpr_write_b32 a25, v229\n"
"v_accvgpr_write_b32 a26, v230\n"
"v_accvgpr_write_b32 a27, v231\n"
"v_accvgpr_read_b32 v228, a28\n"
"v_accvgpr_read_b32 v229, a29\n"
"v_accvgpr_read_b32 v230, a30\n"
"v_accvgpr_read_b32 v231, a31\n"
"v_mul_f32 v228, v228, v227\n"
"v_mul_f32 v229, v229, v227\n"
"v_mul_f32 v230, v230, v227\n"
"v_mul_f32 v231, v231, v227\n"
"v_accvgpr_write_b32 a28, v228\n"
"v_accvgpr_write_b32 a29, v229\n"
"v_accvgpr_write_b32 a30, v230\n"
"v_accvgpr_write_b32 a31, v231\n"
"v_exp_f32 v228, v160\n"
"v_exp_f32 v229, v161\n"
"s_nop 0\n"
"v_cvt_pk_bf16_f32 v36, v228, v229\n"
"v_exp_f32 v230, v162\n"
"v_exp_f32 v231, v163\n"
"s_nop 0\n"
"v_cvt_pk_bf16_f32 v37, v230, v231\n"
"v_exp_f32 v228, v164\n"
"v_exp_f32 v229, v165\n"
"s_nop 0\n"
"v_cvt_pk_bf16_f32 v38, v228, v229\n"
"v_exp_f32 v230, v166\n"
"v_exp_f32 v231, v167\n"
"s_nop 0\n"
"v_cvt_pk_bf16_f32 v39, v230, v231\n"
"s_nop 1\n"
"v_mfma_f32_16x16x32_bf16 v[100:103], v[116:119], v[36:39], 0\n"
"v_max3_f32 v224, v168, v169, v170\n"
"v_max3_f32 v224, v224, v171, v172\n"
"v_max3_f32 v224, v224, v173, v174\n"
"v_max_f32 v224, v224, v175\n"
"v_mov_b32 v225, v224\n"
"s_nop 1\n"
"v_permlane32_swap_b32 v224, v225\n"
"v_max_f32 v224, v224, v225\n"
"v_mov_b32 v225, v224\n"
"s_nop 1\n"
"v_permlane16_swap_b32 v224, v225\n"
"v_max_f32 v224, v224, v225\n"
"v_add_f32 v226, s54, v224\n"
"v_max_f32 v226, 0, v226\n"
"v_exp_f32 v227, -v226\n"
"v_sub_f32 v168, v168, v226\n"
"v_sub_f32 v169, v169, v226\n"
"v_sub_f32 v170, v170, v226\n"
"v_sub_f32 v171, v171, v226\n"
"v_sub_f32 v172, v172, v226\n"
"v_sub_f32 v173, v173, v226\n"
"v_sub_f32 v174, v174, v226\n"
"v_sub_f32 v175, v175, v226\n"
"v_sub_f32 v136, v136, v226\n"
"v_sub_f32 v137, v137, v226\n"
"v_sub_f32 v138, v138, v226\n"
"v_sub_f32 v139, v139, v226\n"
"v_sub_f32 v140, v140, v226\n"
"v_sub_f32 v141, v141, v226\n"
"v_sub_f32 v142, v142, v226\n"
"v_sub_f32 v143, v143, v226\n"
"v_sub_f32 v196, v196, v226\n"
"v_sub_f32 v197, v197, v226\n"
"v_sub_f32 v198, v198, v226\n"
"v_sub_f32 v199, v199, v226\n"
"v_mul_f32 v213, v213, v227\n"
"v_accvgpr_read_b32 v228, a32\n"
"v_accvgpr_read_b32 v229, a33\n"
"v_accvgpr_read_b32 v230, a34\n"
"v_accvgpr_read_b32 v231, a35\n"
"v_mul_f32 v228, v228, v227\n"
"v_mul_f32 v229, v229, v227\n"
"v_mul_f32 v230, v230, v227\n"
"v_mul_f32 v231, v231, v227\n"
"v_accvgpr_write_b32 a32, v228\n"
"v_accvgpr_write_b32 a33, v229\n"
"v_accvgpr_write_b32 a34, v230\n"
"v_accvgpr_write_b32 a35, v231\n"
"v_accvgpr_read_b32 v228, a36\n"
"v_accvgpr_read_b32 v229, a37\n"
"v_accvgpr_read_b32 v230, a38\n"
"v_accvgpr_read_b32 v231, a39\n"
"v_mul_f32 v228, v228, v227\n"
"v_mul_f32 v229, v229, v227\n"
"v_mul_f32 v230, v230, v227\n"
"v_mul_f32 v231, v231, v227\n"
"v_accvgpr_write_b32 a36, v228\n"
"v_accvgpr_write_b32 a37, v229\n"
"v_accvgpr_write_b32 a38, v230\n"
"v_accvgpr_write_b32 a39, v231\n"
"v_accvgpr_read_b32 v228, a40\n"
"v_accvgpr_read_b32 v229, a41\n"
"v_accvgpr_read_b32 v230, a42\n"
"v_accvgpr_read_b32 v231, a43\n"
"v_mul_f32 v228, v228, v227\n"
"v_mul_f32 v229, v229, v227\n"
"v_mul_f32 v230, v230, v227\n"
"v_mul_f32 v231, v231, v227\n"
"v_accvgpr_write_b32 a40, v228\n"
"v_accvgpr_write_b32 a41, v229\n"
"v_accvgpr_write_b32 a42, v230\n"
"v_accvgpr_write_b32 a43, v231\n"
"v_accvgpr_read_b32 v228, a44\n"
"v_accvgpr_read_b32 v229, a45\n"
"v_accvgpr_read_b32 v230, a46\n"
"v_accvgpr_read_b32 v231, a47\n"
"v_mul_f32 v228, v228, v227\n"
"v_mul_f32 v229, v229, v227\n"
"v_mul_f32 v230, v230, v227\n"
"v_mul_f32 v231, v231, v227\n"
"v_accvgpr_write_b32 a44, v228\n"
"v_accvgpr_write_b32 a45, v229\n"
"v_accvgpr_write_b32 a46, v230\n"
"v_accvgpr_write_b32 a47, v231\n"
"v_accvgpr_read_b32 v228, a48\n"
"v_accvgpr_read_b32 v229, a49\n"
"v_accvgpr_read_b32 v230, a50\n"
"v_accvgpr_read_b32 v231, a51\n"
"v_mul_f32 v228, v228, v227\n"
"v_mul_f32 v229, v229, v227\n"
"v_mul_f32 v230, v230, v227\n"
"v_mul_f32 v231, v231, v227\n"
"v_accvgpr_write_b32 a48, v228\n"
"v_accvgpr_write_b32 a49, v229\n"
"v_accvgpr_write_b32 a50, v230\n"
"v_accvgpr_write_b32 a51, v231\n"
"v_accvgpr_read_b32 v228, a52\n"
"v_accvgpr_read_b32 v229, a53\n"
"v_accvgpr_read_b32 v230, a54\n"
"v_accvgpr_read_b32 v231, a55\n"
"v_mul_f32 v228, v228, v227\n"
"v_mul_f32 v229, v229, v227\n"
"v_mul_f32 v230, v230, v227\n"
"v_mul_f32 v231, v231, v227\n"
"v_accvgpr_write_b32 a52, v228\n"
"v_accvgpr_write_b32 a53, v229\n"
"v_accvgpr_write_b32 a54, v230\n"
"v_accvgpr_write_b32 a55, v231\n"
"v_accvgpr_read_b32 v228, a56\n"
"v_accvgpr_read_b32 v229, a57\n"
"v_accvgpr_read_b32 v230, a58\n"
"v_accvgpr_read_b32 v231, a59\n"
"v_mul_f32 v228, v228, v227\n"
"v_mul_f32 v229, v229, v227\n"
"v_mul_f32 v230, v230, v227\n"
"v_mul_f32 v231, v231, v227\n"
"v_accvgpr_write_b32 a56, v228\n"
"v_accvgpr_write_b32 a57, v229\n"
"v_accvgpr_write_b32 a58, v230\n"
"v_accvgpr_write_b32 a59, v231\n"
"v_accvgpr_read_b32 v228, a60\n"
"v_accvgpr_read_b32 v229, a61\n"
"v_accvgpr_read_b32 v230, a62\n"
"v_accvgpr_read_b32 v231, a63\n"
"v_mul_f32 v228, v228, v227\n"
"v_mul_f32 v229, v229, v227\n"
"v_mul_f32 v230, v230, v227\n"
"v_mul_f32 v231, v231, v227\n"
"v_accvgpr_write_b32 a60, v228\n"
"v_accvgpr_write_b32 a61, v229\n"
"v_accvgpr_write_b32 a62, v230\n"
"v_accvgpr_write_b32 a63, v231\n"
"v_exp_f32 v228, v168\n"
"v_exp_f32 v229, v169\n"
"s_nop 0\n"
"v_cvt_pk_bf16_f32 v44, v228, v229\n"
"v_exp_f32 v230, v170\n"
"v_exp_f32 v231, v171\n"
"s_nop 0\n"
"v_cvt_pk_bf16_f32 v45, v230, v231\n"
"v_exp_f32 v228, v172\n"
"v_exp_f32 v229, v173\n"
"s_nop 0\n"
"v_cvt_pk_bf16_f32 v46, v228, v229\n"
"v_exp_f32 v230, v174\n"
"v_exp_f32 v231, v175\n"
"s_nop 0\n"
"v_cvt_pk_bf16_f32 v47, v230, v231\n"
"s_nop 1\n"
"v_mfma_f32_16x16x32_bf16 v[108:111], v[116:119], v[44:47], 0\n"
"s_nop 15\n"
"s_setpc_b64 s[62:63]\n"
"L_sub11_%=:\n"
"s_nop 15\n"
"v_max3_f32 v224, v176, v177, v178\n"
"v_max3_f32 v224, v224, v179, v180\n"
"v_max3_f32 v224, v224, v181, v182\n"
"v_max_f32 v224, v224, v183\n"
"v_mov_b32 v225, v224\n"
"s_nop 1\n"
"v_permlane32_swap_b32 v224, v225\n"
"v_max_f32 v224, v224, v225\n"
"v_mov_b32 v225, v224\n"
"s_nop 1\n"
"v_permlane16_swap_b32 v224, v225\n"
"v_max_f32 v224, v224, v225\n"
"v_add_f32 v226, s54, v224\n"
"v_max_f32 v226, 0, v226\n"
"v_exp_f32 v227, -v226\n"
"v_sub_f32 v176, v176, v226\n"
"v_sub_f32 v177, v177, v226\n"
"v_sub_f32 v178, v178, v226\n"
"v_sub_f32 v179, v179, v226\n"
"v_sub_f32 v180, v180, v226\n"
"v_sub_f32 v181, v181, v226\n"
"v_sub_f32 v182, v182, v226\n"
"v_sub_f32 v183, v183, v226\n"
"v_sub_f32 v144, v144, v226\n"
"v_sub_f32 v145, v145, v226\n"
"v_sub_f32 v146, v146, v226\n"
"v_sub_f32 v147, v147, v226\n"
"v_sub_f32 v148, v148, v226\n"
"v_sub_f32 v149, v149, v226\n"
"v_sub_f32 v150, v150, v226\n"
"v_sub_f32 v151, v151, v226\n"
"v_sub_f32 v200, v200, v226\n"
"v_sub_f32 v201, v201, v226\n"
"v_sub_f32 v202, v202, v226\n"
"v_sub_f32 v203, v203, v226\n"
"v_mul_f32 v214, v214, v227\n"
"v_accvgpr_read_b32 v228, a64\n"
"v_accvgpr_read_b32 v229, a65\n"
"v_accvgpr_read_b32 v230, a66\n"
"v_accvgpr_read_b32 v231, a67\n"
"v_mul_f32 v228, v228, v227\n"
"v_mul_f32 v229, v229, v227\n"
"v_mul_f32 v230, v230, v227\n"
"v_mul_f32 v231, v231, v227\n"
"v_accvgpr_write_b32 a64, v228\n"
"v_accvgpr_write_b32 a65, v229\n"
"v_accvgpr_write_b32 a66, v230\n"
"v_accvgpr_write_b32 a67, v231\n"
"v_accvgpr_read_b32 v228, a68\n"
"v_accvgpr_read_b32 v229, a69\n"
"v_accvgpr_read_b32 v230, a70\n"
"v_accvgpr_read_b32 v231, a71\n"
"v_mul_f32 v228, v228, v227\n"
"v_mul_f32 v229, v229, v227\n"
"v_mul_f32 v230, v230, v227\n"
"v_mul_f32 v231, v231, v227\n"
"v_accvgpr_write_b32 a68, v228\n"
"v_accvgpr_write_b32 a69, v229\n"
"v_accvgpr_write_b32 a70, v230\n"
"v_accvgpr_write_b32 a71, v231\n"
"v_accvgpr_read_b32 v228, a72\n"
"v_accvgpr_read_b32 v229, a73\n"
"v_accvgpr_read_b32 v230, a74\n"
"v_accvgpr_read_b32 v231, a75\n"
"v_mul_f32 v228, v228, v227\n"
"v_mul_f32 v229, v229, v227\n"
"v_mul_f32 v230, v230, v227\n"
"v_mul_f32 v231, v231, v227\n"
"v_accvgpr_write_b32 a72, v228\n"
"v_accvgpr_write_b32 a73, v229\n"
"v_accvgpr_write_b32 a74, v230\n"
"v_accvgpr_write_b32 a75, v231\n"
"v_accvgpr_read_b32 v228, a76\n"
"v_accvgpr_read_b32 v229, a77\n"
"v_accvgpr_read_b32 v230, a78\n"
"v_accvgpr_read_b32 v231, a79\n"
"v_mul_f32 v228, v228, v227\n"
"v_mul_f32 v229, v229, v227\n"
"v_mul_f32 v230, v230, v227\n"
"v_mul_f32 v231, v231, v227\n"
"v_accvgpr_write_b32 a76, v228\n"
"v_accvgpr_write_b32 a77, v229\n"
"v_accvgpr_write_b32 a78, v230\n"
"v_accvgpr_write_b32 a79, v231\n"
"v_accvgpr_read_b32 v228, a80\n"
"v_accvgpr_read_b32 v229, a81\n"
"v_accvgpr_read_b32 v230, a82\n"
"v_accvgpr_read_b32 v231, a83\n"
"v_mul_f32 v228, v228, v227\n"
"v_mul_f32 v229, v229, v227\n"
"v_mul_f32 v230, v230, v227\n"
"v_mul_f32 v231, v231, v227\n"
"v_accvgpr_write_b32 a80, v228\n"
"v_accvgpr_write_b32 a81, v229\n"
"v_accvgpr_write_b32 a82, v230\n"
"v_accvgpr_write_b32 a83, v231\n"
"v_accvgpr_read_b32 v228, a84\n"
"v_accvgpr_read_b32 v229, a85\n"
"v_accvgpr_read_b32 v230, a86\n"
"v_accvgpr_read_b32 v231, a87\n"
"v_mul_f32 v228, v228, v227\n"
"v_mul_f32 v229, v229, v227\n"
"v_mul_f32 v230, v230, v227\n"
"v_mul_f32 v231, v231, v227\n"
"v_accvgpr_write_b32 a84, v228\n"
"v_accvgpr_write_b32 a85, v229\n"
"v_accvgpr_write_b32 a86, v230\n"
"v_accvgpr_write_b32 a87, v231\n"
"v_accvgpr_read_b32 v228, a88\n"
"v_accvgpr_read_b32 v229, a89\n"
"v_accvgpr_read_b32 v230, a90\n"
"v_accvgpr_read_b32 v231, a91\n"
"v_mul_f32 v228, v228, v227\n"
"v_mul_f32 v229, v229, v227\n"
"v_mul_f32 v230, v230, v227\n"
"v_mul_f32 v231, v231, v227\n"
"v_accvgpr_write_b32 a88, v228\n"
"v_accvgpr_write_b32 a89, v229\n"
"v_accvgpr_write_b32 a90, v230\n"
"v_accvgpr_write_b32 a91, v231\n"
"v_accvgpr_read_b32 v228, a92\n"
"v_accvgpr_read_b32 v229, a93\n"
"v_accvgpr_read_b32 v230, a94\n"
"v_accvgpr_read_b32 v231, a95\n"
"v_mul_f32 v228, v228, v227\n"
"v_mul_f32 v229, v229, v227\n"
"v_mul_f32 v230, v230, v227\n"
"v_mul_f32 v231, v231, v227\n"
"v_accvgpr_write_b32 a92, v228\n"
"v_accvgpr_write_b32 a93, v229\n"
"v_accvgpr_write_b32 a94, v230\n"
"v_accvgpr_write_b32 a95, v231\n"
"v_exp_f32 v228, v176\n"
"v_exp_f32 v229, v177\n"
"s_nop 0\n"
"v_cvt_pk_bf16_f32 v52, v228, v229\n"
"v_exp_f32 v230, v178\n"
"v_exp_f32 v231, v179\n"
"s_nop 0\n"
"v_cvt_pk_bf16_f32 v53, v230, v231\n"
"v_exp_f32 v228, v180\n"
"v_exp_f32 v229, v181\n"
"s_nop 0\n"
"v_cvt_pk_bf16_f32 v54, v228, v229\n"
"v_exp_f32 v230, v182\n"
"v_exp_f32 v231, v183\n"
"s_nop 0\n"
"v_cvt_pk_bf16_f32 v55, v230, v231\n"
"s_nop 1\n"
"v_mfma_f32_16x16x32_bf16 v[100:103], v[116:119], v[52:55], 0\n"
"v_max3_f32 v224, v184, v185, v186\n"
"v_max3_f32 v224, v224, v187, v188\n"
"v_max3_f32 v224, v224, v189, v190\n"
"v_max_f32 v224, v224, v191\n"
"v_mov_b32 v225, v224\n"
"s_nop 1\n"
"v_permlane32_swap_b32 v224, v225\n"
"v_max_f32 v224, v224, v225\n"
"v_mov_b32 v225, v224\n"
"s_nop 1\n"
"v_permlane16_swap_b32 v224, v225\n"
"v_max_f32 v224, v224, v225\n"
"v_add_f32 v226, s54, v224\n"
"v_max_f32 v226, 0, v226\n"
"v_exp_f32 v227, -v226\n"
"v_sub_f32 v184, v184, v226\n"
"v_sub_f32 v185, v185, v226\n"
"v_sub_f32 v186, v186, v226\n"
"v_sub_f32 v187, v187, v226\n"
"v_sub_f32 v188, v188, v226\n"
"v_sub_f32 v189, v189, v226\n"
"v_sub_f32 v190, v190, v226\n"
"v_sub_f32 v191, v191, v226\n"
"v_sub_f32 v152, v152, v226\n"
"v_sub_f32 v153, v153, v226\n"
"v_sub_f32 v154, v154, v226\n"
"v_sub_f32 v155, v155, v226\n"
"v_sub_f32 v156, v156, v226\n"
"v_sub_f32 v157, v157, v226\n"
"v_sub_f32 v158, v158, v226\n"
"v_sub_f32 v159, v159, v226\n"
"v_sub_f32 v204, v204, v226\n"
"v_sub_f32 v205, v205, v226\n"
"v_sub_f32 v206, v206, v226\n"
"v_sub_f32 v207, v207, v226\n"
"v_mul_f32 v215, v215, v227\n"
"v_accvgpr_read_b32 v228, a96\n"
"v_accvgpr_read_b32 v229, a97\n"
"v_accvgpr_read_b32 v230, a98\n"
"v_accvgpr_read_b32 v231, a99\n"
"v_mul_f32 v228, v228, v227\n"
"v_mul_f32 v229, v229, v227\n"
"v_mul_f32 v230, v230, v227\n"
"v_mul_f32 v231, v231, v227\n"
"v_accvgpr_write_b32 a96, v228\n"
"v_accvgpr_write_b32 a97, v229\n"
"v_accvgpr_write_b32 a98, v230\n"
"v_accvgpr_write_b32 a99, v231\n"
"v_accvgpr_read_b32 v228, a100\n"
"v_accvgpr_read_b32 v229, a101\n"
"v_accvgpr_read_b32 v230, a102\n"
"v_accvgpr_read_b32 v231, a103\n"
"v_mul_f32 v228, v228, v227\n"
"v_mul_f32 v229, v229, v227\n"
"v_mul_f32 v230, v230, v227\n"
"v_mul_f32 v231, v231, v227\n"
"v_accvgpr_write_b32 a100, v228\n"
"v_accvgpr_write_b32 a101, v229\n"
"v_accvgpr_write_b32 a102, v230\n"
"v_accvgpr_write_b32 a103, v231\n"
"v_accvgpr_read_b32 v228, a104\n"
"v_accvgpr_read_b32 v229, a105\n"
"v_accvgpr_read_b32 v230, a106\n"
"v_accvgpr_read_b32 v231, a107\n"
"v_mul_f32 v228, v228, v227\n"
"v_mul_f32 v229, v229, v227\n"
"v_mul_f32 v230, v230, v227\n"
"v_mul_f32 v231, v231, v227\n"
"v_accvgpr_write_b32 a104, v228\n"
"v_accvgpr_write_b32 a105, v229\n"
"v_accvgpr_write_b32 a106, v230\n"
"v_accvgpr_write_b32 a107, v231\n"
"v_accvgpr_read_b32 v228, a108\n"
"v_accvgpr_read_b32 v229, a109\n"
"v_accvgpr_read_b32 v230, a110\n"
"v_accvgpr_read_b32 v231, a111\n"
"v_mul_f32 v228, v228, v227\n"
"v_mul_f32 v229, v229, v227\n"
"v_mul_f32 v230, v230, v227\n"
"v_mul_f32 v231, v231, v227\n"
"v_accvgpr_write_b32 a108, v228\n"
"v_accvgpr_write_b32 a109, v229\n"
"v_accvgpr_write_b32 a110, v230\n"
"v_accvgpr_write_b32 a111, v231\n"
"v_accvgpr_read_b32 v228, a112\n"
"v_accvgpr_read_b32 v229, a113\n"
"v_accvgpr_read_b32 v230, a114\n"
"v_accvgpr_read_b32 v231, a115\n"
"v_mul_f32 v228, v228, v227\n"
"v_mul_f32 v229, v229, v227\n"
"v_mul_f32 v230, v230, v227\n"
"v_mul_f32 v231, v231, v227\n"
"v_accvgpr_write_b32 a112, v228\n"
"v_accvgpr_write_b32 a113, v229\n"
"v_accvgpr_write_b32 a114, v230\n"
"v_accvgpr_write_b32 a115, v231\n"
"v_accvgpr_read_b32 v228, a116\n"
"v_accvgpr_read_b32 v229, a117\n"
"v_accvgpr_read_b32 v230, a118\n"
"v_accvgpr_read_b32 v231, a119\n"
"v_mul_f32 v228, v228, v227\n"
"v_mul_f32 v229, v229, v227\n"
"v_mul_f32 v230, v230, v227\n"
"v_mul_f32 v231, v231, v227\n"
"v_accvgpr_write_b32 a116, v228\n"
"v_accvgpr_write_b32 a117, v229\n"
"v_accvgpr_write_b32 a118, v230\n"
"v_accvgpr_write_b32 a119, v231\n"
"v_accvgpr_read_b32 v228, a120\n"
"v_accvgpr_read_b32 v229, a121\n"
"v_accvgpr_read_b32 v230, a122\n"
"v_accvgpr_read_b32 v231, a123\n"
"v_mul_f32 v228, v228, v227\n"
"v_mul_f32 v229, v229, v227\n"
"v_mul_f32 v230, v230, v227\n"
"v_mul_f32 v231, v231, v227\n"
"v_accvgpr_write_b32 a120, v228\n"
"v_accvgpr_write_b32 a121, v229\n"
"v_accvgpr_write_b32 a122, v230\n"
"v_accvgpr_write_b32 a123, v231\n"
"v_accvgpr_read_b32 v228, a124\n"
"v_accvgpr_read_b32 v229, a125\n"
"v_accvgpr_read_b32 v230, a126\n"
"v_accvgpr_read_b32 v231, a127\n"
"v_mul_f32 v228, v228, v227\n"
"v_mul_f32 v229, v229, v227\n"
"v_mul_f32 v230, v230, v227\n"
"v_mul_f32 v231, v231, v227\n"
"v_accvgpr_write_b32 a124, v228\n"
"v_accvgpr_write_b32 a125, v229\n"
"v_accvgpr_write_b32 a126, v230\n"
"v_accvgpr_write_b32 a127, v231\n"
"v_exp_f32 v228, v184\n"
"v_exp_f32 v229, v185\n"
"s_nop 0\n"
"v_cvt_pk_bf16_f32 v60, v228, v229\n"
"v_exp_f32 v230, v186\n"
"v_exp_f32 v231, v187\n"
"s_nop 0\n"
"v_cvt_pk_bf16_f32 v61, v230, v231\n"
"v_exp_f32 v228, v188\n"
"v_exp_f32 v229, v189\n"
"s_nop 0\n"
"v_cvt_pk_bf16_f32 v62, v228, v229\n"
"v_exp_f32 v230, v190\n"
"v_exp_f32 v231, v191\n"
"s_nop 0\n"
"v_cvt_pk_bf16_f32 v63, v230, v231\n"
"s_nop 1\n"
"v_mfma_f32_16x16x32_bf16 v[108:111], v[116:119], v[60:63], 0\n"
"s_nop 15\n"
"s_setpc_b64 s[62:63]\n"
"L_sub00_%=:\n"
"s_nop 15\n"
"v_max3_f32 v224, v128, v129, v130\n"
"v_max3_f32 v224, v224, v131, v132\n"
"v_max3_f32 v224, v224, v133, v134\n"
"v_max_f32 v224, v224, v135\n"
"v_mov_b32 v225, v224\n"
"s_nop 1\n"
"v_permlane32_swap_b32 v224, v225\n"
"v_max_f32 v224, v224, v225\n"
"v_mov_b32 v225, v224\n"
"s_nop 1\n"
"v_permlane16_swap_b32 v224, v225\n"
"v_max_f32 v224, v224, v225\n"
"v_add_f32 v226, s54, v224\n"
"v_max_f32 v226, 0, v226\n"
"v_exp_f32 v227, -v226\n"
"v_sub_f32 v128, v128, v226\n"
"v_sub_f32 v129, v129, v226\n"
"v_sub_f32 v130, v130, v226\n"
"v_sub_f32 v131, v131, v226\n"
"v_sub_f32 v132, v132, v226\n"
"v_sub_f32 v133, v133, v226\n"
"v_sub_f32 v134, v134, v226\n"
"v_sub_f32 v135, v135, v226\n"
"v_sub_f32 v160, v160, v226\n"
"v_sub_f32 v161, v161, v226\n"
"v_sub_f32 v162, v162, v226\n"
"v_sub_f32 v163, v163, v226\n"
"v_sub_f32 v164, v164, v226\n"
"v_sub_f32 v165, v165, v226\n"
"v_sub_f32 v166, v166, v226\n"
"v_sub_f32 v167, v167, v226\n"
"v_sub_f32 v192, v192, v226\n"
"v_sub_f32 v193, v193, v226\n"
"v_sub_f32 v194, v194, v226\n"
"v_sub_f32 v195, v195, v226\n"
"v_mul_f32 v212, v212, v227\n"
"v_accvgpr_read_b32 v228, a0\n"
"v_accvgpr_read_b32 v229, a1\n"
"v_accvgpr_read_b32 v230, a2\n"
"v_accvgpr_read_b32 v231, a3\n"
"v_mul_f32 v228, v228, v227\n"
"v_mul_f32 v229, v229, v227\n"
"v_mul_f32 v230, v230, v227\n"
"v_mul_f32 v231, v231, v227\n"
"v_accvgpr_write_b32 a0, v228\n"
"v_accvgpr_write_b32 a1, v229\n"
"v_accvgpr_write_b32 a2, v230\n"
"v_accvgpr_write_b32 a3, v231\n"
"v_accvgpr_read_b32 v228, a4\n"
"v_accvgpr_read_b32 v229, a5\n"
"v_accvgpr_read_b32 v230, a6\n"
"v_accvgpr_read_b32 v231, a7\n"
"v_mul_f32 v228, v228, v227\n"
"v_mul_f32 v229, v229, v227\n"
"v_mul_f32 v230, v230, v227\n"
"v_mul_f32 v231, v231, v227\n"
"v_accvgpr_write_b32 a4, v228\n"
"v_accvgpr_write_b32 a5, v229\n"
"v_accvgpr_write_b32 a6, v230\n"
"v_accvgpr_write_b32 a7, v231\n"
"v_accvgpr_read_b32 v228, a8\n"
"v_accvgpr_read_b32 v229, a9\n"
"v_accvgpr_read_b32 v230, a10\n"
"v_accvgpr_read_b32 v231, a11\n"
"v_mul_f32 v228, v228, v227\n"
"v_mul_f32 v229, v229, v227\n"
"v_mul_f32 v230, v230, v227\n"
"v_mul_f32 v231, v231, v227\n"
"v_accvgpr_write_b32 a8, v228\n"
"v_accvgpr_write_b32 a9, v229\n"
"v_accvgpr_write_b32 a10, v230\n"
"v_accvgpr_write_b32 a11, v231\n"
"v_accvgpr_read_b32 v228, a12\n"
"v_accvgpr_read_b32 v229, a13\n"
"v_accvgpr_read_b32 v230, a14\n"
"v_accvgpr_read_b32 v231, a15\n"
"v_mul_f32 v228, v228, v227\n"
"v_mul_f32 v229, v229, v227\n"
"v_mul_f32 v230, v230, v227\n"
"v_mul_f32 v231, v231, v227\n"
"v_accvgpr_write_b32 a12, v228\n"
"v_accvgpr_write_b32 a13, v229\n"
"v_accvgpr_write_b32 a14, v230\n"
"v_accvgpr_write_b32 a15, v231\n"
"v_accvgpr_read_b32 v228, a16\n"
"v_accvgpr_read_b32 v229, a17\n"
"v_accvgpr_read_b32 v230, a18\n"
"v_accvgpr_read_b32 v231, a19\n"
"v_mul_f32 v228, v228, v227\n"
"v_mul_f32 v229, v229, v227\n"
"v_mul_f32 v230, v230, v227\n"
"v_mul_f32 v231, v231, v227\n"
"v_accvgpr_write_b32 a16, v228\n"
"v_accvgpr_write_b32 a17, v229\n"
"v_accvgpr_write_b32 a18, v230\n"
"v_accvgpr_write_b32 a19, v231\n"
"v_accvgpr_read_b32 v228, a20\n"
"v_accvgpr_read_b32 v229, a21\n"
"v_accvgpr_read_b32 v230, a22\n"
"v_accvgpr_read_b32 v231, a23\n"
"v_mul_f32 v228, v228, v227\n"
"v_mul_f32 v229, v229, v227\n"
"v_mul_f32 v230, v230, v227\n"
"v_mul_f32 v231, v231, v227\n"
"v_accvgpr_write_b32 a20, v228\n"
"v_accvgpr_write_b32 a21, v229\n"
"v_accvgpr_write_b32 a22, v230\n"
"v_accvgpr_write_b32 a23, v231\n"
"v_accvgpr_read_b32 v228, a24\n"
"v_accvgpr_read_b32 v229, a25\n"
"v_accvgpr_read_b32 v230, a26\n"
"v_accvgpr_read_b32 v231, a27\n"
"v_mul_f32 v228, v228, v227\n"
"v_mul_f32 v229, v229, v227\n"
"v_mul_f32 v230, v230, v227\n"
"v_mul_f32 v231, v231, v227\n"
"v_accvgpr_write_b32 a24, v228\n"
"v_accvgpr_write_b32 a25, v229\n"
"v_accvgpr_write_b32 a26, v230\n"
"v_accvgpr_write_b32 a27, v231\n"
"v_accvgpr_read_b32 v228, a28\n"
"v_accvgpr_read_b32 v229, a29\n"
"v_accvgpr_read_b32 v230, a30\n"
"v_accvgpr_read_b32 v231, a31\n"
"v_mul_f32 v228, v228, v227\n"
"v_mul_f32 v229, v229, v227\n"
"v_mul_f32 v230, v230, v227\n"
"v_mul_f32 v231, v231, v227\n"
"v_accvgpr_write_b32 a28, v228\n"
"v_accvgpr_write_b32 a29, v229\n"
"v_accvgpr_write_b32 a30, v230\n"
"v_accvgpr_write_b32 a31, v231\n"
"v_exp_f32 v228, v128\n"
"v_exp_f32 v229, v129\n"
"s_nop 0\n"
"v_cvt_pk_bf16_f32 v4, v228, v229\n"
"v_exp_f32 v230, v130\n"
"v_exp_f32 v231, v131\n"
"s_nop 0\n"
"v_cvt_pk_bf16_f32 v5, v230, v231\n"
"v_exp_f32 v228, v132\n"
"v_exp_f32 v229, v133\n"
"s_nop 0\n"
"v_cvt_pk_bf16_f32 v6, v228, v229\n"
"v_exp_f32 v230, v134\n"
"v_exp_f32 v231, v135\n"
"s_nop 0\n"
"v_cvt_pk_bf16_f32 v7, v230, v231\n"
"s_nop 1\n"
"v_mfma_f32_16x16x32_bf16 v[100:103], v[116:119], v[4:7], 0\n"
"v_max3_f32 v224, v136, v137, v138\n"
"v_max3_f32 v224, v224, v139, v140\n"
"v_max3_f32 v224, v224, v141, v142\n"
"v_max_f32 v224, v224, v143\n"
"v_mov_b32 v225, v224\n"
"s_nop 1\n"
"v_permlane32_swap_b32 v224, v225\n"
"v_max_f32 v224, v224, v225\n"
"v_mov_b32 v225, v224\n"
"s_nop 1\n"
"v_permlane16_swap_b32 v224, v225\n"
"v_max_f32 v224, v224, v225\n"
"v_add_f32 v226, s54, v224\n"
"v_max_f32 v226, 0, v226\n"
"v_exp_f32 v227, -v226\n"
"v_sub_f32 v136, v136, v226\n"
"v_sub_f32 v137, v137, v226\n"
"v_sub_f32 v138, v138, v226\n"
"v_sub_f32 v139, v139, v226\n"
"v_sub_f32 v140, v140, v226\n"
"v_sub_f32 v141, v141, v226\n"
"v_sub_f32 v142, v142, v226\n"
"v_sub_f32 v143, v143, v226\n"
"v_sub_f32 v168, v168, v226\n"
"v_sub_f32 v169, v169, v226\n"
"v_sub_f32 v170, v170, v226\n"
"v_sub_f32 v171, v171, v226\n"
"v_sub_f32 v172, v172, v226\n"
"v_sub_f32 v173, v173, v226\n"
"v_sub_f32 v174, v174, v226\n"
"v_sub_f32 v175, v175, v226\n"
"v_sub_f32 v196, v196, v226\n"
"v_sub_f32 v197, v197, v226\n"
"v_sub_f32 v198, v198, v226\n"
"v_sub_f32 v199, v199, v226\n"
"v_mul_f32 v213, v213, v227\n"
"v_accvgpr_read_b32 v228, a32\n"
"v_accvgpr_read_b32 v229, a33\n"
"v_accvgpr_read_b32 v230, a34\n"
"v_accvgpr_read_b32 v231, a35\n"
"v_mul_f32 v228, v228, v227\n"
"v_mul_f32 v229, v229, v227\n"
"v_mul_f32 v230, v230, v227\n"
"v_mul_f32 v231, v231, v227\n"
"v_accvgpr_write_b32 a32, v228\n"
"v_accvgpr_write_b32 a33, v229\n"
"v_accvgpr_write_b32 a34, v230\n"
"v_accvgpr_write_b32 a35, v231\n"
"v_accvgpr_read_b32 v228, a36\n"
"v_accvgpr_read_b32 v229, a37\n"
"v_accvgpr_read_b32 v230, a38\n"
"v_accvgpr_read_b32 v231, a39\n"
"v_mul_f32 v228, v228, v227\n"
"v_mul_f32 v229, v229, v227\n"
"v_mul_f32 v230, v230, v227\n"
"v_mul_f32 v231, v231, v227\n"
"v_accvgpr_write_b32 a36, v228\n"
"v_accvgpr_write_b32 a37, v229\n"
"v_accvgpr_write_b32 a38, v230\n"
"v_accvgpr_write_b32 a39, v231\n"
"v_accvgpr_read_b32 v228, a40\n"
"v_accvgpr_read_b32 v229, a41\n"
"v_accvgpr_read_b32 v230, a42\n"
"v_accvgpr_read_b32 v231, a43\n"
"v_mul_f32 v228, v228, v227\n"
"v_mul_f32 v229, v229, v227\n"
"v_mul_f32 v230, v230, v227\n"
"v_mul_f32 v231, v231, v227\n"
"v_accvgpr_write_b32 a40, v228\n"
"v_accvgpr_write_b32 a41, v229\n"
"v_accvgpr_write_b32 a42, v230\n"
"v_accvgpr_write_b32 a43, v231\n"
"v_accvgpr_read_b32 v228, a44\n"
"v_accvgpr_read_b32 v229, a45\n"
"v_accvgpr_read_b32 v230, a46\n"
"v_accvgpr_read_b32 v231, a47\n"
"v_mul_f32 v228, v228, v227\n"
"v_mul_f32 v229, v229, v227\n"
"v_mul_f32 v230, v230, v227\n"
"v_mul_f32 v231, v231, v227\n"
"v_accvgpr_write_b32 a44, v228\n"
"v_accvgpr_write_b32 a45, v229\n"
"v_accvgpr_write_b32 a46, v230\n"
"v_accvgpr_write_b32 a47, v231\n"
"v_accvgpr_read_b32 v228, a48\n"
"v_accvgpr_read_b32 v229, a49\n"
"v_accvgpr_read_b32 v230, a50\n"
"v_accvgpr_read_b32 v231, a51\n"
"v_mul_f32 v228, v228, v227\n"
"v_mul_f32 v229, v229, v227\n"
"v_mul_f32 v230, v230, v227\n"
"v_mul_f32 v231, v231, v227\n"
"v_accvgpr_write_b32 a48, v228\n"
"v_accvgpr_write_b32 a49, v229\n"
"v_accvgpr_write_b32 a50, v230\n"
"v_accvgpr_write_b32 a51, v231\n"
"v_accvgpr_read_b32 v228, a52\n"
"v_accvgpr_read_b32 v229, a53\n"
"v_accvgpr_read_b32 v230, a54\n"
"v_accvgpr_read_b32 v231, a55\n"
"v_mul_f32 v228, v228, v227\n"
"v_mul_f32 v229, v229, v227\n"
"v_mul_f32 v230, v230, v227\n"
"v_mul_f32 v231, v231, v227\n"
"v_accvgpr_write_b32 a52, v228\n"
"v_accvgpr_write_b32 a53, v229\n"
"v_accvgpr_write_b32 a54, v230\n"
"v_accvgpr_write_b32 a55, v231\n"
"v_accvgpr_read_b32 v228, a56\n"
"v_accvgpr_read_b32 v229, a57\n"
"v_accvgpr_read_b32 v230, a58\n"
"v_accvgpr_read_b32 v231, a59\n"
"v_mul_f32 v228, v228, v227\n"
"v_mul_f32 v229, v229, v227\n"
"v_mul_f32 v230, v230, v227\n"
"v_mul_f32 v231, v231, v227\n"
"v_accvgpr_write_b32 a56, v228\n"
"v_accvgpr_write_b32 a57, v229\n"
"v_accvgpr_write_b32 a58, v230\n"
"v_accvgpr_write_b32 a59, v231\n"
"v_accvgpr_read_b32 v228, a60\n"
"v_accvgpr_read_b32 v229, a61\n"
"v_accvgpr_read_b32 v230, a62\n"
"v_accvgpr_read_b32 v231, a63\n"
"v_mul_f32 v228, v228, v227\n"
"v_mul_f32 v229, v229, v227\n"
"v_mul_f32 v230, v230, v227\n"
"v_mul_f32 v231, v231, v227\n"
"v_accvgpr_write_b32 a60, v228\n"
"v_accvgpr_write_b32 a61, v229\n"
"v_accvgpr_write_b32 a62, v230\n"
"v_accvgpr_write_b32 a63, v231\n"
"v_exp_f32 v228, v136\n"
"v_exp_f32 v229, v137\n"
"s_nop 0\n"
"v_cvt_pk_bf16_f32 v12, v228, v229\n"
"v_exp_f32 v230, v138\n"
"v_exp_f32 v231, v139\n"
"s_nop 0\n"
"v_cvt_pk_bf16_f32 v13, v230, v231\n"
"v_exp_f32 v228, v140\n"
"v_exp_f32 v229, v141\n"
"s_nop 0\n"
"v_cvt_pk_bf16_f32 v14, v228, v229\n"
"v_exp_f32 v230, v142\n"
"v_exp_f32 v231, v143\n"
"s_nop 0\n"
"v_cvt_pk_bf16_f32 v15, v230, v231\n"
"s_nop 1\n"
"v_mfma_f32_16x16x32_bf16 v[108:111], v[116:119], v[12:15], 0\n"
"s_nop 15\n"
"s_setpc_b64 s[62:63]\n"
"L_sub01_%=:\n"
"s_nop 15\n"
"v_max3_f32 v224, v144, v145, v146\n"
"v_max3_f32 v224, v224, v147, v148\n"
"v_max3_f32 v224, v224, v149, v150\n"
"v_max_f32 v224, v224, v151\n"
"v_mov_b32 v225, v224\n"
"s_nop 1\n"
"v_permlane32_swap_b32 v224, v225\n"
"v_max_f32 v224, v224, v225\n"
"v_mov_b32 v225, v224\n"
"s_nop 1\n"
"v_permlane16_swap_b32 v224, v225\n"
"v_max_f32 v224, v224, v225\n"
"v_add_f32 v226, s54, v224\n"
"v_max_f32 v226, 0, v226\n"
"v_exp_f32 v227, -v226\n"
"v_sub_f32 v144, v144, v226\n"
"v_sub_f32 v145, v145, v226\n"
"v_sub_f32 v146, v146, v226\n"
"v_sub_f32 v147, v147, v226\n"
"v_sub_f32 v148, v148, v226\n"
"v_sub_f32 v149, v149, v226\n"
"v_sub_f32 v150, v150, v226\n"
"v_sub_f32 v151, v151, v226\n"
"v_sub_f32 v176, v176, v226\n"
"v_sub_f32 v177, v177, v226\n"
"v_sub_f32 v178, v178, v226\n"
"v_sub_f32 v179, v179, v226\n"
"v_sub_f32 v180, v180, v226\n"
"v_sub_f32 v181, v181, v226\n"
"v_sub_f32 v182, v182, v226\n"
"v_sub_f32 v183, v183, v226\n"
"v_sub_f32 v200, v200, v226\n"
"v_sub_f32 v201, v201, v226\n"
"v_sub_f32 v202, v202, v226\n"
"v_sub_f32 v203, v203, v226\n"
"v_mul_f32 v214, v214, v227\n"
"v_accvgpr_read_b32 v228, a64\n"
"v_accvgpr_read_b32 v229, a65\n"
"v_accvgpr_read_b32 v230, a66\n"
"v_accvgpr_read_b32 v231, a67\n"
"v_mul_f32 v228, v228, v227\n"
"v_mul_f32 v229, v229, v227\n"
"v_mul_f32 v230, v230, v227\n"
"v_mul_f32 v231, v231, v227\n"
"v_accvgpr_write_b32 a64, v228\n"
"v_accvgpr_write_b32 a65, v229\n"
"v_accvgpr_write_b32 a66, v230\n"
"v_accvgpr_write_b32 a67, v231\n"
"v_accvgpr_read_b32 v228, a68\n"
"v_accvgpr_read_b32 v229, a69\n"
"v_accvgpr_read_b32 v230, a70\n"
"v_accvgpr_read_b32 v231, a71\n"
"v_mul_f32 v228, v228, v227\n"
"v_mul_f32 v229, v229, v227\n"
"v_mul_f32 v230, v230, v227\n"
"v_mul_f32 v231, v231, v227\n"
"v_accvgpr_write_b32 a68, v228\n"
"v_accvgpr_write_b32 a69, v229\n"
"v_accvgpr_write_b32 a70, v230\n"
"v_accvgpr_write_b32 a71, v231\n"
"v_accvgpr_read_b32 v228, a72\n"
"v_accvgpr_read_b32 v229, a73\n"
"v_accvgpr_read_b32 v230, a74\n"
"v_accvgpr_read_b32 v231, a75\n"
"v_mul_f32 v228, v228, v227\n"
"v_mul_f32 v229, v229, v227\n"
"v_mul_f32 v230, v230, v227\n"
"v_mul_f32 v231, v231, v227\n"
"v_accvgpr_write_b32 a72, v228\n"
"v_accvgpr_write_b32 a73, v229\n"
"v_accvgpr_write_b32 a74, v230\n"
"v_accvgpr_write_b32 a75, v231\n"
"v_accvgpr_read_b32 v228, a76\n"
"v_accvgpr_read_b32 v229, a77\n"
"v_accvgpr_read_b32 v230, a78\n"
"v_accvgpr_read_b32 v231, a79\n"
"v_mul_f32 v228, v228, v227\n"
"v_mul_f32 v229, v229, v227\n"
"v_mul_f32 v230, v230, v227\n"
"v_mul_f32 v231, v231, v227\n"
"v_accvgpr_write_b32 a76, v228\n"
"v_accvgpr_write_b32 a77, v229\n"
"v_accvgpr_write_b32 a78, v230\n"
"v_accvgpr_write_b32 a79, v231\n"
"v_accvgpr_read_b32 v228, a80\n"
"v_accvgpr_read_b32 v229, a81\n"
"v_accvgpr_read_b32 v230, a82\n"
"v_accvgpr_read_b32 v231, a83\n"
"v_mul_f32 v228, v228, v227\n"
"v_mul_f32 v229, v229, v227\n"
"v_mul_f32 v230, v230, v227\n"
"v_mul_f32 v231, v231, v227\n"
"v_accvgpr_write_b32 a80, v228\n"
"v_accvgpr_write_b32 a81, v229\n"
"v_accvgpr_write_b32 a82, v230\n"
"v_accvgpr_write_b32 a83, v231\n"
"v_accvgpr_read_b32 v228, a84\n"
"v_accvgpr_read_b32 v229, a85\n"
"v_accvgpr_read_b32 v230, a86\n"
"v_accvgpr_read_b32 v231, a87\n"
"v_mul_f32 v228, v228, v227\n"
"v_mul_f32 v229, v229, v227\n"
"v_mul_f32 v230, v230, v227\n"
"v_mul_f32 v231, v231, v227\n"
"v_accvgpr_write_b32 a84, v228\n"
"v_accvgpr_write_b32 a85, v229\n"
"v_accvgpr_write_b32 a86, v230\n"
"v_accvgpr_write_b32 a87, v231\n"
"v_accvgpr_read_b32 v228, a88\n"
"v_accvgpr_read_b32 v229, a89\n"
"v_accvgpr_read_b32 v230, a90\n"
"v_accvgpr_read_b32 v231, a91\n"
"v_mul_f32 v228, v228, v227\n"
"v_mul_f32 v229, v229, v227\n"
"v_mul_f32 v230, v230, v227\n"
"v_mul_f32 v231, v231, v227\n"
"v_accvgpr_write_b32 a88, v228\n"
"v_accvgpr_write_b32 a89, v229\n"
"v_accvgpr_write_b32 a90, v230\n"
"v_accvgpr_write_b32 a91, v231\n"
"v_accvgpr_read_b32 v228, a92\n"
"v_accvgpr_read_b32 v229, a93\n"
"v_accvgpr_read_b32 v230, a94\n"
"v_accvgpr_read_b32 v231, a95\n"
"v_mul_f32 v228, v228, v227\n"
"v_mul_f32 v229, v229, v227\n"
"v_mul_f32 v230, v230, v227\n"
"v_mul_f32 v231, v231, v227\n"
"v_accvgpr_write_b32 a92, v228\n"
"v_accvgpr_write_b32 a93, v229\n"
"v_accvgpr_write_b32 a94, v230\n"
"v_accvgpr_write_b32 a95, v231\n"
"v_exp_f32 v228, v144\n"
"v_exp_f32 v229, v145\n"
"s_nop 0\n"
"v_cvt_pk_bf16_f32 v20, v228, v229\n"
"v_exp_f32 v230, v146\n"
"v_exp_f32 v231, v147\n"
"s_nop 0\n"
"v_cvt_pk_bf16_f32 v21, v230, v231\n"
"v_exp_f32 v228, v148\n"
"v_exp_f32 v229, v149\n"
"s_nop 0\n"
"v_cvt_pk_bf16_f32 v22, v228, v229\n"
"v_exp_f32 v230, v150\n"
"v_exp_f32 v231, v151\n"
"s_nop 0\n"
"v_cvt_pk_bf16_f32 v23, v230, v231\n"
"s_nop 1\n"
"v_mfma_f32_16x16x32_bf16 v[100:103], v[116:119], v[20:23], 0\n"
"v_max3_f32 v224, v152, v153, v154\n"
"v_max3_f32 v224, v224, v155, v156\n"
"v_max3_f32 v224, v224, v157, v158\n"
"v_max_f32 v224, v224, v159\n"
"v_mov_b32 v225, v224\n"
"s_nop 1\n"
"v_permlane32_swap_b32 v224, v225\n"
"v_max_f32 v224, v224, v225\n"
"v_mov_b32 v225, v224\n"
"s_nop 1\n"
"v_permlane16_swap_b32 v224, v225\n"
"v_max_f32 v224, v224, v225\n"
"v_add_f32 v226, s54, v224\n"
"v_max_f32 v226, 0, v226\n"
"v_exp_f32 v227, -v226\n"
"v_sub_f32 v152, v152, v226\n"
"v_sub_f32 v153, v153, v226\n"
"v_sub_f32 v154, v154, v226\n"
"v_sub_f32 v155, v155, v226\n"
"v_sub_f32 v156, v156, v226\n"
"v_sub_f32 v157, v157, v226\n"
"v_sub_f32 v158, v158, v226\n"
"v_sub_f32 v159, v159, v226\n"
"v_sub_f32 v184, v184, v226\n"
"v_sub_f32 v185, v185, v226\n"
"v_sub_f32 v186, v186, v226\n"
"v_sub_f32 v187, v187, v226\n"
"v_sub_f32 v188, v188, v226\n"
"v_sub_f32 v189, v189, v226\n"
"v_sub_f32 v190, v190, v226\n"
"v_sub_f32 v191, v191, v226\n"
"v_sub_f32 v204, v204, v226\n"
"v_sub_f32 v205, v205, v226\n"
"v_sub_f32 v206, v206, v226\n"
"v_sub_f32 v207, v207, v226\n"
"v_mul_f32 v215, v215, v227\n"
"v_accvgpr_read_b32 v228, a96\n"
"v_accvgpr_read_b32 v229, a97\n"
"v_accvgpr_read_b32 v230, a98\n"
"v_accvgpr_read_b32 v231, a99\n"
"v_mul_f32 v228, v228, v227\n"
"v_mul_f32 v229, v229, v227\n"
"v_mul_f32 v230, v230, v227\n"
"v_mul_f32 v231, v231, v227\n"
"v_accvgpr_write_b32 a96, v228\n"
"v_accvgpr_write_b32 a97, v229\n"
"v_accvgpr_write_b32 a98, v230\n"
"v_accvgpr_write_b32 a99, v231\n"
"v_accvgpr_read_b32 v228, a100\n"
"v_accvgpr_read_b32 v229, a101\n"
"v_accvgpr_read_b32 v230, a102\n"
"v_accvgpr_read_b32 v231, a103\n"
"v_mul_f32 v228, v228, v227\n"
"v_mul_f32 v229, v229, v227\n"
"v_mul_f32 v230, v230, v227\n"
"v_mul_f32 v231, v231, v227\n"
"v_accvgpr_write_b32 a100, v228\n"
"v_accvgpr_write_b32 a101, v229\n"
"v_accvgpr_write_b32 a102, v230\n"
"v_accvgpr_write_b32 a103, v231\n"
"v_accvgpr_read_b32 v228, a104\n"
"v_accvgpr_read_b32 v229, a105\n"
"v_accvgpr_read_b32 v230, a106\n"
"v_accvgpr_read_b32 v231, a107\n"
"v_mul_f32 v228, v228, v227\n"
"v_mul_f32 v229, v229, v227\n"
"v_mul_f32 v230, v230, v227\n"
"v_mul_f32 v231, v231, v227\n"
"v_accvgpr_write_b32 a104, v228\n"
"v_accvgpr_write_b32 a105, v229\n"
"v_accvgpr_write_b32 a106, v230\n"
"v_accvgpr_write_b32 a107, v231\n"
"v_accvgpr_read_b32 v228, a108\n"
"v_accvgpr_read_b32 v229, a109\n"
"v_accvgpr_read_b32 v230, a110\n"
"v_accvgpr_read_b32 v231, a111\n"
"v_mul_f32 v228, v228, v227\n"
"v_mul_f32 v229, v229, v227\n"
"v_mul_f32 v230, v230, v227\n"
"v_mul_f32 v231, v231, v227\n"
"v_accvgpr_write_b32 a108, v228\n"
"v_accvgpr_write_b32 a109, v229\n"
"v_accvgpr_write_b32 a110, v230\n"
"v_accvgpr_write_b32 a111, v231\n"
"v_accvgpr_read_b32 v228, a112\n"
"v_accvgpr_read_b32 v229, a113\n"
"v_accvgpr_read_b32 v230, a114\n"
"v_accvgpr_read_b32 v231, a115\n"
"v_mul_f32 v228, v228, v227\n"
"v_mul_f32 v229, v229, v227\n"
"v_mul_f32 v230, v230, v227\n"
"v_mul_f32 v231, v231, v227\n"
"v_accvgpr_write_b32 a112, v228\n"
"v_accvgpr_write_b32 a113, v229\n"
"v_accvgpr_write_b32 a114, v230\n"
"v_accvgpr_write_b32 a115, v231\n"
"v_accvgpr_read_b32 v228, a116\n"
"v_accvgpr_read_b32 v229, a117\n"
"v_accvgpr_read_b32 v230, a118\n"
"v_accvgpr_read_b32 v231, a119\n"
"v_mul_f32 v228, v228, v227\n"
"v_mul_f32 v229, v229, v227\n"
"v_mul_f32 v230, v230, v227\n"
"v_mul_f32 v231, v231, v227\n"
"v_accvgpr_write_b32 a116, v228\n"
"v_accvgpr_write_b32 a117, v229\n"
"v_accvgpr_write_b32 a118, v230\n"
"v_accvgpr_write_b32 a119, v231\n"
"v_accvgpr_read_b32 v228, a120\n"
"v_accvgpr_read_b32 v229, a121\n"
"v_accvgpr_read_b32 v230, a122\n"
"v_accvgpr_read_b32 v231, a123\n"
"v_mul_f32 v228, v228, v227\n"
"v_mul_f32 v229, v229, v227\n"
"v_mul_f32 v230, v230, v227\n"
"v_mul_f32 v231, v231, v227\n"
"v_accvgpr_write_b32 a120, v228\n"
"v_accvgpr_write_b32 a121, v229\n"
"v_accvgpr_write_b32 a122, v230\n"
"v_accvgpr_write_b32 a123, v231\n"
"v_accvgpr_read_b32 v228, a124\n"
"v_accvgpr_read_b32 v229, a125\n"
"v_accvgpr_read_b32 v230, a126\n"
"v_accvgpr_read_b32 v231, a127\n"
"v_mul_f32 v228, v228, v227\n"
"v_mul_f32 v229, v229, v227\n"
"v_mul_f32 v230, v230, v227\n"
"v_mul_f32 v231, v231, v227\n"
"v_accvgpr_write_b32 a124, v228\n"
"v_accvgpr_write_b32 a125, v229\n"
"v_accvgpr_write_b32 a126, v230\n"
"v_accvgpr_write_b32 a127, v231\n"
"v_exp_f32 v228, v152\n"
"v_exp_f32 v229, v153\n"
"s_nop 0\n"
"v_cvt_pk_bf16_f32 v28, v228, v229\n"
"v_exp_f32 v230, v154\n"
"v_exp_f32 v231, v155\n"
"s_nop 0\n"
"v_cvt_pk_bf16_f32 v29, v230, v231\n"
"v_exp_f32 v228, v156\n"
"v_exp_f32 v229, v157\n"
"s_nop 0\n"
"v_cvt_pk_bf16_f32 v30, v228, v229\n"
"v_exp_f32 v230, v158\n"
"v_exp_f32 v231, v159\n"
"s_nop 0\n"
"v_cvt_pk_bf16_f32 v31, v230, v231\n"
"s_nop 1\n"
"v_mfma_f32_16x16x32_bf16 v[108:111], v[116:119], v[28:31], 0\n"
"s_nop 15\n"
"s_setpc_b64 s[62:63]\n"
"L_end_%=:\n"
    : "+{v0}"(tid0)
    : "{s[4:5]}"(xn), "{s[6:7]}"(tabs), "{s[8:9]}"(wq), "{s[10:11]}"(kc), "{s[12:13]}"(vc), "{s[14:15]}"(ofrag), "{s16}"(bid), "{s17}"(ldsb),
      "{s[92:93]}"(wo), "{s[94:95]}"(wg), "{s[96:97]}"(wu), "{s[98:99]}"(wd)
    : "v1","v2","v3","v4","v5","v6","v7","v8","v9","v10","v11","v12","v13","v14","v15","v16","v17","v18","v19","v20","v21","v22","v23","v24",
"v25","v26","v27","v28","v29","v30","v31","v32","v33","v34","v35","v36","v37","v38","v39","v40","v41","v42","v43","v44","v45","v46","v47","v48",
"v49","v50","v51","v52","v53","v54","v55","v56","v57","v58","v59","v60","v61","v62","v63","v64","v65","v66","v67","v68","v69","v70","v71","v72",
"v73","v74","v75","v76","v77","v78","v79","v80","v81","v82","v83","v84","v85","v86","v87","v88","v89","v90","v91","v92","v93","v94","v95","v96",
"v97","v98","v99","v100","v101","v102","v103","v104","v105","v106","v107","v108","v109","v110","v111","v112","v113","v114","v115","v116","v117","v118","v119","v120",
"v121","v122","v123","v124","v125","v126","v127","v128","v129","v130","v131","v132","v133","v134","v135","v136","v137","v138","v139","v140","v141","v142","v143","v144",
"v145","v146","v147","v148","v149","v150","v151","v152","v153","v154","v155","v156","v157","v158","v159","v160","v161","v162","v163","v164","v165","v166","v167","v168",
"v169","v170","v171","v172","v173","v174","v175","v176","v177","v178","v179","v180","v181","v182","v183","v184","v185","v186","v187","v188","v189","v190","v191","v192",
"v193","v194","v195","v196","v197","v198","v199","v200","v201","v202","v203","v204","v205","v206","v207","v208","v209","v210","v211","v212","v213","v214","v215","v216",
"v217","v218","v219","v220","v221","v222","v223","v224","v225","v226","v227","v228","v229","v230","v231","v232","v233","v234","v235","v236","v237","v238","v239","v240",
"v241","v242","v243","v244","v245","v246","v247","v248","v249","v250","v251","v252","v253","v254","v255","a0","a1","a2","a3","a4","a5","a6","a7","a8",
"a9","a10","a11","a12","a13","a14","a15","a16","a17","a18","a19","a20","a21","a22","a23","a24","a25","a26","a27","a28","a29","a30","a31","a32",
"a33","a34","a35","a36","a37","a38","a39","a40","a41","a42","a43","a44","a45","a46","a47","a48","a49","a50","a51","a52","a53","a54","a55","a56",
"a57","a58","a59","a60","a61","a62","a63","a64","a65","a66","a67","a68","a69","a70","a71","a72","a73","a74","a75","a76","a77","a78","a79","a80",
"a81","a82","a83","a84","a85","a86","a87","a88","a89","a90","a91","a92","a93","a94","a95","a96","a97","a98","a99","a100","a101","a102","a103","a104",
"a105","a106","a107","a108","a109","a110","a111","a112","a113","a114","a115","a116","a117","a118","a119","a120","a121","a122","a123","a124","a125","a126","a127","a128",
"a129","a130","a131","a132","a133","a134","a135","a136","a137","a138","a139","a140","a141","a142","a143","a144","a145","a146","a147","a148","a149","a150","a151","a152",
"a153","a154","a155","a156","a157","a158","a159","a160","a161","a162","a163","a164","a165","a166","a167","a168","a169","a170","a171","a172","a173","a174","a175","a176",
"a177","a178","a179","a180","a181","a182","a183","a184","a185","a186","a187","a188","a189","a190","a191","a192","a193","a194","a195","a196","a197","a198","a199","a200",
"a201","a202","a203","a204","a205","a206","a207","a208","a209","a210","a211","a212","a213","a214","a215","a216","a217","a218","a219","a220","a221","a222","a223","a224",
"a225","a226","a227","a228","a229","a230","a231","a232","a233","a234","a235","a236","a237","a238","a239","a240","a241","a242","a243","a244","a245","a246","a247","a248",
"a249","a250","a251","a252","a253","a254","a255","s20","s21","s22","s23","s24","s25","s26","s27","s28","s29","s30","s31","s36","s37","s38","s39","s40",
"s41","s42","s43","s44","s45","s46","s47","s48","s49","s50","s51","s52","s53","s54","s55","s56","s57","s58","s59","s60","s61","s62","s63","s64",
"s65","s66","s67","s68","s69","s70","s71","vcc","scc","memory");
    const unsigned head = (bid&7)*4 + ((bid>>3)&3), y = bid>>5;
    publish(cnt + 32*(y*4 + (head>>3)), w==0 && lane_now()==0);
  }
  {
    const unsigned sl = (bid&7)>>1, rt = (bid&1)*8 + ((bid>>3)&7), ct = bid>>6;
    const void* ofrag = ws + WS_O; const unsigned* c = cnt + 32*((rt>>1)*4 + sl);
    f32x16 a00, a01, a10, a11;
    asm volatile(
"v_mbcnt_lo_u32_b32 v75, -1, 0\n"
"v_mbcnt_hi_u32_b32 v75, -1, v75\n"
"v_bfe_u32 v72, v75, 4, 1\n"
"v_lshlrev_b32 v72, 1, v72\n"
"v_and_b32 v73, 3, v75\n"
"v_xor_b32 v72, v72, v73\n"
"v_lshlrev_b32 v72, 4, v72\n"
"v_lshrrev_b32 v73, 5, v75\n"
"v_lshl_add_u32 v72, v73, 6, v72\n"
"v_bfe_u32 v73, v75, 2, 3\n"
"v_lshl_add_u32 v72, v73, 10, v72\n"
"v_add_u32 v64, 0x400, v72\n"
"v_add_u32 v65, 0x80, v72\n"
"v_add_u32 v66, 0x1c00, v72\n"
"v_add_u32 v67, 0x1880, v72\n"
"s_bfe_u32 s35, s16, 0x20001\n"
"s_lshl_b32 s35, s35, 20\n"
"s_lshr_b32 s36, s16, 6\n"
"s_lshl_b32 s36, s36, 8\n"
"s_add_u32 s35, s35, s36\n"
"s_sub_u32 s36, 0x100400, s36\n"
"s_add_u32 s24, s6, s35\n"
"s_addc_u32 s25, s7, 0\n"
"s_sub_u32 s24, s24, 0x400\n"
"s_subb_u32 s25, s25, 0\n"
"s_and_b32 s25, s25, 0xffff\n"
"s_mov_b32 s26, s36\n"
"s_mov_b32 s27, 0x20000\n"
"s_lshl_b32 s30, s17, 14\n"
"s_add_u32 s30, s30, 0x70000\n"
"s_lshl_b32 s35, s17, 12\n"
"s_add_u32 s29, s18, s35\n"
"v_lshlrev_b32 v78, 4, v75\n"
"v_add_u32 v79, 0x1f800, v78\n"
"s_and_b32 s35, s16, 1\n"
"s_lshl_b32 s35, s35, 3\n"
"s_bfe_u32 s36, s16, 0x30003\n"
"s_add_u32 s35, s35, s36\n"
"s_lshl_b32 s35, s35, 2\n"
"s_add_u32 s35, s35, s17\n"
"s_lshl_b32 s35, s35, 18\n"
"s_bfe_u32 s36, s16, 0x20001\n"
"s_lshl_b32 s36, s36, 15\n"
"s_add_u32 s35, s35, s36\n"
"s_add_u32 s20, s4, s35\n"
"s_addc_u32 s21, s5, 0\n"
"s_and_b32 s21, s21, 0xffff\n"
"s_mov_b32 s22, 0x28000\n"
"s_mov_b32 s23, 0x20000\n"
"s_mov_b32 s31, 0\n"
"s_add_u32 s37, s29, 0x1c000\n"
"s_lshr_b32 s35, s17, 1\n"
"s_lshl_b32 s35, s35, 10\n"
"s_add_u32 s35, s35, s18\n"
"v_lshrrev_b32 v72, 5, v75\n"
"v_lshlrev_b32 v72, 11, v72\n"
"v_bfe_u32 v73, v75, 2, 3\n"
"v_lshl_add_u32 v72, v73, 6, v72\n"
"v_and_b32 v73, 3, v75\n"
"v_lshl_add_u32 v72, v73, 3, v72\n"
"v_bfe_u32 v73, v75, 4, 1\n"
"v_lshl_add_u32 v72, v73, 5, v72\n"
"v_add_u32 v68, s35, v72\n"
"v_xor_b32 v69, 32, v68\n"
"v_add_u32 v70, 0xc000, v68\n"
"v_add_u32 v71, 0xc000, v69\n"
"v_add_u32 v76, 0x18000, v68\n"
"v_add_u32 v88, 0x18000, v69\n"
"s_and_b32 s35, s17, 1\n"
"s_lshl_b32 s35, s35, 13\n"
"s_add_u32 s35, s35, s18\n"
"s_add_u32 s35, s35, 0x1c000\n"
"v_add_u32 v77, s35, v78\n"
"v_mbcnt_lo_u32_b32 v107, -1, 0\n"
"v_mbcnt_hi_u32_b32 v107, -1, v107\n"
"v_add_u32 v104, 0, v107\n"
"v_mul_u32_u24 v105, 0x124a, v104\n"
"v_lshrrev_b32 v105, 16, v105\n"
"v_mul_u32_u24 v106, 0xd20, v105\n"
"v_lshl_add_u32 v105, v104, 4, v106\n"
"v_add_u32 v96, 0xc00, v105\n"
"v_add_u32 v104, 64, v107\n"
"v_mul_u32_u24 v105, 0x124a, v104\n"
"v_lshrrev_b32 v105, 16, v105\n"
"v_mul_u32_u24 v106, 0xd20, v105\n"
"v_lshl_add_u32 v105, v104, 4, v106\n"
"v_add_u32 v97, 0x800, v105\n"
"v_add_u32 v104, 0x80, v107\n"
"v_mul_u32_u24 v105, 0x124a, v104\n"
"v_lshrrev_b32 v105, 16, v105\n"
"v_mul_u32_u24 v106, 0xd20, v105\n"
"v_lshl_add_u32 v105, v104, 4, v106\n"
"v_add_u32 v98, 0x400, v105\n"
"v_add_u32 v104, 0xc0, v107\n"
"v_mul_u32_u24 v105, 0x124a, v104\n"
"v_lshrrev_b32 v105, 16, v105\n"
"v_mul_u32_u24 v106, 0xd20, v105\n"
"v_lshl_add_u32 v105, v104, 4, v106\n"
"v_add_u32 v99, 0, v105\n"
"v_add_u32 v104, 0x100, v107\n"
"v_mul_u32_u24 v105, 0x124a, v104\n"
"v_lshrrev_b32 v105, 16, v105\n"
"v_mul_u32_u24 v106, 0xd20, v105\n"
"v_lshl_add_u32 v105, v104, 4, v106\n"
"v_add_u32 v100, 0xc00, v105\n"
"v_add_u32 v104, 0x140, v107\n"
"v_mul_u32_u24 v105, 0x124a, v104\n"
"v_lshrrev_b32 v105, 16, v105\n"
"v_mul_u32_u24 v106, 0xd20, v105\n"
"v_lshl_add_u32 v105, v104, 4, v106\n"
"v_add_u32 v101, 0x800, v105\n"
"v_add_u32 v104, 0x180, v107\n"
"v_mul_u32_u24 v105, 0x124a, v104\n"
"v_lshrrev_b32 v105, 16, v105\n"
"v_mul_u32_u24 v106, 0xd20, v105\n"
"v_lshl_add_u32 v105, v104, 4, v106\n"
"v_add_u32 v102, 0x400, v105\n"
"s_cmp_lt_u32 s17, 2\n"
"s_cselect_b32 s46, s8, s10\n"
"s_cselect_b32 s47, s9, s11\n"
"s_bfe_u32 s38, s16, 0x20001\n"
"s_lshl_b32 s38, s38, 2\n"
"s_lshr_b32 s39, s16, 6\n"
"s_add_u32 s38, s38, s39\n"
"s_mul_i32 s38, s38, 0xe0\n"
"s_sub_u32 s39, 0x1c0c00, s38\n"
"s_add_u32 s40, s46, s38\n"
"s_addc_u32 s41, s47, 0\n"
"s_sub_u32 s40, s40, 0xc00\n"
"s_subb_u32 s41, s41, 0\n"
"s_and_b32 s41, s41, 0xffff\n"
"s_mov_b32 s42, s39\n"
"s_mov_b32 s43, 0x20000\n"
"s_and_b32 s38, s17, 1\n"
"s_mul_i32 s45, s38, 0x1c000\n"
"s_mul_i32 s39, s38, 0x1c00\n"
"s_lshr_b32 s38, s17, 1\n"
"s_mul_i32 s38, s38, 0x3880\n"
"s_add_u32 s44, s39, s38\n"
"s_add_u32 s44, s44, s18\n"
"s_cmp_lg_u32 s17, 0\n"
"s_cbranch_scc1 L_op_done_%=\n"
"v_mov_b32 v81, 0\n"
"s_mov_b32 s36, 0x61a80\n"
"L_op_poll_%=:\n"
"global_load_dword v80, v81, s[12:13] sc1\n"
"s_waitcnt vmcnt(0)\n"
"v_readfirstlane_b32 s35, v80\n"
"s_cmp_ge_u32 s35, 8\n"
"s_cbranch_scc1 L_op_done_%=\n"
"s_sub_u32 s36, s36, 1\n"
"s_cmp_eq_u32 s36, 0\n"
"s_cbranch_scc1 L_op_done_%=\n"
"s_sleep 1\n"
"s_branch L_op_poll_%=\n"
"L_op_done_%=:\n"
"s_barrier\n"
"s_add_u32 m0, s37, 0\n"
"s_nop 0\n"
"buffer_load_dwordx4 v78, s[20:23], s31 offen offset:0 sc1 lds\n"
"buffer_load_dwordx4 v78, s[20:23], s31 offen offset:1024 sc1 lds\n"
"buffer_load_dwordx4 v79, s[20:23], s31 offen offset:2048 sc1 lds\n"
"buffer_load_dwordx4 v79, s[20:23], s31 offen offset:3072 sc1 lds\n"
"s_add_u32 s31, s31, 0x800\n"
"s_add_u32 m0, s37, 0x4000\n"
"s_nop 0\n"
"buffer_load_dwordx4 v78, s[20:23], s31 offen offset:0 sc1 lds\n"
"buffer_load_dwordx4 v78, s[20:23], s31 offen offset:1024 sc1 lds\n"
"buffer_load_dwordx4 v79, s[20:23], s31 offen offset:2048 sc1 lds\n"
"buffer_load_dwordx4 v79, s[20:23], s31 offen offset:3072 sc1 lds\n"
"s_add_u32 s31, s31, 0x800\n"
"s_add_u32 m0, s37, 0x8000\n"
"s_nop 0\n"
"buffer_load_dwordx4 v78, s[20:23], s31 offen offset:0 sc1 lds\n"
"buffer_load_dwordx4 v78, s[20:23], s31 offen offset:1024 sc1 lds\n"
"buffer_load_dwordx4 v79, s[20:23], s31 offen offset:2048 sc1 lds\n"
"buffer_load_dwordx4 v79, s[20:23], s31 offen offset:3072 sc1 lds\n"
"s_add_u32 s31, s31, 0x800\n"
"s_waitcnt vmcnt(8)\n"
"s_barrier\n"
"ds_read_b128 a[64:67], v77 offset:0\n"
"ds_read_b128 a[68:71], v77 offset:2048\n"
"ds_read_b128 a[72:75], v77 offset:4096\n"
"ds_read_b128 a[76:79], v77 offset:6144\n"
"ds_read_b64_tr_b16 a[0:1], v68 offset:0\n"
"ds_read_b64_tr_b16 a[2:3], v68 offset:4096\n"
"ds_read_b64_tr_b16 a[4:5], v69 offset:0\n"
"ds_read_b64_tr_b16 a[6:7], v69 offset:4096\n"
"ds_read_b64_tr_b16 a[8:9], v68 offset:512\n"
"ds_read_b64_tr_b16 a[10:11], v68 offset:4608\n"
"ds_read_b64_tr_b16 a[12:13], v69 offset:512\n"
"ds_read_b64_tr_b16 a[14:15], v69 offset:4608\n"
"ds_read_b128 a[80:83], v77 offset:1024\n"
"ds_read_b128 a[84:87], v77 offset:3072\n"
"ds_read_b128 a[88:91], v77 offset:5120\n"
"ds_read_b128 a[92:95], v77 offset:7168\n"
"ds_read_b64_tr_b16 a[16:17], v68 offset:8192\n"
"ds_read_b64_tr_b16 a[18:19], v68 offset:12288\n"
"ds_read_b64_tr_b16 a[20:21], v69 offset:8192\n"
"ds_read_b64_tr_b16 a[22:23], v69 offset:12288\n"
"ds_read_b64_tr_b16 a[24:25], v68 offset:8704\n"
"ds_read_b64_tr_b16 a[26:27], v68 offset:12800\n"
"ds_read_b64_tr_b16 a[28:29], v69 offset:8704\n"
"ds_read_b64_tr_b16 a[30:31], v69 offset:12800\n"
"s_waitcnt lgkmcnt(0)\n"
"v_mfma_f32_16x16x32_bf16 v[0:3], a[0:3], a[64:67], 0\n"
"s_waitcnt vmcnt(4)\n"
"s_barrier\n"
"v_mfma_f32_16x16x32_bf16 v[0:3], a[16:19], a[80:83], v[0:3]\n"
"ds_read_b128 a[96:99], v77 offset:16384\n"
"s_add_u32 m0, s37, 0\n"
"v_mfma_f32_16x16x32_bf16 v[4:7], a[4:7], a[64:67], 0\n"
"ds_read_b128 a[100:103], v77 offset:18432\n"
"buffer_load_dwordx4 v78, s[20:23], s31 offen offset:0 sc1 lds\n"
"v_mfma_f32_16x16x32_bf16 v[4:7], a[20:23], a[80:83], v[4:7]\n"
"ds_read_b128 a[104:107], v77 offset:20480\n"
"v_mfma_f32_16x16x32_bf16 v[8:11], a[8:11], a[64:67], 0\n"
"ds_read_b128 a[108:111], v77 offset:22528\n"
"v_mfma_f32_16x16x32_bf16 v[8:11], a[24:27], a[80:83], v[8:11]\n"
"ds_read_b64_tr_b16 a[32:33], v68 offset:16384\n"
"buffer_load_dwordx4 v78, s[20:23], s31 offen offset:1024 sc1 lds\n"
"v_mfma_f32_16x16x32_bf16 v[12:15], a[12:15], a[64:67], 0\n"
"ds_read_b64_tr_b16 a[34:35], v68 offset:20480\n"
"v_mfma_f32_16x16x32_bf16 v[12:15], a[28:31], a[80:83], v[12:15]\n"
"ds_read_b64_tr_b16 a[36:37], v69 offset:16384\n"
"v_mfma_f32_16x16x32_bf16 v[16:19], a[0:3], a[68:71], 0\n"
"ds_read_b64_tr_b16 a[38:39], v69 offset:20480\n"
"buffer_load_dwordx4 v79, s[20:23], s31 offen offset:2048 sc1 lds\n"
"v_mfma_f32_16x16x32_bf16 v[16:19], a[16:19], a[84:87], v[16:19]\n"
"ds_read_b64_tr_b16 a[40:41], v68 offset:16896\n"
"v_mfma_f32_16x16x32_bf16 v[20:23], a[4:7], a[68:71], 0\n"
"ds_read_b64_tr_b16 a[42:43], v68 offset:20992\n"
"v_mfma_f32_16x16x32_bf16 v[20:23], a[20:23], a[84:87], v[20:23]\n"
"ds_read_b64_tr_b16 a[44:45], v69 offset:16896\n"
"buffer_load_dwordx4 v79, s[20:23], s31 offen offset:3072 sc1 lds\n"
"v_mfma_f32_16x16x32_bf16 v[24:27], a[8:11], a[68:71], 0\n"
"ds_read_b64_tr_b16 a[46:47], v69 offset:20992\n"
"s_add_u32 s31, s31, 0x800\n"
"v_mfma_f32_16x16x32_bf16 v[24:27], a[24:27], a[84:87], v[24:27]\n"
"ds_read_b128 a[112:115], v77 offset:17408\n"
"v_mfma_f32_16x16x32_bf16 v[28:31], a[12:15], a[68:71], 0\n"
"ds_read_b128 a[116:119], v77 offset:19456\n"
"s_add_u32 m0, s29, 0\n"
"v_mfma_f32_16x16x32_bf16 v[28:31], a[28:31], a[84:87], v[28:31]\n"
"ds_read_b128 a[120:123], v77 offset:21504\n"
"v_mfma_f32_16x16x32_bf16 v[32:35], a[0:3], a[72:75], 0\n"
"ds_read_b128 a[124:127], v77 offset:23552\n"
"buffer_load_dwordx4 v64, s[24:27], s30 offen offset:0 lds\n"
"v_mfma_f32_16x16x32_bf16 v[32:35], a[16:19], a[88:91], v[32:35]\n"
"ds_read_b64_tr_b16 a[48:49], v68 offset:24576\n"
"v_mfma_f32_16x16x32_bf16 v[36:39], a[4:7], a[72:75], 0\n"
"ds_read_b64_tr_b16 a[50:51], v68 offset:28672\n"
"v_mfma_f32_16x16x32_bf16 v[36:39], a[20:23], a[88:91], v[36:39]\n"
"ds_read_b64_tr_b16 a[52:53], v69 offset:24576\n"
"v_mfma_f32_16x16x32_bf16 v[40:43], a[8:11], a[72:75], 0\n"
"ds_read_b64_tr_b16 a[54:55], v69 offset:28672\n"
"buffer_load_dwordx4 v65, s[24:27], s30 offen offset:1024 lds\n"
"v_mfma_f32_16x16x32_bf16 v[40:43], a[24:27], a[88:91], v[40:43]\n"
"ds_read_b64_tr_b16 a[56:57], v68 offset:25088\n"
"v_mfma_f32_16x16x32_bf16 v[44:47], a[12:15], a[72:75], 0\n"
"ds_read_b64_tr_b16 a[58:59], v68 offset:29184\n"
"v_mfma_f32_16x16x32_bf16 v[44:47], a[28:31], a[88:91], v[44:47]\n"
"ds_read_b64_tr_b16 a[60:61], v69 offset:25088\n"
"v_mfma_f32_16x16x32_bf16 v[48:51], a[0:3], a[76:79], 0\n"
"ds_read_b64_tr_b16 a[62:63], v69 offset:29184\n"
"buffer_load_dwordx4 v66, s[24:27], s30 offen offset:2048 lds\n"
"v_mfma_f32_16x16x32_bf16 v[48:51], a[16:19], a[92:95], v[48:51]\n"
"v_mfma_f32_16x16x32_bf16 v[52:55], a[4:7], a[76:79], 0\n"
"v_mfma_f32_16x16x32_bf16 v[52:55], a[20:23], a[92:95], v[52:55]\n"
"v_mfma_f32_16x16x32_bf16 v[56:59], a[8:11], a[76:79], 0\n"
"buffer_load_dwordx4 v67, s[24:27], s30 offen offset:3072 lds\n"
"v_mfma_f32_16x16x32_bf16 v[56:59], a[24:27], a[92:95], v[56:59]\n"
"v_mfma_f32_16x16x32_bf16 v[60:63], a[12:15], a[76:79], 0\n"
"s_add_u32 s30, s30, 0x10000\n"
"v_mfma_f32_16x16x32_bf16 v[60:63], a[28:31], a[92:95], v[60:63]\n"
"s_waitcnt lgkmcnt(0)\n"
"v_mfma_f32_16x16x32_bf16 v[0:3], a[32:35], a[96:99], v[0:3]\n"
"s_waitcnt vmcnt(8)\n"
"s_barrier\n"
"v_mfma_f32_16x16x32_bf16 v[0:3], a[48:51], a[112:115], v[0:3]\n"
"ds_read_b128 a[64:67], v77 offset:32768\n"
"s_add_u32 m0, s37, 0x4000\n"
"v_mfma_f32_16x16x32_bf16 v[4:7], a[36:39], a[96:99], v[4:7]\n"
"ds_read_b128 a[68:71], v77 offset:34816\n"
"buffer_load_dwordx4 v78, s[20:23], s31 offen offset:0 sc1 lds\n"
"v_mfma_f32_16x16x32_bf16 v[4:7], a[52:55], a[112:115], v[4:7]\n"
"ds_read_b128 a[72:75], v77 offset:36864\n"
"v_mfma_f32_16x16x32_bf16 v[8:11], a[40:43], a[96:99], v[8:11]\n"
"ds_read_b128 a[76:79], v77 offset:38912\n"
"v_mfma_f32_16x16x32_bf16 v[8:11], a[56:59], a[112:115], v[8:11]\n"
"ds_read_b64_tr_b16 a[0:1], v68 offset:32768\n"
"buffer_load_dwordx4 v78, s[20:23], s31 offen offset:1024 sc1 lds\n"
"v_mfma_f32_16x16x32_bf16 v[12:15], a[44:47], a[96:99], v[12:15]\n"
"ds_read_b64_tr_b16 a[2:3], v68 offset:36864\n"
"v_mfma_f32_16x16x32_bf16 v[12:15], a[60:63], a[112:115], v[12:15]\n"
"ds_read_b64_tr_b16 a[4:5], v69 offset:32768\n"
"v_mfma_f32_16x16x32_bf16 v[16:19], a[32:35], a[100:103], v[16:19]\n"
"ds_read_b64_tr_b16 a[6:7], v69 offset:36864\n"
"buffer_load_dwordx4 v79, s[20:23], s31 offen offset:2048 sc1 lds\n"
"v_mfma_f32_16x16x32_bf16 v[16:19], a[48:51], a[116:119], v[16:19]\n"
"ds_read_b64_tr_b16 a[8:9], v68 offset:33280\n"
"v_mfma_f32_16x16x32_bf16 v[20:23], a[36:39], a[100:103], v[20:23]\n"
"ds_read_b64_tr_b16 a[10:11], v68 offset:37376\n"
"v_mfma_f32_16x16x32_bf16 v[20:23], a[52:55], a[116:119], v[20:23]\n"
"ds_read_b64_tr_b16 a[12:13], v69 offset:33280\n"
"buffer_load_dwordx4 v79, s[20:23], s31 offen offset:3072 sc1 lds\n"
"v_mfma_f32_16x16x32_bf16 v[24:27], a[40:43], a[100:103], v[24:27]\n"
"ds_read_b64_tr_b16 a[14:15], v69 offset:37376\n"
"s_add_u32 s31, s31, 0x800\n"
"v_mfma_f32_16x16x32_bf16 v[24:27], a[56:59], a[116:119], v[24:27]\n"
"ds_read_b128 a[80:83], v77 offset:33792\n"
"v_mfma_f32_16x16x32_bf16 v[28:31], a[44:47], a[100:103], v[28:31]\n"
"ds_read_b128 a[84:87], v77 offset:35840\n"
"s_add_u32 m0, s29, 0x4000\n"
"v_mfma_f32_16x16x32_bf16 v[28:31], a[60:63], a[116:119], v[28:31]\n"
"ds_read_b128 a[88:91], v77 offset:37888\n"
"v_mfma_f32_16x16x32_bf16 v[32:35], a[32:35], a[104:107], v[32:35]\n"
"ds_read_b128 a[92:95], v77 offset:39936\n"
"buffer_load_dwordx4 v64, s[24:27], s30 offen offset:0 lds\n"
"v_mfma_f32_16x16x32_bf16 v[32:35], a[48:51], a[120:123], v[32:35]\n"
"ds_read_b64_tr_b16 a[16:17], v68 offset:40960\n"
"v_mfma_f32_16x16x32_bf16 v[36:39], a[36:39], a[104:107], v[36:39]\n"
"ds_read_b64_tr_b16 a[18:19], v68 offset:45056\n"
"v_mfma_f32_16x16x32_bf16 v[36:39], a[52:55], a[120:123], v[36:39]\n"
"ds_read_b64_tr_b16 a[20:21], v69 offset:40960\n"
"v_mfma_f32_16x16x32_bf16 v[40:43], a[40:43], a[104:107], v[40:43]\n"
"ds_read_b64_tr_b16 a[22:23], v69 offset:45056\n"
"buffer_load_dwordx4 v65, s[24:27], s30 offen offset:1024 lds\n"
"v_mfma_f32_16x16x32_bf16 v[40:43], a[56:59], a[120:123], v[40:43]\n"
"ds_read_b64_tr_b16 a[24:25], v68 offset:41472\n"
"v_mfma_f32_16x16x32_bf16 v[44:47], a[44:47], a[104:107], v[44:47]\n"
"ds_read_b64_tr_b16 a[26:27], v68 offset:45568\n"
"v_mfma_f32_16x16x32_bf16 v[44:47], a[60:63], a[120:123], v[44:47]\n"
"ds_read_b64_tr_b16 a[28:29], v69 offset:41472\n"
"v_mfma_f32_16x16x32_bf16 v[48:51], a[32:35], a[108:111], v[48:51]\n"
"ds_read_b64_tr_b16 a[30:31], v69 offset:45568\n"
"buffer_load_dwordx4 v66, s[24:27], s30 offen offset:2048 lds\n"
"v_mfma_f32_16x16x32_bf16 v[48:51], a[48:51], a[124:127], v[48:51]\n"
"v_mfma_f32_16x16x32_bf16 v[52:55], a[36:39], a[108:111], v[52:55]\n"
"v_mfma_f32_16x16x32_bf16 v[52:55], a[52:55], a[124:127], v[52:55]\n"
"v_mfma_f32_16x16x32_bf16 v[56:59], a[40:43], a[108:111], v[56:59]\n"
"buffer_load_dwordx4 v67, s[24:27], s30 offen offset:3072 lds\n"
"v_mfma_f32_16x16x32_bf16 v[56:59], a[56:59], a[124:127], v[56:59]\n"
"v_mfma_f32_16x16x32_bf16 v[60:63], a[44:47], a[108:111], v[60:63]\n"
"s_add_u32 s30, s30, 0x10000\n"
"v_mfma_f32_16x16x32_bf16 v[60:63], a[60:63], a[124:127], v[60:63]\n"
"s_waitcnt lgkmcnt(0)\n"
"v_mfma_f32_16x16x32_bf16 v[0:3], a[0:3], a[64:67], v[0:3]\n"
"s_waitcnt vmcnt(12)\n"
"s_barrier\n"
"v_mfma_f32_16x16x32_bf16 v[0:3], a[16:19], a[80:83], v[0:3]\n"
"ds_read_b128 a[96:99], v77 offset:0\n"
"s_add_u32 m0, s37, 0x8000\n"
"v_mfma_f32_16x16x32_bf16 v[4:7], a[4:7], a[64:67], v[4:7]\n"
"ds_read_b128 a[100:103], v77 offset:2048\n"
"buffer_load_dwordx4 v78, s[20:23], s31 offen offset:0 sc1 lds\n"
"v_mfma_f32_16x16x32_bf16 v[4:7], a[20:23], a[80:83], v[4:7]\n"
"ds_read_b128 a[104:107], v77 offset:4096\n"
"v_mfma_f32_16x16x32_bf16 v[8:11], a[8:11], a[64:67], v[8:11]\n"
"ds_read_b128 a[108:111], v77 offset:6144\n"
"v_mfma_f32_16x16x32_bf16 v[8:11], a[24:27], a[80:83], v[8:11]\n"
"ds_read_b64_tr_b16 a[32:33], v70 offset:0\n"
"buffer_load_dwordx4 v78, s[20:23], s31 offen offset:1024 sc1 lds\n"
"v_mfma_f32_16x16x32_bf16 v[12:15], a[12:15], a[64:67], v[12:15]\n"
"ds_read_b64_tr_b16 a[34:35], v70 offset:4096\n"
"v_mfma_f32_16x16x32_bf16 v[12:15], a[28:31], a[80:83], v[12:15]\n"
"ds_read_b64_tr_b16 a[36:37], v71 offset:0\n"
"v_mfma_f32_16x16x32_bf16 v[16:19], a[0:3], a[68:71], v[16:19]\n"
"ds_read_b64_tr_b16 a[38:39], v71 offset:4096\n"
"buffer_load_dwordx4 v79, s[20:23], s31 offen offset:2048 sc1 lds\n"
"v_mfma_f32_16x16x32_bf16 v[16:19], a[16:19], a[84:87], v[16:19]\n"
"ds_read_b64_tr_b16 a[40:41], v70 offset:512\n"
"v_mfma_f32_16x16x32_bf16 v[20:23], a[4:7], a[68:71], v[20:23]\n"
"ds_read_b64_tr_b16 a[42:43], v70 offset:4608\n"
"v_mfma_f32_16x16x32_bf16 v[20:23], a[20:23], a[84:87], v[20:23]\n"
"ds_read_b64_tr_b16 a[44:45], v71 offset:512\n"
"buffer_load_dwordx4 v79, s[20:23], s31 offen offset:3072 sc1 lds\n"
"v_mfma_f32_16x16x32_bf16 v[24:27], a[8:11], a[68:71], v[24:27]\n"
"ds_read_b64_tr_b16 a[46:47], v71 offset:4608\n"
"s_add_u32 s31, s31, 0x800\n"
"v_mfma_f32_16x16x32_bf16 v[24:27], a[24:27], a[84:87], v[24:27]\n"
"ds_read_b128 a[112:115], v77 offset:1024\n"
"v_mfma_f32_16x16x32_bf16 v[28:31], a[12:15], a[68:71], v[28:31]\n"
"ds_read_b128 a[116:119], v77 offset:3072\n"
"s_add_u32 m0, s29, 0x8000\n"
"v_mfma_f32_16x16x32_bf16 v[28:31], a[28:31], a[84:87], v[28:31]\n"
"ds_read_b128 a[120:123], v77 offset:5120\n"
"v_mfma_f32_16x16x32_bf16 v[32:35], a[0:3], a[72:75], v[32:35]\n"
"ds_read_b128 a[124:127], v77 offset:7168\n"
"buffer_load_dwordx4 v64, s[24:27], s30 offen offset:0 lds\n"
"v_mfma_f32_16x16x32_bf16 v[32:35], a[16:19], a[88:91], v[32:35]\n"
"ds_read_b64_tr_b16 a[48:49], v70 offset:8192\n"
"v_mfma_f32_16x16x32_bf16 v[36:39], a[4:7], a[72:75], v[36:39]\n"
"ds_read_b64_tr_b16 a[50:51], v70 offset:12288\n"
"v_mfma_f32_16x16x32_bf16 v[36:39], a[20:23], a[88:91], v[36:39]\n"
"ds_read_b64_tr_b16 a[52:53], v71 offset:8192\n"
"v_mfma_f32_16x16x32_bf16 v[40:43], a[8:11], a[72:75], v[40:43]\n"
"ds_read_b64_tr_b16 a[54:55], v71 offset:12288\n"
"buffer_load_dwordx4 v65, s[24:27], s30 offen offset:1024 lds\n"
"v_mfma_f32_16x16x32_bf16 v[40:43], a[24:27], a[88:91], v[40:43]\n"
"ds_read_b64_tr_b16 a[56:57], v70 offset:8704\n"
"v_mfma_f32_16x16x32_bf16 v[44:47], a[12:15], a[72:75], v[44:47]\n"
"ds_read_b64_tr_b16 a[58:59], v70 offset:12800\n"
"v_mfma_f32_16x16x32_bf16 v[44:47], a[28:31], a[88:91], v[44:47]\n"
"ds_read_b64_tr_b16 a[60:61], v71 offset:8704\n"
"v_mfma_f32_16x16x32_bf16 v[48:51], a[0:3], a[76:79], v[48:51]\n"
"ds_read_b64_tr_b16 a[62:63], v71 offset:12800\n"
"buffer_load_dwordx4 v66, s[24:27], s30 offen offset:2048 lds\n"
"v_mfma_f32_16x16x32_bf16 v[48:51], a[16:19], a[92:95], v[48:51]\n"
"v_mfma_f32_16x16x32_bf16 v[52:55], a[4:7], a[76:79], v[52:55]\n"
"v_mfma_f32_16x16x32_bf16 v[52:55], a[20:23], a[92:95], v[52:55]\n"
"v_mfma_f32_16x16x32_bf16 v[56:59], a[8:11], a[76:79], v[56:59]\n"
"buffer_load_dwordx4 v67, s[24:27], s30 offen offset:3072 lds\n"
"v_mfma_f32_16x16x32_bf16 v[56:59], a[24:27], a[92:95], v[56:59]\n"
"v_mfma_f32_16x16x32_bf16 v[60:63], a[12:15], a[76:79], v[60:63]\n"
"s_add_u32 s30, s30, 0x10000\n"
"v_mfma_f32_16x16x32_bf16 v[60:63], a[28:31], a[92:95], v[60:63]\n"
"s_waitcnt lgkmcnt(0)\n"
"v_mfma_f32_16x16x32_bf16 v[0:3], a[32:35], a[96:99], v[0:3]\n"
"s_waitcnt vmcnt(12)\n"
"s_barrier\n"
"v_mfma_f32_16x16x32_bf16 v[0:3], a[48:51], a[112:115], v[0:3]\n"
"ds_read_b128 a[64:67], v77 offset:16384\n"
"s_add_u32 m0, s37, 0\n"
"v_mfma_f32_16x16x32_bf16 v[4:7], a[36:39], a[96:99], v[4:7]\n"
"ds_read_b128 a[68:71], v77 offset:18432\n"
"buffer_load_dwordx4 v78, s[20:23], s31 offen offset:0 sc1 lds\n"
"v_mfma_f32_16x16x32_bf16 v[4:7], a[52:55], a[112:115], v[4:7]\n"
"ds_read_b128 a[72:75], v77 offset:20480\n"
"v_mfma_f32_16x16x32_bf16 v[8:11], a[40:43], a[96:99], v[8:11]\n"
"ds_read_b128 a[76:79], v77 offset:22528\n"
"v_mfma_f32_16x16x32_bf16 v[8:11], a[56:59], a[112:115], v[8:11]\n"
"ds_read_b64_tr_b16 a[0:1], v70 offset:16384\n"
"buffer_load_dwordx4 v78, s[20:23], s31 offen offset:1024 sc1 lds\n"
"v_mfma_f32_16x16x32_bf16 v[12:15], a[44:47], a[96:99], v[12:15]\n"
"ds_read_b64_tr_b16 a[2:3], v70 offset:20480\n"
"v_mfma_f32_16x16x32_bf16 v[12:15], a[60:63], a[112:115], v[12:15]\n"
"ds_read_b64_tr_b16 a[4:5], v71 offset:16384\n"
"v_mfma_f32_16x16x32_bf16 v[16:19], a[32:35], a[100:103], v[16:19]\n"
"ds_read_b64_tr_b16 a[6:7], v71 offset:20480\n"
"buffer_load_dwordx4 v79, s[20:23], s31 offen offset:2048 sc1 lds\n"
"v_mfma_f32_16x16x32_bf16 v[16:19], a[48:51], a[116:119], v[16:19]\n"
"ds_read_b64_tr_b16 a[8:9], v70 offset:16896\n"
"v_mfma_f32_16x16x32_bf16 v[20:23], a[36:39], a[100:103], v[20:23]\n"
"ds_read_b64_tr_b16 a[10:11], v70 offset:20992\n"
"v_mfma_f32_16x16x32_bf16 v[20:23], a[52:55], a[116:119], v[20:23]\n"
"ds_read_b64_tr_b16 a[12:13], v71 offset:16896\n"
"buffer_load_dwordx4 v79, s[20:23], s31 offen offset:3072 sc1 lds\n"
"v_mfma_f32_16x16x32_bf16 v[24:27], a[40:43], a[100:103], v[24:27]\n"
"ds_read_b64_tr_b16 a[14:15], v71 offset:20992\n"
"s_add_u32 s31, s31, 0x800\n"
"v_mfma_f32_16x16x32_bf16 v[24:27], a[56:59], a[116:119], v[24:27]\n"
"ds_read_b128 a[80:83], v77 offset:17408\n"
"v_mfma_f32_16x16x32_bf16 v[28:31], a[44:47], a[100:103], v[28:31]\n"
"ds_read_b128 a[84:87], v77 offset:19456\n"
"s_add_u32 m0, s29, 0xc000\n"
"v_mfma_f32_16x16x32_bf16 v[28:31], a[60:63], a[116:119], v[28:31]\n"
"ds_read_b128 a[88:91], v77 offset:21504\n"
"v_mfma_f32_16x16x32_bf16 v[32:35], a[32:35], a[104:107], v[32:35]\n"
"ds_read_b128 a[92:95], v77 offset:23552\n"
"buffer_load_dwordx4 v64, s[24:27], s30 offen offset:0 lds\n"
"v_mfma_f32_16x16x32_bf16 v[32:35], a[48:51], a[120:123], v[32:35]\n"
"ds_read_b64_tr_b16 a[16:17], v70 offset:24576\n"
"v_mfma_f32_16x16x32_bf16 v[36:39], a[36:39], a[104:107], v[36:39]\n"
"ds_read_b64_tr_b16 a[18:19], v70 offset:28672\n"
"v_mfma_f32_16x16x32_bf16 v[36:39], a[52:55], a[120:123], v[36:39]\n"
"ds_read_b64_tr_b16 a[20:21], v71 offset:24576\n"
"v_mfma_f32_16x16x32_bf16 v[40:43], a[40:43], a[104:107], v[40:43]\n"
"ds_read_b64_tr_b16 a[22:23], v71 offset:28672\n"
"buffer_load_dwordx4 v65, s[24:27], s30 offen offset:1024 lds\n"
"v_mfma_f32_16x16x32_bf16 v[40:43], a[56:59], a[120:123], v[40:43]\n"
"ds_read_b64_tr_b16 a[24:25], v70 offset:25088\n"
"v_mfma_f32_16x16x32_bf16 v[44:47], a[44:47], a[104:107], v[44:47]\n"
"ds_read_b64_tr_b16 a[26:27], v70 offset:29184\n"
"v_mfma_f32_16x16x32_bf16 v[44:47], a[60:63], a[120:123], v[44:47]\n"
"ds_read_b64_tr_b16 a[28:29], v71 offset:25088\n"
"v_mfma_f32_16x16x32_bf16 v[48:51], a[32:35], a[108:111], v[48:51]\n"
"ds_read_b64_tr_b16 a[30:31], v71 offset:29184\n"
"buffer_load_dwordx4 v66, s[24:27], s30 offen offset:2048 lds\n"
"v_mfma_f32_16x16x32_bf16 v[48:51], a[48:51], a[124:127], v[48:51]\n"
"v_mfma_f32_16x16x32_bf16 v[52:55], a[36:39], a[108:111], v[52:55]\n"
"v_mfma_f32_16x16x32_bf16 v[52:55], a[52:55], a[124:127], v[52:55]\n"
"v_mfma_f32_16x16x32_bf16 v[56:59], a[40:43], a[108:111], v[56:59]\n"
"buffer_load_dwordx4 v67, s[24:27], s30 offen offset:3072 lds\n"
"v_mfma_f32_16x16x32_bf16 v[56:59], a[56:59], a[124:127], v[56:59]\n"
"v_mfma_f32_16x16x32_bf16 v[60:63], a[44:47], a[108:111], v[60:63]\n"
"s_add_u32 s30, s30, 0x10000\n"
"v_mfma_f32_16x16x32_bf16 v[60:63], a[60:63], a[124:127], v[60:63]\n"
"s_waitcnt lgkmcnt(0)\n"
"v_mfma_f32_16x16x32_bf16 v[0:3], a[0:3], a[64:67], v[0:3]\n"
"s_waitcnt vmcnt(12)\n"
"s_barrier\n"
"v_mfma_f32_16x16x32_bf16 v[0:3], a[16:19], a[80:83], v[0:3]\n"
"ds_read_b128 a[96:99], v77 offset:32768\n"
"s_add_u32 m0, s37, 0x4000\n"
"v_mfma_f32_16x16x32_bf16 v[4:7], a[4:7], a[64:67], v[4:7]\n"
"ds_read_b128 a[100:103], v77 offset:34816\n"
"buffer_load_dwordx4 v78, s[20:23], s31 offen offset:0 sc1 lds\n"
"v_mfma_f32_16x16x32_bf16 v[4:7], a[20:23], a[80:83], v[4:7]\n"
"ds_read_b128 a[104:107], v77 offset:36864\n"
"v_mfma_f32_16x16x32_bf16 v[8:11], a[8:11], a[64:67], v[8:11]\n"
"ds_read_b128 a[108:111], v77 offset:38912\n"
"v_mfma_f32_16x16x32_bf16 v[8:11], a[24:27], a[80:83], v[8:11]\n"
"ds_read_b64_tr_b16 a[32:33], v70 offset:32768\n"
"buffer_load_dwordx4 v78, s[20:23], s31 offen offset:1024 sc1 lds\n"
"v_mfma_f32_16x16x32_bf16 v[12:15], a[12:15], a[64:67], v[12:15]\n"
"ds_read_b64_tr_b16 a[34:35], v70 offset:36864\n"
"v_mfma_f32_16x16x32_bf16 v[12:15], a[28:31], a[80:83], v[12:15]\n"
"ds_read_b64_tr_b16 a[36:37], v71 offset:32768\n"
"v_mfma_f32_16x16x32_bf16 v[16:19], a[0:3], a[68:71], v[16:19]\n"
"ds_read_b64_tr_b16 a[38:39], v71 offset:36864\n"
"buffer_load_dwordx4 v79, s[20:23], s31 offen offset:2048 sc1 lds\n"
"v_mfma_f32_16x16x32_bf16 v[16:19], a[16:19], a[84:87], v[16:19]\n"
"ds_read_b64_tr_b16 a[40:41], v70 offset:33280\n"
"v_mfma_f32_16x16x32_bf16 v[20:23], a[4:7], a[68:71], v[20:23]\n"
"ds_read_b64_tr_b16 a[42:43], v70 offset:37376\n"
"v_mfma_f32_16x16x32_bf16 v[20:23], a[20:23], a[84:87], v[20:23]\n"
"ds_read_b64_tr_b16 a[44:45], v71 offset:33280\n"
"buffer_load_dwordx4 v79, s[20:23], s31 offen offset:3072 sc1 lds\n"
"v_mfma_f32_16x16x32_bf16 v[24:27], a[8:11], a[68:71], v[24:27]\n"
"ds_read_b64_tr_b16 a[46:47], v71 offset:37376\n"
"s_add_u32 s31, s31, 0x800\n"
"v_mfma_f32_16x16x32_bf16 v[24:27], a[24:27], a[84:87], v[24:27]\n"
"ds_read_b128 a[112:115], v77 offset:33792\n"
"v_mfma_f32_16x16x32_bf16 v[28:31], a[12:15], a[68:71], v[28:31]\n"
"ds_read_b128 a[116:119], v77 offset:35840\n"
"s_add_u32 m0, s29, 0x10000\n"
"v_mfma_f32_16x16x32_bf16 v[28:31], a[28:31], a[84:87], v[28:31]\n"
"ds_read_b128 a[120:123], v77 offset:37888\n"
"v_mfma_f32_16x16x32_bf16 v[32:35], a[0:3], a[72:75], v[32:35]\n"
"ds_read_b128 a[124:127], v77 offset:39936\n"
"buffer_load_dwordx4 v64, s[24:27], s30 offen offset:0 lds\n"
"v_mfma_f32_16x16x32_bf16 v[32:35], a[16:19], a[88:91], v[32:35]\n"
"ds_read_b64_tr_b16 a[48:49], v70 offset:40960\n"
"v_mfma_f32_16x16x32_bf16 v[36:39], a[4:7], a[72:75], v[36:39]\n"
"ds_read_b64_tr_b16 a[50:51], v70 offset:45056\n"
"v_mfma_f32_16x16x32_bf16 v[36:39], a[20:23], a[88:91], v[36:39]\n"
"ds_read_b64_tr_b16 a[52:53], v71 offset:40960\n"
"v_mfma_f32_16x16x32_bf16 v[40:43], a[8:11], a[72:75], v[40:43]\n"
"ds_read_b64_tr_b16 a[54:55], v71 offset:45056\n"
"buffer_load_dwordx4 v65, s[24:27], s30 offen offset:1024 lds\n"
"v_mfma_f32_16x16x32_bf16 v[40:43], a[24:27], a[88:91], v[40:43]\n"
"ds_read_b64_tr_b16 a[56:57], v70 offset:41472\n"
"v_mfma_f32_16x16x32_bf16 v[44:47], a[12:15], a[72:75], v[44:47]\n"
"ds_read_b64_tr_b16 a[58:59], v70 offset:45568\n"
"v_mfma_f32_16x16x32_bf16 v[44:47], a[28:31], a[88:91], v[44:47]\n"
"ds_read_b64_tr_b16 a[60:61], v71 offset:41472\n"
"v_mfma_f32_16x16x32_bf16 v[48:51], a[0:3], a[76:79], v[48:51]\n"
"ds_read_b64_tr_b16 a[62:63], v71 offset:45568\n"
"buffer_load_dwordx4 v66, s[24:27], s30 offen offset:2048 lds\n"
"v_mfma_f32_16x16x32_bf16 v[48:51], a[16:19], a[92:95], v[48:51]\n"
"v_mfma_f32_16x16x32_bf16 v[52:55], a[4:7], a[76:79], v[52:55]\n"
"v_mfma_f32_16x16x32_bf16 v[52:55], a[20:23], a[92:95], v[52:55]\n"
"v_mfma_f32_16x16x32_bf16 v[56:59], a[8:11], a[76:79], v[56:59]\n"
"buffer_load_dwordx4 v67, s[24:27], s30 offen offset:3072 lds\n"
"v_mfma_f32_16x16x32_bf16 v[56:59], a[24:27], a[92:95], v[56:59]\n"
"v_mfma_f32_16x16x32_bf16 v[60:63], a[12:15], a[76:79], v[60:63]\n"
"s_add_u32 s30, s30, 0x10000\n"
"v_mfma_f32_16x16x32_bf16 v[60:63], a[28:31], a[92:95], v[60:63]\n"
"s_waitcnt lgkmcnt(0)\n"
"v_mfma_f32_16x16x32_bf16 v[0:3], a[32:35], a[96:99], v[0:3]\n"
"s_waitcnt vmcnt(12)\n"
"s_barrier\n"
"v_mfma_f32_16x16x32_bf16 v[0:3], a[48:51], a[112:115], v[0:3]\n"
"ds_read_b128 a[64:67], v77 offset:0\n"
"s_add_u32 m0, s37, 0x8000\n"
"v_mfma_f32_16x16x32_bf16 v[4:7], a[36:39], a[96:99], v[4:7]\n"
"ds_read_b128 a[68:71], v77 offset:2048\n"
"buffer_load_dwordx4 v78, s[20:23], s31 offen offset:0 sc1 lds\n"
"v_mfma_f32_16x16x32_bf16 v[4:7], a[52:55], a[112:115], v[4:7]\n"
"ds_read_b128 a[72:75], v77 offset:4096\n"
"v_mfma_f32_16x16x32_bf16 v[8:11], a[40:43], a[96:99], v[8:11]\n"
"ds_read_b128 a[76:79], v77 offset:6144\n"
"v_mfma_f32_16x16x32_bf16 v[8:11], a[56:59], a[112:115], v[8:11]\n"
"ds_read_b64_tr_b16 a[0:1], v76 offset:0\n"
"buffer_load_dwordx4 v78, s[20:23], s31 offen offset:1024 sc1 lds\n"
"v_mfma_f32_16x16x32_bf16 v[12:15], a[44:47], a[96:99], v[12:15]\n"
"ds_read_b64_tr_b16 a[2:3], v76 offset:4096\n"
"v_mfma_f32_16x16x32_bf16 v[12:15], a[60:63], a[112:115], v[12:15]\n"
"ds_read_b64_tr_b16 a[4:5], v88 offset:0\n"
"v_mfma_f32_16x16x32_bf16 v[16:19], a[32:35], a[100:103], v[16:19]\n"
"ds_read_b64_tr_b16 a[6:7], v88 offset:4096\n"
"buffer_load_dwordx4 v79, s[20:23], s31 offen offset:2048 sc1 lds\n"
"v_mfma_f32_16x16x32_bf16 v[16:19], a[48:51], a[116:119], v[16:19]\n"
"ds_read_b64_tr_b16 a[8:9], v76 offset:512\n"
"v_mfma_f32_16x16x32_bf16 v[20:23], a[36:39], a[100:103], v[20:23]\n"
"ds_read_b64_tr_b16 a[10:11], v76 offset:4608\n"
"v_mfma_f32_16x16x32_bf16 v[20:23], a[52:55], a[116:119], v[20:23]\n"
"ds_read_b64_tr_b16 a[12:13], v88 offset:512\n"
"buffer_load_dwordx4 v79, s[20:23], s31 offen offset:3072 sc1 lds\n"
"v_mfma_f32_16x16x32_bf16 v[24:27], a[40:43], a[100:103], v[24:27]\n"
"ds_read_b64_tr_b16 a[14:15], v88 offset:4608\n"
"s_add_u32 s31, s31, 0x800\n"
"v_mfma_f32_16x16x32_bf16 v[24:27], a[56:59], a[116:119], v[24:27]\n"
"ds_read_b128 a[80:83], v77 offset:1024\n"
"v_mfma_f32_16x16x32_bf16 v[28:31], a[44:47], a[100:103], v[28:31]\n"
"ds_read_b128 a[84:87], v77 offset:3072\n"
"s_add_u32 m0, s29, 0x14000\n"
"v_mfma_f32_16x16x32_bf16 v[28:31], a[60:63], a[116:119], v[28:31]\n"
"ds_read_b128 a[88:91], v77 offset:5120\n"
"v_mfma_f32_16x16x32_bf16 v[32:35], a[32:35], a[104:107], v[32:35]\n"
"ds_read_b128 a[92:95], v77 offset:7168\n"
"buffer_load_dwordx4 v64, s[24:27], s30 offen offset:0 lds\n"
"v_mfma_f32_16x16x32_bf16 v[32:35], a[48:51], a[120:123], v[32:35]\n"
"ds_read_b64_tr_b16 a[16:17], v76 offset:8192\n"
"v_mfma_f32_16x16x32_bf16 v[36:39], a[36:39], a[104:107], v[36:39]\n"
"ds_read_b64_tr_b16 a[18:19], v76 offset:12288\n"
"v_mfma_f32_16x16x32_bf16 v[36:39], a[52:55], a[120:123], v[36:39]\n"
"ds_read_b64_tr_b16 a[20:21], v88 offset:8192\n"
"v_mfma_f32_16x16x32_bf16 v[40:43], a[40:43], a[104:107], v[40:43]\n"
"ds_read_b64_tr_b16 a[22:23], v88 offset:12288\n"
"buffer_load_dwordx4 v65, s[24:27], s30 offen offset:1024 lds\n"
"v_mfma_f32_16x16x32_bf16 v[40:43], a[56:59], a[120:123], v[40:43]\n"
"ds_read_b64_tr_b16 a[24:25], v76 offset:8704\n"
"v_mfma_f32_16x16x32_bf16 v[44:47], a[44:47], a[104:107], v[44:47]\n"
"ds_read_b64_tr_b16 a[26:27], v76 offset:12800\n"
"v_mfma_f32_16x16x32_bf16 v[44:47], a[60:63], a[120:123], v[44:47]\n"
"ds_read_b64_tr_b16 a[28:29], v88 offset:8704\n"
"v_mfma_f32_16x16x32_bf16 v[48:51], a[32:35], a[108:111], v[48:51]\n"
"ds_read_b64_tr_b16 a[30:31], v88 offset:12800\n"
"buffer_load_dwordx4 v66, s[24:27], s30 offen offset:2048 lds\n"
"v_mfma_f32_16x16x32_bf16 v[48:51], a[48:51], a[124:127], v[48:51]\n"
"v_mfma_f32_16x16x32_bf16 v[52:55], a[36:39], a[108:111], v[52:55]\n"
"v_mfma_f32_16x16x32_bf16 v[52:55], a[52:55], a[124:127], v[52:55]\n"
"v_mfma_f32_16x16x32_bf16 v[56:59], a[40:43], a[108:111], v[56:59]\n"
"buffer_load_dwordx4 v67, s[24:27], s30 offen offset:3072 lds\n"
"v_mfma_f32_16x16x32_bf16 v[56:59], a[56:59], a[124:127], v[56:59]\n"
"v_mfma_f32_16x16x32_bf16 v[60:63], a[44:47], a[108:111], v[60:63]\n"
"s_add_u32 s30, s30, 0x10000\n"
"v_mfma_f32_16x16x32_bf16 v[60:63], a[60:63], a[124:127], v[60:63]\n"
"s_waitcnt lgkmcnt(0)\n"
"v_mfma_f32_16x16x32_bf16 v[0:3], a[0:3], a[64:67], v[0:3]\n"
"s_waitcnt vmcnt(12)\n"
"s_barrier\n"
"v_mfma_f32_16x16x32_bf16 v[0:3], a[16:19], a[80:83], v[0:3]\n"
"ds_read_b128 a[96:99], v77 offset:16384\n"
"s_add_u32 m0, s37, 0\n"
"v_mfma_f32_16x16x32_bf16 v[4:7], a[4:7], a[64:67], v[4:7]\n"
"ds_read_b128 a[100:103], v77 offset:18432\n"
"buffer_load_dwordx4 v78, s[20:23], s31 offen offset:0 sc1 lds\n"
"v_mfma_f32_16x16x32_bf16 v[4:7], a[20:23], a[80:83], v[4:7]\n"
"ds_read_b128 a[104:107], v77 offset:20480\n"
"v_mfma_f32_16x16x32_bf16 v[8:11], a[8:11], a[64:67], v[8:11]\n"
"ds_read_b128 a[108:111], v77 offset:22528\n"
"v_mfma_f32_16x16x32_bf16 v[8:11], a[24:27], a[80:83], v[8:11]\n"
"ds_read_b64_tr_b16 a[32:33], v68 offset:0\n"
"buffer_load_dwordx4 v78, s[20:23], s31 offen offset:1024 sc1 lds\n"
"v_mfma_f32_16x16x32_bf16 v[12:15], a[12:15], a[64:67], v[12:15]\n"
"ds_read_b64_tr_b16 a[34:35], v68 offset:4096\n"
"v_mfma_f32_16x16x32_bf16 v[12:15], a[28:31], a[80:83], v[12:15]\n"
"ds_read_b64_tr_b16 a[36:37], v69 offset:0\n"
"v_mfma_f32_16x16x32_bf16 v[16:19], a[0:3], a[68:71], v[16:19]\n"
"ds_read_b64_tr_b16 a[38:39], v69 offset:4096\n"
"buffer_load_dwordx4 v79, s[20:23], s31 offen offset:2048 sc1 lds\n"
"v_mfma_f32_16x16x32_bf16 v[16:19], a[16:19], a[84:87], v[16:19]\n"
"ds_read_b64_tr_b16 a[40:41], v68 offset:512\n"
"v_mfma_f32_16x16x32_bf16 v[20:23], a[4:7], a[68:71], v[20:23]\n"
"ds_read_b64_tr_b16 a[42:43], v68 offset:4608\n"
"v_mfma_f32_16x16x32_bf16 v[20:23], a[20:23], a[84:87], v[20:23]\n"
"ds_read_b64_tr_b16 a[44:45], v69 offset:512\n"
"buffer_load_dwordx4 v79, s[20:23], s31 offen offset:3072 sc1 lds\n"
"v_mfma_f32_16x16x32_bf16 v[24:27], a[8:11], a[68:71], v[24:27]\n"
"ds_read_b64_tr_b16 a[46:47], v69 offset:4608\n"
"s_add_u32 s31, s31, 0x800\n"
"v_mfma_f32_16x16x32_bf16 v[24:27], a[24:27], a[84:87], v[24:27]\n"
"ds_read_b128 a[112:115], v77 offset:17408\n"
"v_mfma_f32_16x16x32_bf16 v[28:31], a[12:15], a[68:71], v[28:31]\n"
"ds_read_b128 a[116:119], v77 offset:19456\n"
"s_add_u32 m0, s29, 0x18000\n"
"v_mfma_f32_16x16x32_bf16 v[28:31], a[28:31], a[84:87], v[28:31]\n"
"ds_read_b128 a[120:123], v77 offset:21504\n"
"v_mfma_f32_16x16x32_bf16 v[32:35], a[0:3], a[72:75], v[32:35]\n"
"ds_read_b128 a[124:127], v77 offset:23552\n"
"buffer_load_dwordx4 v64, s[24:27], s30 offen offset:0 lds\n"
"v_mfma_f32_16x16x32_bf16 v[32:35], a[16:19], a[88:91], v[32:35]\n"
"ds_read_b64_tr_b16 a[48:49], v68 offset:8192\n"
"v_mfma_f32_16x16x32_bf16 v[36:39], a[4:7], a[72:75], v[36:39]\n"
"ds_read_b64_tr_b16 a[50:51], v68 offset:12288\n"
"v_mfma_f32_16x16x32_bf16 v[36:39], a[20:23], a[88:91], v[36:39]\n"
"ds_read_b64_tr_b16 a[52:53], v69 offset:8192\n"
"v_mfma_f32_16x16x32_bf16 v[40:43], a[8:11], a[72:75], v[40:43]\n"
"ds_read_b64_tr_b16 a[54:55], v69 offset:12288\n"
"buffer_load_dwordx4 v65, s[24:27], s30 offen offset:1024 lds\n"
"v_mfma_f32_16x16x32_bf16 v[40:43], a[24:27], a[88:91], v[40:43]\n"
"ds_read_b64_tr_b16 a[56:57], v68 offset:8704\n"
"v_mfma_f32_16x16x32_bf16 v[44:47], a[12:15], a[72:75], v[44:47]\n"
"ds_read_b64_tr_b16 a[58:59], v68 offset:12800\n"
"v_mfma_f32_16x16x32_bf16 v[44:47], a[28:31], a[88:91], v[44:47]\n"
"ds_read_b64_tr_b16 a[60:61], v69 offset:8704\n"
"v_mfma_f32_16x16x32_bf16 v[48:51], a[0:3], a[76:79], v[48:51]\n"
"ds_read_b64_tr_b16 a[62:63], v69 offset:12800\n"
"buffer_load_dwordx4 v66, s[24:27], s30 offen offset:2048 lds\n"
"v_mfma_f32_16x16x32_bf16 v[48:51], a[16:19], a[92:95], v[48:51]\n"
"v_mfma_f32_16x16x32_bf16 v[52:55], a[4:7], a[76:79], v[52:55]\n"
"v_mfma_f32_16x16x32_bf16 v[52:55], a[20:23], a[92:95], v[52:55]\n"
"v_mfma_f32_16x16x32_bf16 v[56:59], a[8:11], a[76:79], v[56:59]\n"
"buffer_load_dwordx4 v67, s[24:27], s30 offen offset:3072 lds\n"
"v_mfma_f32_16x16x32_bf16 v[56:59], a[24:27], a[92:95], v[56:59]\n"
"v_mfma_f32_16x16x32_bf16 v[60:63], a[12:15], a[76:79], v[60:63]\n"
"s_add_u32 s30, s30, 0x10000\n"
"v_mfma_f32_16x16x32_bf16 v[60:63], a[28:31], a[92:95], v[60:63]\n"
"s_waitcnt lgkmcnt(0)\n"
"v_mfma_f32_16x16x32_bf16 v[0:3], a[32:35], a[96:99], v[0:3]\n"
"s_waitcnt vmcnt(12)\n"
"s_barrier\n"
"v_mfma_f32_16x16x32_bf16 v[0:3], a[48:51], a[112:115], v[0:3]\n"
"ds_read_b128 a[64:67], v77 offset:32768\n"
"s_add_u32 m0, s37, 0x4000\n"
"v_mfma_f32_16x16x32_bf16 v[4:7], a[36:39], a[96:99], v[4:7]\n"
"ds_read_b128 a[68:71], v77 offset:34816\n"
"buffer_load_dwordx4 v78, s[20:23], s31 offen offset:0 sc1 lds\n"
"v_mfma_f32_16x16x32_bf16 v[4:7], a[52:55], a[112:115], v[4:7]\n"
"ds_read_b128 a[72:75], v77 offset:36864\n"
"v_mfma_f32_16x16x32_bf16 v[8:11], a[40:43], a[96:99], v[8:11]\n"
"ds_read_b128 a[76:79], v77 offset:38912\n"
"v_mfma_f32_16x16x32_bf16 v[8:11], a[56:59], a[112:115], v[8:11]\n"
"ds_read_b64_tr_b16 a[0:1], v68 offset:16384\n"
"buffer_load_dwordx4 v78, s[20:23], s31 offen offset:1024 sc1 lds\n"
"v_mfma_f32_16x16x32_bf16 v[12:15], a[44:47], a[96:99], v[12:15]\n"
"ds_read_b64_tr_b16 a[2:3], v68 offset:20480\n"
"v_mfma_f32_16x16x32_bf16 v[12:15], a[60:63], a[112:115], v[12:15]\n"
"ds_read_b64_tr_b16 a[4:5], v69 offset:16384\n"
"v_mfma_f32_16x16x32_bf16 v[16:19], a[32:35], a[100:103], v[16:19]\n"
"ds_read_b64_tr_b16 a[6:7], v69 offset:20480\n"
"buffer_load_dwordx4 v79, s[20:23], s31 offen offset:2048 sc1 lds\n"
"v_mfma_f32_16x16x32_bf16 v[16:19], a[48:51], a[116:119], v[16:19]\n"
"ds_read_b64_tr_b16 a[8:9], v68 offset:16896\n"
"v_mfma_f32_16x16x32_bf16 v[20:23], a[36:39], a[100:103], v[20:23]\n"
"ds_read_b64_tr_b16 a[10:11], v68 offset:20992\n"
"v_mfma_f32_16x16x32_bf16 v[20:23], a[52:55], a[116:119], v[20:23]\n"
"ds_read_b64_tr_b16 a[12:13], v69 offset:16896\n"
"buffer_load_dwordx4 v79, s[20:23], s31 offen offset:3072 sc1 lds\n"
"v_mfma_f32_16x16x32_bf16 v[24:27], a[40:43], a[100:103], v[24:27]\n"
"ds_read_b64_tr_b16 a[14:15], v69 offset:20992\n"
"s_add_u32 s31, s31, 0x800\n"
"v_mfma_f32_16x16x32_bf16 v[24:27], a[56:59], a[116:119], v[24:27]\n"
"ds_read_b128 a[80:83], v77 offset:33792\n"
"v_mfma_f32_16x16x32_bf16 v[28:31], a[44:47], a[100:103], v[28:31]\n"
"ds_read_b128 a[84:87], v77 offset:35840\n"
"s_add_u32 m0, s29, 0\n"
"v_mfma_f32_16x16x32_bf16 v[28:31], a[60:63], a[116:119], v[28:31]\n"
"ds_read_b128 a[88:91], v77 offset:37888\n"
"v_mfma_f32_16x16x32_bf16 v[32:35], a[32:35], a[104:107], v[32:35]\n"
"ds_read_b128 a[92:95], v77 offset:39936\n"
"buffer_load_dwordx4 v64, s[24:27], s30 offen offset:0 lds\n"
"v_mfma_f32_16x16x32_bf16 v[32:35], a[48:51], a[120:123], v[32:35]\n"
"ds_read_b64_tr_b16 a[16:17], v68 offset:24576\n"
"v_mfma_f32_16x16x32_bf16 v[36:39], a[36:39], a[104:107], v[36:39]\n"
"ds_read_b64_tr_b16 a[18:19], v68 offset:28672\n"
"v_mfma_f32_16x16x32_bf16 v[36:39], a[52:55], a[120:123], v[36:39]\n"
"ds_read_b64_tr_b16 a[20:21], v69 offset:24576\n"
"v_mfma_f32_16x16x32_bf16 v[40:43], a[40:43], a[104:107], v[40:43]\n"
"ds_read_b64_tr_b16 a[22:23], v69 offset:28672\n"
"buffer_load_dwordx4 v65, s[24:27], s30 offen offset:1024 lds\n"
"v_mfma_f32_16x16x32_bf16 v[40:43], a[56:59], a[120:123], v[40:43]\n"
"ds_read_b64_tr_b16 a[24:25], v68 offset:25088\n"
"v_mfma_f32_16x16x32_bf16 v[44:47], a[44:47], a[104:107], v[44:47]\n"
"ds_read_b64_tr_b16 a[26:27], v68 offset:29184\n"
"v_mfma_f32_16x16x32_bf16 v[44:47], a[60:63], a[120:123], v[44:47]\n"
"ds_read_b64_tr_b16 a[28:29], v69 offset:25088\n"
"v_mfma_f32_16x16x32_bf16 v[48:51], a[32:35], a[108:111], v[48:51]\n"
"ds_read_b64_tr_b16 a[30:31], v69 offset:29184\n"
"buffer_load_dwordx4 v66, s[24:27], s30 offen offset:2048 lds\n"
"v_mfma_f32_16x16x32_bf16 v[48:51], a[48:51], a[124:127], v[48:51]\n"
"v_mfma_f32_16x16x32_bf16 v[52:55], a[36:39], a[108:111], v[52:55]\n"
"v_mfma_f32_16x16x32_bf16 v[52:55], a[52:55], a[124:127], v[52:55]\n"
"v_mfma_f32_16x16x32_bf16 v[56:59], a[40:43], a[108:111], v[56:59]\n"
"buffer_load_dwordx4 v67, s[24:27], s30 offen offset:3072 lds\n"
"v_mfma_f32_16x16x32_bf16 v[56:59], a[56:59], a[124:127], v[56:59]\n"
"v_mfma_f32_16x16x32_bf16 v[60:63], a[44:47], a[108:111], v[60:63]\n"
"s_add_u32 s30, s30, 0x10000\n"
"v_mfma_f32_16x16x32_bf16 v[60:63], a[60:63], a[124:127], v[60:63]\n"
"s_waitcnt lgkmcnt(0)\n"
"v_mfma_f32_16x16x32_bf16 v[0:3], a[0:3], a[64:67], v[0:3]\n"
"s_waitcnt vmcnt(12)\n"
"s_barrier\n"
"v_mfma_f32_16x16x32_bf16 v[0:3], a[16:19], a[80:83], v[0:3]\n"
"ds_read_b128 a[96:99], v77 offset:0\n"
"s_add_u32 m0, s37, 0x8000\n"
"v_mfma_f32_16x16x32_bf16 v[4:7], a[4:7], a[64:67], v[4:7]\n"
"ds_read_b128 a[100:103], v77 offset:2048\n"
"buffer_load_dwordx4 v78, s[20:23], s31 offen offset:0 sc1 lds\n"
"v_mfma_f32_16x16x32_bf16 v[4:7], a[20:23], a[80:83], v[4:7]\n"
"ds_read_b128 a[104:107], v77 offset:4096\n"
"v_mfma_f32_16x16x32_bf16 v[8:11], a[8:11], a[64:67], v[8:11]\n"
"ds_read_b128 a[108:111], v77 offset:6144\n"
"v_mfma_f32_16x16x32_bf16 v[8:11], a[24:27], a[80:83], v[8:11]\n"
"ds_read_b64_tr_b16 a[32:33], v68 offset:32768\n"
"buffer_load_dwordx4 v78, s[20:23], s31 offen offset:1024 sc1 lds\n"
"v_mfma_f32_16x16x32_bf16 v[12:15], a[12:15], a[64:67], v[12:15]\n"
"ds_read_b64_tr_b16 a[34:35], v68 offset:36864\n"
"v_mfma_f32_16x16x32_bf16 v[12:15], a[28:31], a[80:83], v[12:15]\n"
"ds_read_b64_tr_b16 a[36:37], v69 offset:32768\n"
"v_mfma_f32_16x16x32_bf16 v[16:19], a[0:3], a[68:71], v[16:19]\n"
"ds_read_b64_tr_b16 a[38:39], v69 offset:36864\n"
"buffer_load_dwordx4 v79, s[20:23], s31 offen offset:2048 sc1 lds\n"
"v_mfma_f32_16x16x32_bf16 v[16:19], a[16:19], a[84:87], v[16:19]\n"
"ds_read_b64_tr_b16 a[40:41], v68 offset:33280\n"
"v_mfma_f32_16x16x32_bf16 v[20:23], a[4:7], a[68:71], v[20:23]\n"
"ds_read_b64_tr_b16 a[42:43], v68 offset:37376\n"
"v_mfma_f32_16x16x32_bf16 v[20:23], a[20:23], a[84:87], v[20:23]\n"
"ds_read_b64_tr_b16 a[44:45], v69 offset:33280\n"
"buffer_load_dwordx4 v79, s[20:23], s31 offen offset:3072 sc1 lds\n"
"v_mfma_f32_16x16x32_bf16 v[24:27], a[8:11], a[68:71], v[24:27]\n"
"ds_read_b64_tr_b16 a[46:47], v69 offset:37376\n"
"s_add_u32 s31, s31, 0x800\n"
"v_mfma_f32_16x16x32_bf16 v[24:27], a[24:27], a[84:87], v[24:27]\n"
"ds_read_b128 a[112:115], v77 offset:1024\n"
"v_mfma_f32_16x16x32_bf16 v[28:31], a[12:15], a[68:71], v[28:31]\n"
"ds_read_b128 a[116:119], v77 offset:3072\n"
"s_add_u32 m0, s29, 0x4000\n"
"v_mfma_f32_16x16x32_bf16 v[28:31], a[28:31], a[84:87], v[28:31]\n"
"ds_read_b128 a[120:123], v77 offset:5120\n"
"v_mfma_f32_16x16x32_bf16 v[32:35], a[0:3], a[72:75], v[32:35]\n"
"ds_read_b128 a[124:127], v77 offset:7168\n"
"buffer_load_dwordx4 v64, s[24:27], s30 offen offset:0 lds\n"
"v_mfma_f32_16x16x32_bf16 v[32:35], a[16:19], a[88:91], v[32:35]\n"
"ds_read_b64_tr_b16 a[48:49], v68 offset:40960\n"
"v_mfma_f32_16x16x32_bf16 v[36:39], a[4:7], a[72:75], v[36:39]\n"
"ds_read_b64_tr_b16 a[50:51], v68 offset:45056\n"
"v_mfma_f32_16x16x32_bf16 v[36:39], a[20:23], a[88:91], v[36:39]\n"
"ds_read_b64_tr_b16 a[52:53], v69 offset:40960\n"
"v_mfma_f32_16x16x32_bf16 v[40:43], a[8:11], a[72:75], v[40:43]\n"
"ds_read_b64_tr_b16 a[54:55], v69 offset:45056\n"
"buffer_load_dwordx4 v65, s[24:27], s30 offen offset:1024 lds\n"
"v_mfma_f32_16x16x32_bf16 v[40:43], a[24:27], a[88:91], v[40:43]\n"
"ds_read_b64_tr_b16 a[56:57], v68 offset:41472\n"
"v_mfma_f32_16x16x32_bf16 v[44:47], a[12:15], a[72:75], v[44:47]\n"
"ds_read_b64_tr_b16 a[58:59], v68 offset:45568\n"
"v_mfma_f32_16x16x32_bf16 v[44:47], a[28:31], a[88:91], v[44:47]\n"
"ds_read_b64_tr_b16 a[60:61], v69 offset:41472\n"
"v_mfma_f32_16x16x32_bf16 v[48:51], a[0:3], a[76:79], v[48:51]\n"
"ds_read_b64_tr_b16 a[62:63], v69 offset:45568\n"
"buffer_load_dwordx4 v66, s[24:27], s30 offen offset:2048 lds\n"
"v_mfma_f32_16x16x32_bf16 v[48:51], a[16:19], a[92:95], v[48:51]\n"
"v_mfma_f32_16x16x32_bf16 v[52:55], a[4:7], a[76:79], v[52:55]\n"
"v_mfma_f32_16x16x32_bf16 v[52:55], a[20:23], a[92:95], v[52:55]\n"
"v_mfma_f32_16x16x32_bf16 v[56:59], a[8:11], a[76:79], v[56:59]\n"
"buffer_load_dwordx4 v67, s[24:27], s30 offen offset:3072 lds\n"
"v_mfma_f32_16x16x32_bf16 v[56:59], a[24:27], a[92:95], v[56:59]\n"
"v_mfma_f32_16x16x32_bf16 v[60:63], a[12:15], a[76:79], v[60:63]\n"
"s_add_u32 s30, s30, 0x10000\n"
"v_mfma_f32_16x16x32_bf16 v[60:63], a[28:31], a[92:95], v[60:63]\n"
"s_waitcnt lgkmcnt(0)\n"
"v_mfma_f32_16x16x32_bf16 v[0:3], a[32:35], a[96:99], v[0:3]\n"
"s_waitcnt vmcnt(12)\n"
"s_barrier\n"
"v_mfma_f32_16x16x32_bf16 v[0:3], a[48:51], a[112:115], v[0:3]\n"
"ds_read_b128 a[64:67], v77 offset:16384\n"
"s_add_u32 m0, s37, 0\n"
"v_mfma_f32_16x16x32_bf16 v[4:7], a[36:39], a[96:99], v[4:7]\n"
"ds_read_b128 a[68:71], v77 offset:18432\n"
"buffer_load_dwordx4 v78, s[20:23], s31 offen offset:0 sc1 lds\n"
"v_mfma_f32_16x16x32_bf16 v[4:7], a[52:55], a[112:115], v[4:7]\n"
"ds_read_b128 a[72:75], v77 offset:20480\n"
"v_mfma_f32_16x16x32_bf16 v[8:11], a[40:43], a[96:99], v[8:11]\n"
"ds_read_b128 a[76:79], v77 offset:22528\n"
"v_mfma_f32_16x16x32_bf16 v[8:11], a[56:59], a[112:115], v[8:11]\n"
"ds_read_b64_tr_b16 a[0:1], v70 offset:0\n"
"buffer_load_dwordx4 v78, s[20:23], s31 offen offset:1024 sc1 lds\n"
"v_mfma_f32_16x16x32_bf16 v[12:15], a[44:47], a[96:99], v[12:15]\n"
"ds_read_b64_tr_b16 a[2:3], v70 offset:4096\n"
"v_mfma_f32_16x16x32_bf16 v[12:15], a[60:63], a[112:115], v[12:15]\n"
"ds_read_b64_tr_b16 a[4:5], v71 offset:0\n"
"v_mfma_f32_16x16x32_bf16 v[16:19], a[32:35], a[100:103], v[16:19]\n"
"ds_read_b64_tr_b16 a[6:7], v71 offset:4096\n"
"buffer_load_dwordx4 v79, s[20:23], s31 offen offset:2048 sc1 lds\n"
"v_mfma_f32_16x16x32_bf16 v[16:19], a[48:51], a[116:119], v[16:19]\n"
"ds_read_b64_tr_b16 a[8:9], v70 offset:512\n"
"v_mfma_f32_16x16x32_bf16 v[20:23], a[36:39], a[100:103], v[20:23]\n"
"ds_read_b64_tr_b16 a[10:11], v70 offset:4608\n"
"v_mfma_f32_16x16x32_bf16 v[20:23], a[52:55], a[116:119], v[20:23]\n"
"ds_read_b64_tr_b16 a[12:13], v71 offset:512\n"
"buffer_load_dwordx4 v79, s[20:23], s31 offen offset:3072 sc1 lds\n"
"v_mfma_f32_16x16x32_bf16 v[24:27], a[40:43], a[100:103], v[24:27]\n"
"ds_read_b64_tr_b16 a[14:15], v71 offset:4608\n"
"s_add_u32 s31, s31, 0x800\n"
"v_mfma_f32_16x16x32_bf16 v[24:27], a[56:59], a[116:119], v[24:27]\n"
"ds_read_b128 a[80:83], v77 offset:17408\n"
"v_mfma_f32_16x16x32_bf16 v[28:31], a[44:47], a[100:103], v[28:31]\n"
"ds_read_b128 a[84:87], v77 offset:19456\n"
"v_mfma_f32_16x16x32_bf16 v[28:31], a[60:63], a[116:119], v[28:31]\n"
"ds_read_b128 a[88:91], v77 offset:21504\n"
"v_mfma_f32_16x16x32_bf16 v[32:35], a[32:35], a[104:107], v[32:35]\n"
"ds_read_b128 a[92:95], v77 offset:23552\n"
"v_mfma_f32_16x16x32_bf16 v[32:35], a[48:51], a[120:123], v[32:35]\n"
"ds_read_b64_tr_b16 a[16:17], v70 offset:8192\n"
"v_mfma_f32_16x16x32_bf16 v[36:39], a[36:39], a[104:107], v[36:39]\n"
"ds_read_b64_tr_b16 a[18:19], v70 offset:12288\n"
"v_mfma_f32_16x16x32_bf16 v[36:39], a[52:55], a[120:123], v[36:39]\n"
"ds_read_b64_tr_b16 a[20:21], v71 offset:8192\n"
"v_mfma_f32_16x16x32_bf16 v[40:43], a[40:43], a[104:107], v[40:43]\n"
"ds_read_b64_tr_b16 a[22:23], v71 offset:12288\n"
"v_mfma_f32_16x16x32_bf16 v[40:43], a[56:59], a[120:123], v[40:43]\n"
"ds_read_b64_tr_b16 a[24:25], v70 offset:8704\n"
"v_mfma_f32_16x16x32_bf16 v[44:47], a[44:47], a[104:107], v[44:47]\n"
"ds_read_b64_tr_b16 a[26:27], v70 offset:12800\n"
"v_mfma_f32_16x16x32_bf16 v[44:47], a[60:63], a[120:123], v[44:47]\n"
"ds_read_b64_tr_b16 a[28:29], v71 offset:8704\n"
"v_mfma_f32_16x16x32_bf16 v[48:51], a[32:35], a[108:111], v[48:51]\n"
"ds_read_b64_tr_b16 a[30:31], v71 offset:12800\n"
"v_mfma_f32_16x16x32_bf16 v[48:51], a[48:51], a[124:127], v[48:51]\n"
"v_mfma_f32_16x16x32_bf16 v[52:55], a[36:39], a[108:111], v[52:55]\n"
"v_mfma_f32_16x16x32_bf16 v[52:55], a[52:55], a[124:127], v[52:55]\n"
"v_mfma_f32_16x16x32_bf16 v[56:59], a[40:43], a[108:111], v[56:59]\n"
"v_mfma_f32_16x16x32_bf16 v[56:59], a[56:59], a[124:127], v[56:59]\n"
"v_mfma_f32_16x16x32_bf16 v[60:63], a[44:47], a[108:111], v[60:63]\n"
"v_mfma_f32_16x16x32_bf16 v[60:63], a[60:63], a[124:127], v[60:63]\n"
"s_waitcnt lgkmcnt(0)\n"
"v_mfma_f32_16x16x32_bf16 v[0:3], a[0:3], a[64:67], v[0:3]\n"
"s_waitcnt vmcnt(8)\n"
"s_barrier\n"
"v_mfma_f32_16x16x32_bf16 v[0:3], a[16:19], a[80:83], v[0:3]\n"
"ds_read_b128 a[96:99], v77 offset:32768\n"
"s_add_u32 m0, s37, 0x4000\n"
"v_mfma_f32_16x16x32_bf16 v[4:7], a[4:7], a[64:67], v[4:7]\n"
"ds_read_b128 a[100:103], v77 offset:34816\n"
"buffer_load_dwordx4 v78, s[20:23], s31 offen offset:0 sc1 lds\n"
"v_mfma_f32_16x16x32_bf16 v[4:7], a[20:23], a[80:83], v[4:7]\n"
"ds_read_b128 a[104:107], v77 offset:36864\n"
"v_mfma_f32_16x16x32_bf16 v[8:11], a[8:11], a[64:67], v[8:11]\n"
"ds_read_b128 a[108:111], v77 offset:38912\n"
"v_mfma_f32_16x16x32_bf16 v[8:11], a[24:27], a[80:83], v[8:11]\n"
"ds_read_b64_tr_b16 a[32:33], v70 offset:16384\n"
"buffer_load_dwordx4 v78, s[20:23], s31 offen offset:1024 sc1 lds\n"
"v_mfma_f32_16x16x32_bf16 v[12:15], a[12:15], a[64:67], v[12:15]\n"
"ds_read_b64_tr_b16 a[34:35], v70 offset:20480\n"
"v_mfma_f32_16x16x32_bf16 v[12:15], a[28:31], a[80:83], v[12:15]\n"
"ds_read_b64_tr_b16 a[36:37], v71 offset:16384\n"
"v_mfma_f32_16x16x32_bf16 v[16:19], a[0:3], a[68:71], v[16:19]\n"
"ds_read_b64_tr_b16 a[38:39], v71 offset:20480\n"
"buffer_load_dwordx4 v79, s[20:23], s31 offen offset:2048 sc1 lds\n"
"v_mfma_f32_16x16x32_bf16 v[16:19], a[16:19], a[84:87], v[16:19]\n"
"ds_read_b64_tr_b16 a[40:41], v70 offset:16896\n"
"v_mfma_f32_16x16x32_bf16 v[20:23], a[4:7], a[68:71], v[20:23]\n"
"ds_read_b64_tr_b16 a[42:43], v70 offset:20992\n"
"v_mfma_f32_16x16x32_bf16 v[20:23], a[20:23], a[84:87], v[20:23]\n"
"ds_read_b64_tr_b16 a[44:45], v71 offset:16896\n"
"buffer_load_dwordx4 v79, s[20:23], s31 offen offset:3072 sc1 lds\n"
"v_mfma_f32_16x16x32_bf16 v[24:27], a[8:11], a[68:71], v[24:27]\n"
"ds_read_b64_tr_b16 a[46:47], v71 offset:20992\n"
"s_add_u32 s31, s31, 0x800\n"
"v_mfma_f32_16x16x32_bf16 v[24:27], a[24:27], a[84:87], v[24:27]\n"
"ds_read_b128 a[112:115], v77 offset:33792\n"
"s_add_u32 m0, s44, 0x8000\n"
"v_mfma_f32_16x16x32_bf16 v[28:31], a[12:15], a[68:71], v[28:31]\n"
"ds_read_b128 a[116:119], v77 offset:35840\n"
"buffer_load_dwordx4 v96, s[40:43], s45 offen offset:0 lds\n"
"v_mfma_f32_16x16x32_bf16 v[28:31], a[28:31], a[84:87], v[28:31]\n"
"ds_read_b128 a[120:123], v77 offset:37888\n"
"v_mfma_f32_16x16x32_bf16 v[32:35], a[0:3], a[72:75], v[32:35]\n"
"ds_read_b128 a[124:127], v77 offset:39936\n"
"buffer_load_dwordx4 v97, s[40:43], s45 offen offset:1024 lds\n"
"v_mfma_f32_16x16x32_bf16 v[32:35], a[16:19], a[88:91], v[32:35]\n"
"ds_read_b64_tr_b16 a[48:49], v70 offset:24576\n"
"v_mfma_f32_16x16x32_bf16 v[36:39], a[4:7], a[72:75], v[36:39]\n"
"ds_read_b64_tr_b16 a[50:51], v70 offset:28672\n"
"buffer_load_dwordx4 v98, s[40:43], s45 offen offset:2048 lds\n"
"v_mfma_f32_16x16x32_bf16 v[36:39], a[20:23], a[88:91], v[36:39]\n"
"ds_read_b64_tr_b16 a[52:53], v71 offset:24576\n"
"v_mfma_f32_16x16x32_bf16 v[40:43], a[8:11], a[72:75], v[40:43]\n"
"ds_read_b64_tr_b16 a[54:55], v71 offset:28672\n"
"buffer_load_dwordx4 v99, s[40:43], s45 offen offset:3072 lds\n"
"v_mfma_f32_16x16x32_bf16 v[40:43], a[24:27], a[88:91], v[40:43]\n"
"ds_read_b64_tr_b16 a[56:57], v70 offset:25088\n"
"v_mfma_f32_16x16x32_bf16 v[44:47], a[12:15], a[72:75], v[44:47]\n"
"ds_read_b64_tr_b16 a[58:59], v70 offset:29184\n"
"s_add_u32 m0, s44, 0x9000\n"
"v_mfma_f32_16x16x32_bf16 v[44:47], a[28:31], a[88:91], v[44:47]\n"
"ds_read_b64_tr_b16 a[60:61], v71 offset:25088\n"
"v_mfma_f32_16x16x32_bf16 v[48:51], a[0:3], a[76:79], v[48:51]\n"
"ds_read_b64_tr_b16 a[62:63], v71 offset:29184\n"
"buffer_load_dwordx4 v100, s[40:43], s45 offen offset:0 lds\n"
"v_mfma_f32_16x16x32_bf16 v[48:51], a[16:19], a[92:95], v[48:51]\n"
"v_mfma_f32_16x16x32_bf16 v[52:55], a[4:7], a[76:79], v[52:55]\n"
"buffer_load_dwordx4 v101, s[40:43], s45 offen offset:1024 lds\n"
"v_mfma_f32_16x16x32_bf16 v[52:55], a[20:23], a[92:95], v[52:55]\n"
"v_mfma_f32_16x16x32_bf16 v[56:59], a[8:11], a[76:79], v[56:59]\n"
"buffer_load_dwordx4 v102, s[40:43], s45 offen offset:2048 lds\n"
"v_mfma_f32_16x16x32_bf16 v[56:59], a[24:27], a[92:95], v[56:59]\n"
"v_mfma_f32_16x16x32_bf16 v[60:63], a[12:15], a[76:79], v[60:63]\n"
"s_add_u32 s45, s45, 0x38000\n"
"v_mfma_f32_16x16x32_bf16 v[60:63], a[28:31], a[92:95], v[60:63]\n"
"s_waitcnt lgkmcnt(0)\n"
"v_mfma_f32_16x16x32_bf16 v[0:3], a[32:35], a[96:99], v[0:3]\n"
"s_waitcnt vmcnt(11)\n"
"s_barrier\n"
"v_mfma_f32_16x16x32_bf16 v[0:3], a[48:51], a[112:115], v[0:3]\n"
"ds_read_b128 a[64:67], v77 offset:0\n"
"s_add_u32 m0, s37, 0x8000\n"
"v_mfma_f32_16x16x32_bf16 v[4:7], a[36:39], a[96:99], v[4:7]\n"
"ds_read_b128 a[68:71], v77 offset:2048\n"
"buffer_load_dwordx4 v78, s[20:23], s31 offen offset:0 sc1 lds\n"
"v_mfma_f32_16x16x32_bf16 v[4:7], a[52:55], a[112:115], v[4:7]\n"
"ds_read_b128 a[72:75], v77 offset:4096\n"
"v_mfma_f32_16x16x32_bf16 v[8:11], a[40:43], a[96:99], v[8:11]\n"
"ds_read_b128 a[76:79], v77 offset:6144\n"
"v_mfma_f32_16x16x32_bf16 v[8:11], a[56:59], a[112:115], v[8:11]\n"
"ds_read_b64_tr_b16 a[0:1], v70 offset:32768\n"
"buffer_load_dwordx4 v78, s[20:23], s31 offen offset:1024 sc1 lds\n"
"v_mfma_f32_16x16x32_bf16 v[12:15], a[44:47], a[96:99], v[12:15]\n"
"ds_read_b64_tr_b16 a[2:3], v70 offset:36864\n"
"v_mfma_f32_16x16x32_bf16 v[12:15], a[60:63], a[112:115], v[12:15]\n"
"ds_read_b64_tr_b16 a[4:5], v71 offset:32768\n"
"v_mfma_f32_16x16x32_bf16 v[16:19], a[32:35], a[100:103], v[16:19]\n"
"ds_read_b64_tr_b16 a[6:7], v71 offset:36864\n"
"buffer_load_dwordx4 v79, s[20:23], s31 offen offset:2048 sc1 lds\n"
"v_mfma_f32_16x16x32_bf16 v[16:19], a[48:51], a[116:119], v[16:19]\n"
"ds_read_b64_tr_b16 a[8:9], v70 offset:33280\n"
"v_mfma_f32_16x16x32_bf16 v[20:23], a[36:39], a[100:103], v[20:23]\n"
"ds_read_b64_tr_b16 a[10:11], v70 offset:37376\n"
"v_mfma_f32_16x16x32_bf16 v[20:23], a[52:55], a[116:119], v[20:23]\n"
"ds_read_b64_tr_b16 a[12:13], v71 offset:33280\n"
"buffer_load_dwordx4 v79, s[20:23], s31 offen offset:3072 sc1 lds\n"
"v_mfma_f32_16x16x32_bf16 v[24:27], a[40:43], a[100:103], v[24:27]\n"
"ds_read_b64_tr_b16 a[14:15], v71 offset:37376\n"
"s_add_u32 s31, s31, 0x800\n"
"v_mfma_f32_16x16x32_bf16 v[24:27], a[56:59], a[116:119], v[24:27]\n"
"ds_read_b128 a[80:83], v77 offset:1024\n"
"v_mfma_f32_16x16x32_bf16 v[28:31], a[44:47], a[100:103], v[28:31]\n"
"ds_read_b128 a[84:87], v77 offset:3072\n"
"v_mfma_f32_16x16x32_bf16 v[28:31], a[60:63], a[116:119], v[28:31]\n"
"ds_read_b128 a[88:91], v77 offset:5120\n"
"v_mfma_f32_16x16x32_bf16 v[32:35], a[32:35], a[104:107], v[32:35]\n"
"ds_read_b128 a[92:95], v77 offset:7168\n"
"v_mfma_f32_16x16x32_bf16 v[32:35], a[48:51], a[120:123], v[32:35]\n"
"ds_read_b64_tr_b16 a[16:17], v70 offset:40960\n"
"v_mfma_f32_16x16x32_bf16 v[36:39], a[36:39], a[104:107], v[36:39]\n"
"ds_read_b64_tr_b16 a[18:19], v70 offset:45056\n"
"v_mfma_f32_16x16x32_bf16 v[36:39], a[52:55], a[120:123], v[36:39]\n"
"ds_read_b64_tr_b16 a[20:21], v71 offset:40960\n"
"v_mfma_f32_16x16x32_bf16 v[40:43], a[40:43], a[104:107], v[40:43]\n"
"ds_read_b64_tr_b16 a[22:23], v71 offset:45056\n"
"v_mfma_f32_16x16x32_bf16 v[40:43], a[56:59], a[120:123], v[40:43]\n"
"ds_read_b64_tr_b16 a[24:25], v70 offset:41472\n"
"v_mfma_f32_16x16x32_bf16 v[44:47], a[44:47], a[104:107], v[44:47]\n"
"ds_read_b64_tr_b16 a[26:27], v70 offset:45568\n"
"v_mfma_f32_16x16x32_bf16 v[44:47], a[60:63], a[120:123], v[44:47]\n"
"ds_read_b64_tr_b16 a[28:29], v71 offset:41472\n"
"v_mfma_f32_16x16x32_bf16 v[48:51], a[32:35], a[108:111], v[48:51]\n"
"ds_read_b64_tr_b16 a[30:31], v71 offset:45568\n"
"v_mfma_f32_16x16x32_bf16 v[48:51], a[48:51], a[124:127], v[48:51]\n"
"v_mfma_f32_16x16x32_bf16 v[52:55], a[36:39], a[108:111], v[52:55]\n"
"v_mfma_f32_16x16x32_bf16 v[52:55], a[52:55], a[124:127], v[52:55]\n"
"v_mfma_f32_16x16x32_bf16 v[56:59], a[40:43], a[108:111], v[56:59]\n"
"v_mfma_f32_16x16x32_bf16 v[56:59], a[56:59], a[124:127], v[56:59]\n"
"v_mfma_f32_16x16x32_bf16 v[60:63], a[44:47], a[108:111], v[60:63]\n"
"v_mfma_f32_16x16x32_bf16 v[60:63], a[60:63], a[124:127], v[60:63]\n"
"s_waitcnt lgkmcnt(0)\n"
"v_mfma_f32_16x16x32_bf16 v[0:3], a[0:3], a[64:67], v[0:3]\n"
"s_waitcnt vmcnt(11)\n"
"s_barrier\n"
"v_mfma_f32_16x16x32_bf16 v[0:3], a[16:19], a[80:83], v[0:3]\n"
"ds_read_b128 a[96:99], v77 offset:16384\n"
"s_add_u32 m0, s37, 0\n"
"v_mfma_f32_16x16x32_bf16 v[4:7], a[4:7], a[64:67], v[4:7]\n"
"ds_read_b128 a[100:103], v77 offset:18432\n"
"buffer_load_dwordx4 v78, s[20:23], s31 offen offset:0 sc1 lds\n"
"v_mfma_f32_16x16x32_bf16 v[4:7], a[20:23], a[80:83], v[4:7]\n"
"ds_read_b128 a[104:107], v77 offset:20480\n"
"v_mfma_f32_16x16x32_bf16 v[8:11], a[8:11], a[64:67], v[8:11]\n"
"ds_read_b128 a[108:111], v77 offset:22528\n"
"v_mfma_f32_16x16x32_bf16 v[8:11], a[24:27], a[80:83], v[8:11]\n"
"ds_read_b64_tr_b16 a[32:33], v76 offset:0\n"
"buffer_load_dwordx4 v78, s[20:23], s31 offen offset:1024 sc1 lds\n"
"v_mfma_f32_16x16x32_bf16 v[12:15], a[12:15], a[64:67], v[12:15]\n"
"ds_read_b64_tr_b16 a[34:35], v76 offset:4096\n"
"v_mfma_f32_16x16x32_bf16 v[12:15], a[28:31], a[80:83], v[12:15]\n"
"ds_read_b64_tr_b16 a[36:37], v88 offset:0\n"
"v_mfma_f32_16x16x32_bf16 v[16:19], a[0:3], a[68:71], v[16:19]\n"
"ds_read_b64_tr_b16 a[38:39], v88 offset:4096\n"
"buffer_load_dwordx4 v79, s[20:23], s31 offen offset:2048 sc1 lds\n"
"v_mfma_f32_16x16x32_bf16 v[16:19], a[16:19], a[84:87], v[16:19]\n"
"ds_read_b64_tr_b16 a[40:41], v76 offset:512\n"
"v_mfma_f32_16x16x32_bf16 v[20:23], a[4:7], a[68:71], v[20:23]\n"
"ds_read_b64_tr_b16 a[42:43], v76 offset:4608\n"
"v_mfma_f32_16x16x32_bf16 v[20:23], a[20:23], a[84:87], v[20:23]\n"
"ds_read_b64_tr_b16 a[44:45], v88 offset:512\n"
"buffer_load_dwordx4 v79, s[20:23], s31 offen offset:3072 sc1 lds\n"
"v_mfma_f32_16x16x32_bf16 v[24:27], a[8:11], a[68:71], v[24:27]\n"
"ds_read_b64_tr_b16 a[46:47], v88 offset:4608\n"
"s_add_u32 s31, s31, 0x800\n"
"v_mfma_f32_16x16x32_bf16 v[24:27], a[24:27], a[84:87], v[24:27]\n"
"ds_read_b128 a[112:115], v77 offset:17408\n"
"s_add_u32 m0, s44, 0x10000\n"
"v_mfma_f32_16x16x32_bf16 v[28:31], a[12:15], a[68:71], v[28:31]\n"
"ds_read_b128 a[116:119], v77 offset:19456\n"
"buffer_load_dwordx4 v96, s[40:43], s45 offen offset:0 lds\n"
"v_mfma_f32_16x16x32_bf16 v[28:31], a[28:31], a[84:87], v[28:31]\n"
"ds_read_b128 a[120:123], v77 offset:21504\n"
"v_mfma_f32_16x16x32_bf16 v[32:35], a[0:3], a[72:75], v[32:35]\n"
"ds_read_b128 a[124:127], v77 offset:23552\n"
"buffer_load_dwordx4 v97, s[40:43], s45 offen offset:1024 lds\n"
"v_mfma_f32_16x16x32_bf16 v[32:35], a[16:19], a[88:91], v[32:35]\n"
"ds_read_b64_tr_b16 a[48:49], v76 offset:8192\n"
"v_mfma_f32_16x16x32_bf16 v[36:39], a[4:7], a[72:75], v[36:39]\n"
"ds_read_b64_tr_b16 a[50:51], v76 offset:12288\n"
"buffer_load_dwordx4 v98, s[40:43], s45 offen offset:2048 lds\n"
"v_mfma_f32_16x16x32_bf16 v[36:39], a[20:23], a[88:91], v[36:39]\n"
"ds_read_b64_tr_b16 a[52:53], v88 offset:8192\n"
"v_mfma_f32_16x16x32_bf16 v[40:43], a[8:11], a[72:75], v[40:43]\n"
"ds_read_b64_tr_b16 a[54:55], v88 offset:12288\n"
"buffer_load_dwordx4 v99, s[40:43], s45 offen offset:3072 lds\n"
"v_mfma_f32_16x16x32_bf16 v[40:43], a[24:27], a[88:91], v[40:43]\n"
"ds_read_b64_tr_b16 a[56:57], v76 offset:8704\n"
"v_mfma_f32_16x16x32_bf16 v[44:47], a[12:15], a[72:75], v[44:47]\n"
"ds_read_b64_tr_b16 a[58:59], v76 offset:12800\n"
"s_add_u32 m0, s44, 0x11000\n"
"v_mfma_f32_16x16x32_bf16 v[44:47], a[28:31], a[88:91], v[44:47]\n"
"ds_read_b64_tr_b16 a[60:61], v88 offset:8704\n"
"v_mfma_f32_16x16x32_bf16 v[48:51], a[0:3], a[76:79], v[48:51]\n"
"ds_read_b64_tr_b16 a[62:63], v88 offset:12800\n"
"buffer_load_dwordx4 v100, s[40:43], s45 offen offset:0 lds\n"
"v_mfma_f32_16x16x32_bf16 v[48:51], a[16:19], a[92:95], v[48:51]\n"
"v_mfma_f32_16x16x32_bf16 v[52:55], a[4:7], a[76:79], v[52:55]\n"
"buffer_load_dwordx4 v101, s[40:43], s45 offen offset:1024 lds\n"
"v_mfma_f32_16x16x32_bf16 v[52:55], a[20:23], a[92:95], v[52:55]\n"
"v_mfma_f32_16x16x32_bf16 v[56:59], a[8:11], a[76:79], v[56:59]\n"
"buffer_load_dwordx4 v102, s[40:43], s45 offen offset:2048 lds\n"
"v_mfma_f32_16x16x32_bf16 v[56:59], a[24:27], a[92:95], v[56:59]\n"
"v_mfma_f32_16x16x32_bf16 v[60:63], a[12:15], a[76:79], v[60:63]\n"
"s_add_u32 s45, s45, 0x38000\n"
"v_mfma_f32_16x16x32_bf16 v[60:63], a[28:31], a[92:95], v[60:63]\n"
"s_waitcnt lgkmcnt(0)\n"
"v_mfma_f32_16x16x32_bf16 v[0:3], a[32:35], a[96:99], v[0:3]\n"
"s_waitcnt vmcnt(11)\n"
"s_barrier\n"
"v_mfma_f32_16x16x32_bf16 v[0:3], a[48:51], a[112:115], v[0:3]\n"
"ds_read_b128 a[64:67], v77 offset:32768\n"
"v_mfma_f32_16x16x32_bf16 v[4:7], a[36:39], a[96:99], v[4:7]\n"
"ds_read_b128 a[68:71], v77 offset:34816\n"
"v_mfma_f32_16x16x32_bf16 v[4:7], a[52:55], a[112:115], v[4:7]\n"
"ds_read_b128 a[72:75], v77 offset:36864\n"
"v_mfma_f32_16x16x32_bf16 v[8:11], a[40:43], a[96:99], v[8:11]\n"
"ds_read_b128 a[76:79], v77 offset:38912\n"
"v_mfma_f32_16x16x32_bf16 v[8:11], a[56:59], a[112:115], v[8:11]\n"
"ds_read_b64_tr_b16 a[0:1], v68 offset:0\n"
"v_mfma_f32_16x16x32_bf16 v[12:15], a[44:47], a[96:99], v[12:15]\n"
"ds_read_b64_tr_b16 a[2:3], v68 offset:4096\n"
"v_mfma_f32_16x16x32_bf16 v[12:15], a[60:63], a[112:115], v[12:15]\n"
"ds_read_b64_tr_b16 a[4:5], v69 offset:0\n"
"v_mfma_f32_16x16x32_bf16 v[16:19], a[32:35], a[100:103], v[16:19]\n"
"ds_read_b64_tr_b16 a[6:7], v69 offset:4096\n"
"v_mfma_f32_16x16x32_bf16 v[16:19], a[48:51], a[116:119], v[16:19]\n"
"ds_read_b64_tr_b16 a[8:9], v68 offset:512\n"
"v_mfma_f32_16x16x32_bf16 v[20:23], a[36:39], a[100:103], v[20:23]\n"
"ds_read_b64_tr_b16 a[10:11], v68 offset:4608\n"
"v_mfma_f32_16x16x32_bf16 v[20:23], a[52:55], a[116:119], v[20:23]\n"
"ds_read_b64_tr_b16 a[12:13], v69 offset:512\n"
"v_mfma_f32_16x16x32_bf16 v[24:27], a[40:43], a[100:103], v[24:27]\n"
"ds_read_b64_tr_b16 a[14:15], v69 offset:4608\n"
"v_mfma_f32_16x16x32_bf16 v[24:27], a[56:59], a[116:119], v[24:27]\n"
"ds_read_b128 a[80:83], v77 offset:33792\n"
"v_mfma_f32_16x16x32_bf16 v[28:31], a[44:47], a[100:103], v[28:31]\n"
"ds_read_b128 a[84:87], v77 offset:35840\n"
"v_mfma_f32_16x16x32_bf16 v[28:31], a[60:63], a[116:119], v[28:31]\n"
"ds_read_b128 a[88:91], v77 offset:37888\n"
"v_mfma_f32_16x16x32_bf16 v[32:35], a[32:35], a[104:107], v[32:35]\n"
"ds_read_b128 a[92:95], v77 offset:39936\n"
"v_mfma_f32_16x16x32_bf16 v[32:35], a[48:51], a[120:123], v[32:35]\n"
"ds_read_b64_tr_b16 a[16:17], v68 offset:8192\n"
"v_mfma_f32_16x16x32_bf16 v[36:39], a[36:39], a[104:107], v[36:39]\n"
"ds_read_b64_tr_b16 a[18:19], v68 offset:12288\n"
"v_mfma_f32_16x16x32_bf16 v[36:39], a[52:55], a[120:123], v[36:39]\n"
"ds_read_b64_tr_b16 a[20:21], v69 offset:8192\n"
"v_mfma_f32_16x16x32_bf16 v[40:43], a[40:43], a[104:107], v[40:43]\n"
"ds_read_b64_tr_b16 a[22:23], v69 offset:12288\n"
"v_mfma_f32_16x16x32_bf16 v[40:43], a[56:59], a[120:123], v[40:43]\n"
"ds_read_b64_tr_b16 a[24:25], v68 offset:8704\n"
"v_mfma_f32_16x16x32_bf16 v[44:47], a[44:47], a[104:107], v[44:47]\n"
"ds_read_b64_tr_b16 a[26:27], v68 offset:12800\n"
"v_mfma_f32_16x16x32_bf16 v[44:47], a[60:63], a[120:123], v[44:47]\n"
"ds_read_b64_tr_b16 a[28:29], v69 offset:8704\n"
"v_mfma_f32_16x16x32_bf16 v[48:51], a[32:35], a[108:111], v[48:51]\n"
"ds_read_b64_tr_b16 a[30:31], v69 offset:12800\n"
"v_mfma_f32_16x16x32_bf16 v[48:51], a[48:51], a[124:127], v[48:51]\n"
"v_mfma_f32_16x16x32_bf16 v[52:55], a[36:39], a[108:111], v[52:55]\n"
"v_mfma_f32_16x16x32_bf16 v[52:55], a[52:55], a[124:127], v[52:55]\n"
"v_mfma_f32_16x16x32_bf16 v[56:59], a[40:43], a[108:111], v[56:59]\n"
"v_mfma_f32_16x16x32_bf16 v[56:59], a[56:59], a[124:127], v[56:59]\n"
"v_mfma_f32_16x16x32_bf16 v[60:63], a[44:47], a[108:111], v[60:63]\n"
"v_mfma_f32_16x16x32_bf16 v[60:63], a[60:63], a[124:127], v[60:63]\n"
"s_waitcnt lgkmcnt(0)\n"
"v_mfma_f32_16x16x32_bf16 v[0:3], a[0:3], a[64:67], v[0:3]\n"
"s_waitcnt vmcnt(7)\n"
"s_barrier\n"
"v_mfma_f32_16x16x32_bf16 v[0:3], a[16:19], a[80:83], v[0:3]\n"
"ds_read_b128 a[96:99], v77 offset:0\n"
"v_mfma_f32_16x16x32_bf16 v[4:7], a[4:7], a[64:67], v[4:7]\n"
"ds_read_b128 a[100:103], v77 offset:2048\n"
"v_mfma_f32_16x16x32_bf16 v[4:7], a[20:23], a[80:83], v[4:7]\n"
"ds_read_b128 a[104:107], v77 offset:4096\n"
"v_mfma_f32_16x16x32_bf16 v[8:11], a[8:11], a[64:67], v[8:11]\n"
"ds_read_b128 a[108:111], v77 offset:6144\n"
"v_mfma_f32_16x16x32_bf16 v[8:11], a[24:27], a[80:83], v[8:11]\n"
"ds_read_b64_tr_b16 a[32:33], v68 offset:16384\n"
"v_mfma_f32_16x16x32_bf16 v[12:15], a[12:15], a[64:67], v[12:15]\n"
"ds_read_b64_tr_b16 a[34:35], v68 offset:20480\n"
"v_mfma_f32_16x16x32_bf16 v[12:15], a[28:31], a[80:83], v[12:15]\n"
"ds_read_b64_tr_b16 a[36:37], v69 offset:16384\n"
"v_mfma_f32_16x16x32_bf16 v[16:19], a[0:3], a[68:71], v[16:19]\n"
"ds_read_b64_tr_b16 a[38:39], v69 offset:20480\n"
"v_mfma_f32_16x16x32_bf16 v[16:19], a[16:19], a[84:87], v[16:19]\n"
"ds_read_b64_tr_b16 a[40:41], v68 offset:16896\n"
"v_mfma_f32_16x16x32_bf16 v[20:23], a[4:7], a[68:71], v[20:23]\n"
"ds_read_b64_tr_b16 a[42:43], v68 offset:20992\n"
"v_mfma_f32_16x16x32_bf16 v[20:23], a[20:23], a[84:87], v[20:23]\n"
"ds_read_b64_tr_b16 a[44:45], v69 offset:16896\n"
"v_mfma_f32_16x16x32_bf16 v[24:27], a[8:11], a[68:71], v[24:27]\n"
"ds_read_b64_tr_b16 a[46:47], v69 offset:20992\n"
"v_mfma_f32_16x16x32_bf16 v[24:27], a[24:27], a[84:87], v[24:27]\n"
"ds_read_b128 a[112:115], v77 offset:1024\n"
"v_mfma_f32_16x16x32_bf16 v[28:31], a[12:15], a[68:71], v[28:31]\n"
"ds_read_b128 a[116:119], v77 offset:3072\n"
"v_mfma_f32_16x16x32_bf16 v[28:31], a[28:31], a[84:87], v[28:31]\n"
"ds_read_b128 a[120:123], v77 offset:5120\n"
"v_mfma_f32_16x16x32_bf16 v[32:35], a[0:3], a[72:75], v[32:35]\n"
"ds_read_b128 a[124:127], v77 offset:7168\n"
"v_mfma_f32_16x16x32_bf16 v[32:35], a[16:19], a[88:91], v[32:35]\n"
"ds_read_b64_tr_b16 a[48:49], v68 offset:24576\n"
"v_mfma_f32_16x16x32_bf16 v[36:39], a[4:7], a[72:75], v[36:39]\n"
"ds_read_b64_tr_b16 a[50:51], v68 offset:28672\n"
"v_mfma_f32_16x16x32_bf16 v[36:39], a[20:23], a[88:91], v[36:39]\n"
"ds_read_b64_tr_b16 a[52:53], v69 offset:24576\n"
"v_mfma_f32_16x16x32_bf16 v[40:43], a[8:11], a[72:75], v[40:43]\n"
"ds_read_b64_tr_b16 a[54:55], v69 offset:28672\n"
"v_mfma_f32_16x16x32_bf16 v[40:43], a[24:27], a[88:91], v[40:43]\n"
"ds_read_b64_tr_b16 a[56:57], v68 offset:25088\n"
"v_mfma_f32_16x16x32_bf16 v[44:47], a[12:15], a[72:75], v[44:47]\n"
"ds_read_b64_tr_b16 a[58:59], v68 offset:29184\n"
"v_mfma_f32_16x16x32_bf16 v[44:47], a[28:31], a[88:91], v[44:47]\n"
"ds_read_b64_tr_b16 a[60:61], v69 offset:25088\n"
"v_mfma_f32_16x16x32_bf16 v[48:51], a[0:3], a[76:79], v[48:51]\n"
"ds_read_b64_tr_b16 a[62:63], v69 offset:29184\n"
"v_mfma_f32_16x16x32_bf16 v[48:51], a[16:19], a[92:95], v[48:51]\n"
"v_mfma_f32_16x16x32_bf16 v[52:55], a[4:7], a[76:79], v[52:55]\n"
"v_mfma_f32_16x16x32_bf16 v[52:55], a[20:23], a[92:95], v[52:55]\n"
"v_mfma_f32_16x16x32_bf16 v[56:59], a[8:11], a[76:79], v[56:59]\n"
"v_mfma_f32_16x16x32_bf16 v[56:59], a[24:27], a[92:95], v[56:59]\n"
"v_mfma_f32_16x16x32_bf16 v[60:63], a[12:15], a[76:79], v[60:63]\n"
"v_mfma_f32_16x16x32_bf16 v[60:63], a[28:31], a[92:95], v[60:63]\n"
"s_waitcnt lgkmcnt(0)\n"
"v_mfma_f32_16x16x32_bf16 v[0:3], a[32:35], a[96:99], v[0:3]\n"
"v_mfma_f32_16x16x32_bf16 v[0:3], a[48:51], a[112:115], v[0:3]\n"
"v_mfma_f32_16x16x32_bf16 v[4:7], a[36:39], a[96:99], v[4:7]\n"
"v_mfma_f32_16x16x32_bf16 v[4:7], a[52:55], a[112:115], v[4:7]\n"
"v_mfma_f32_16x16x32_bf16 v[8:11], a[40:43], a[96:99], v[8:11]\n"
"v_mfma_f32_16x16x32_bf16 v[8:11], a[56:59], a[112:115], v[8:11]\n"
"v_mfma_f32_16x16x32_bf16 v[12:15], a[44:47], a[96:99], v[12:15]\n"
"v_mfma_f32_16x16x32_bf16 v[12:15], a[60:63], a[112:115], v[12:15]\n"
"v_mfma_f32_16x16x32_bf16 v[16:19], a[32:35], a[100:103], v[16:19]\n"
"v_mfma_f32_16x16x32_bf16 v[16:19], a[48:51], a[116:119], v[16:19]\n"
"v_mfma_f32_16x16x32_bf16 v[20:23], a[36:39], a[100:103], v[20:23]\n"
"v_mfma_f32_16x16x32_bf16 v[20:23], a[52:55], a[116:119], v[20:23]\n"
"v_mfma_f32_16x16x32_bf16 v[24:27], a[40:43], a[100:103], v[24:27]\n"
"v_mfma_f32_16x16x32_bf16 v[24:27], a[56:59], a[116:119], v[24:27]\n"
"v_mfma_f32_16x16x32_bf16 v[28:31], a[44:47], a[100:103], v[28:31]\n"
"v_mfma_f32_16x16x32_bf16 v[28:31], a[60:63], a[116:119], v[28:31]\n"
"v_mfma_f32_16x16x32_bf16 v[32:35], a[32:35], a[104:107], v[32:35]\n"
"v_mfma_f32_16x16x32_bf16 v[32:35], a[48:51], a[120:123], v[32:35]\n"
"v_mfma_f32_16x16x32_bf16 v[36:39], a[36:39], a[104:107], v[36:39]\n"
"v_mfma_f32_16x16x32_bf16 v[36:39], a[52:55], a[120:123], v[36:39]\n"
"v_mfma_f32_16x16x32_bf16 v[40:43], a[40:43], a[104:107], v[40:43]\n"
"v_mfma_f32_16x16x32_bf16 v[40:43], a[56:59], a[120:123], v[40:43]\n"
"v_mfma_f32_16x16x32_bf16 v[44:47], a[44:47], a[104:107], v[44:47]\n"
"v_mfma_f32_16x16x32_bf16 v[44:47], a[60:63], a[120:123], v[44:47]\n"
"v_mfma_f32_16x16x32_bf16 v[48:51], a[32:35], a[108:111], v[48:51]\n"
"v_mfma_f32_16x16x32_bf16 v[48:51], a[48:51], a[124:127], v[48:51]\n"
"v_mfma_f32_16x16x32_bf16 v[52:55], a[36:39], a[108:111], v[52:55]\n"
"v_mfma_f32_16x16x32_bf16 v[52:55], a[52:55], a[124:127], v[52:55]\n"
"v_mfma_f32_16x16x32_bf16 v[56:59], a[40:43], a[108:111], v[56:59]\n"
"v_mfma_f32_16x16x32_bf16 v[56:59], a[56:59], a[124:127], v[56:59]\n"
"v_mfma_f32_16x16x32_bf16 v[60:63], a[44:47], a[108:111], v[60:63]\n"
"v_mfma_f32_16x16x32_bf16 v[60:63], a[60:63], a[124:127], v[60:63]\n"
"s_nop 15\n"
    : "={v[0:15]}"(a00), "={v[16:31]}"(a01), "={v[32:47]}"(a10), "={v[48:63]}"(a11)
    : "{s[4:5]}"(ofrag), "{s[6:7]}"(wo), "{s[8:9]}"(wg), "{s[10:11]}"(wu), "{s[12:13]}"(c), "{s16}"(bid), "{s17}"(w), "{s18}"(ldsb)
    : "v64","v65","v66","v67","v68","v69","v70","v71","v72","v73","v74","v75","v76","v77","v78","v79","v80","v81","v82","v83","v84","v85","v86","v87",
"v88","v89","v90","v91","v92","v93","v94","v95","v96","v97","v98","v99","v100","v101","v102","v103","v104","v105","v106","v107","a0","a1","a2","a3",
"a4","a5","a6","a7","a8","a9","a10","a11","a12","a13","a14","a15","a16","a17","a18","a19","a20","a21","a22","a23","a24","a25","a26","a27",
"a28","a29","a30","a31","a32","a33","a34","a35","a36","a37","a38","a39","a40","a41","a42","a43","a44","a45","a46","a47","a48","a49","a50","a51",
"a52","a53","a54","a55","a56","a57","a58","a59","a60","a61","a62","a63","a64","a65","a66","a67","a68","a69","a70","a71","a72","a73","a74","a75",
"a76","a77","a78","a79","a80","a81","a82","a83","a84","a85","a86","a87","a88","a89","a90","a91","a92","a93","a94","a95","a96","a97","a98","a99",
"a100","a101","a102","a103","a104","a105","a106","a107","a108","a109","a110","a111","a112","a113","a114","a115","a116","a117","a118","a119","a120","a121","a122","a123",
"a124","a125","a126","a127","s19","s20","s21","s22","s23","s24","s25","s26","s27","s28","s29","s30","s31","s34","s35","s36","s37","s38","s39","s40",
"s41","s42","s43","s44","s45","s46","s47","s48","s49","s50","s51","vcc","scc","memory");
    const unsigned lane = lane_now(), wq_ = w&1, wn = w>>1;
    const rsrc_t r = mk_rsrc(ws + WS_PART, 8388608);
    const unsigned off = sl*2097152u + (((rt*8 + 4*wq_)*16 + ct*4 + wn*2)*64 + lane)*16;
    frag_store(a00, r, off); frag_store(a01, r, off + 16384); frag_store(a10, r, off + 32768); frag_store(a11, r, off + 49152);
    publish(cnt + 32*(32 + rt), w==0 && lane==0);
    asm volatile(
"v_mbcnt_lo_u32_b32 v251, -1, 0\n"
"v_mbcnt_hi_u32_b32 v251, -1, v251\n"
"v_add_u32 v248, 0, v251\n"
"v_mul_u32_u24 v249, 0x124a, v248\n"
"v_lshrrev_b32 v249, 16, v249\n"
"v_mul_u32_u24 v250, 0xd20, v249\n"
"v_lshl_add_u32 v249, v248, 4, v250\n"
"v_add_u32 v240, 0xc00, v249\n"
"v_add_u32 v248, 64, v251\n"
"v_mul_u32_u24 v249, 0x124a, v248\n"
"v_lshrrev_b32 v249, 16, v249\n"
"v_mul_u32_u24 v250, 0xd20, v249\n"
"v_lshl_add_u32 v249, v248, 4, v250\n"
"v_add_u32 v241, 0x800, v249\n"
"v_add_u32 v248, 0x80, v251\n"
"v_mul_u32_u24 v249, 0x124a, v248\n"
"v_lshrrev_b32 v249, 16, v249\n"
"v_mul_u32_u24 v250, 0xd20, v249\n"
"v_lshl_add_u32 v249, v248, 4, v250\n"
"v_add_u32 v242, 0x400, v249\n"
"v_add_u32 v248, 0xc0, v251\n"
"v_mul_u32_u24 v249, 0x124a, v248\n"
"v_lshrrev_b32 v249, 16, v249\n"
"v_mul_u32_u24 v250, 0xd20, v249\n"
"v_lshl_add_u32 v249, v248, 4, v250\n"
"v_add_u32 v243, 0, v249\n"
"v_add_u32 v248, 0x100, v251\n"
"v_mul_u32_u24 v249, 0x124a, v248\n"
"v_lshrrev_b32 v249, 16, v249\n"
"v_mul_u32_u24 v250, 0xd20, v249\n"
"v_lshl_add_u32 v249, v248, 4, v250\n"
"v_add_u32 v244, 0xc00, v249\n"
"v_add_u32 v248, 0x140, v251\n"
"v_mul_u32_u24 v249, 0x124a, v248\n"
"v_lshrrev_b32 v249, 16, v249\n"
"v_mul_u32_u24 v250, 0xd20, v249\n"
"v_lshl_add_u32 v249, v248, 4, v250\n"
"v_add_u32 v245, 0x800, v249\n"
"v_add_u32 v248, 0x180, v251\n"
"v_mul_u32_u24 v249, 0x124a, v248\n"
"v_lshrrev_b32 v249, 16, v249\n"
"v_mul_u32_u24 v250, 0xd20, v249\n"
"v_lshl_add_u32 v249, v248, 4, v250\n"
"v_add_u32 v246, 0x400, v249\n"
"s_cmp_lt_u32 s17, 2\n"
"s_cselect_b32 s46, s8, s10\n"
"s_cselect_b32 s47, s9, s11\n"
"s_bfe_u32 s35, s16, 0x20001\n"
"s_lshl_b32 s35, s35, 2\n"
"s_lshr_b32 s36, s16, 6\n"
"s_add_u32 s35, s35, s36\n"
"s_mul_i32 s35, s35, 0xe0\n"
"s_sub_u32 s36, 0x1c0c00, s35\n"
"s_add_u32 s40, s46, s35\n"
"s_addc_u32 s41, s47, 0\n"
"s_sub_u32 s40, s40, 0xc00\n"
"s_subb_u32 s41, s41, 0\n"
"s_and_b32 s41, s41, 0xffff\n"
"s_mov_b32 s42, s36\n"
"s_mov_b32 s43, 0x20000\n"
"s_and_b32 s35, s17, 1\n"
"s_mul_i32 s45, s35, 0x1c000\n"
"s_add_u32 s45, s45, 0x70000\n"
"s_mul_i32 s36, s35, 0x1c00\n"
"s_lshr_b32 s35, s17, 1\n"
"s_mul_i32 s35, s35, 0x3880\n"
"s_add_u32 s44, s36, s35\n"
"s_add_u32 s44, s44, s18\n"
"s_add_u32 m0, s44, 0x18000\n"
"s_nop 0\n"
"buffer_load_dwordx4 v240, s[40:43], s45 offen offset:0 lds\n"
"buffer_load_dwordx4 v241, s[40:43], s45 offen offset:1024 lds\n"
"buffer_load_dwordx4 v242, s[40:43], s45 offen offset:2048 lds\n"
"buffer_load_dwordx4 v243, s[40:43], s45 offen offset:3072 lds\n"
"s_add_u32 m0, s44, 0x19000\n"
"s_nop 0\n"
"buffer_load_dwordx4 v244, s[40:43], s45 offen offset:0 lds\n"
"buffer_load_dwordx4 v245, s[40:43], s45 offen offset:1024 lds\n"
"buffer_load_dwordx4 v246, s[40:43], s45 offen offset:2048 lds\n"
"s_add_u32 s45, s45, 0x38000\n"
    :: "{s[8:9]}"(wg), "{s[10:11]}"(wu), "{s16}"(bid), "{s17}"(w), "{s18}"(ldsb)
    : "v240","v241","v242","v243","v244","v245","v246","v247","v248","v249","v250","v251","s19","s20","s21","s22","s23","s24","s25","s26","s27","s28","s29","s30",
"s31","s34","s35","s36","s37","s38","s39","s40","s41","s42","s43","s44","s45","s46","s47","s48","s49","s50","s51","vcc","scc","memory");
  }
  {
    float* red = (float*)(lds + LDS_P3);
    const unsigned lane = lane_now(), t = w*64 + lane, rb = bid>>1, oc = bid&1;
    const unsigned r8 = t&7, g4 = (t>>3)&3, kq = t>>5;
    const unsigned l = 8*oc + r8 + 16*g4, row = rb*16 + 8*oc + r8;
    u32x4 pv[2][4]; u32x2 xv[2][2]; f32x4 gv[2][2], tv[2][4];
#pragma unroll
    for(int i=0;i<2;++i){ const int s = kq + 8*i;
#pragma unroll
      for(int a=0;a<2;++a){ const int n = 32*s + 16*a + 4*g4; xv[i][a] = *(const u32x2*)(x + (size_t)row*512 + n); gv[i][a] = *(const f32x4*)(tabs + 2048 + n); }
#pragma unroll
      for(int k=0;k<4;++k) tv[i][k] = *(const f32x4*)(tabs + 1024 + (s*4+g4)*16 + 4*k);
    }
    if(w==0){
      const unsigned* c = cnt + 32*(32 + (bid>>4));
      for(int spin=0; spin<400000; ++spin){
        if(__builtin_amdgcn_readfirstlane(__hip_atomic_load(c, __ATOMIC_RELAXED, __HIP_MEMORY_SCOPE_AGENT)) >= 16u) break;
        __builtin_amdgcn_s_sleep(1);
      }
    }
    asm volatile("" ::: "memory"); __builtin_amdgcn_s_barrier(); asm volatile("" ::: "memory");
    const rsrc_t rp = mk_rsrc(ws + WS_PART, 8388608), rx = mk_rsrc(ws + WS_X1, 2097152), rh = mk_rsrc(ws + WS_H2, 2097152);
#pragma unroll
    for(int i=0;i<2;++i)
#pragma unroll
      for(int k=0;k<4;++k) pv[i][k] = ld16(rp, k*2097152u + ((rb*16 + kq + 8*i)*64 + l)*16);
    float v[16]; float sm = 0;
#pragma unroll
    for(int i=0;i<2;++i){ u32x4 o;
#pragma unroll
      for(int d=0;d<4;++d){
        const float y0 = (blo(pv[i][0][d]) + blo(pv[i][1][d])) + (blo(pv[i][2][d]) + blo(pv[i][3][d]));
        const float y1 = (bhi(pv[i][0][d]) + bhi(pv[i][1][d])) + (bhi(pv[i][2][d]) + bhi(pv[i][3][d]));
        const u32 xx = xv[i][d>>1][d&1];
        o[d] = pk2(blo(xx) + gv[i][d>>1][2*(d&1)]*y0, bhi(xx) + gv[i][d>>1][2*(d&1)+1]*y1);
        v[8*i+2*d] = blo(o[d]); v[8*i+2*d+1] = bhi(o[d]); sm += v[8*i+2*d] + v[8*i+2*d+1];
      }
      st16(rx, ((rb*16 + kq + 8*i)*64 + l)*16, o);
    }
    sm += __shfl_xor(sm, 8); sm += __shfl_xor(sm, 16); sm += __shfl_xor(sm, 32);
    if(lane<8) red[w*8 + r8] = sm;
    asm volatile("s_waitcnt lgkmcnt(0)" ::: "memory"); __builtin_amdgcn_s_barrier(); asm volatile("" ::: "memory");
    const float m = (red[r8]+red[8+r8]+red[16+r8]+red[24+r8])*(1.f/512.f);
    float q = 0;
#pragma unroll
    for(int i=0;i<16;++i){ v[i] -= m; q += v[i]*v[i]; }
    q += __shfl_xor(q, 8); q += __shfl_xor(q, 16); q += __shfl_xor(q, 32);
    if(lane<8) red[32 + w*8 + r8] = q;
    asm volatile("s_waitcnt lgkmcnt(0)" ::: "memory"); __builtin_amdgcn_s_barrier(); asm volatile("" ::: "memory");
    const float r = 1.f/sqrtf((red[32+r8]+red[40+r8]+red[48+r8]+red[56+r8])*(1.f/512.f) + 1e-5f);
#pragma unroll
    for(int i=0;i<2;++i){ u32x4 o;
#pragma unroll
      for(int d=0;d<4;++d){
        const int j0 = 2*d, j1 = 2*d+1;
        o[d] = pk2(v[8*i+j0]*r*tv[i][j0>>2][j0&3] + tv[i][2+(j0>>2)][j0&3], v[8*i+j1]*r*tv[i][j1>>2][j1&3] + tv[i][2+(j1>>2)][j1&3]);
      }
      st16(rh, ((rb*16 + kq + 8*i)*64 + l)*16, o);
    }
    publish(cnt + 32*(48 + (bid>>4)), w==0 && lane==0);
  }
  {
    const unsigned rt = (bid&1)*8 + ((bid>>3)&7), ct = ((bid&7)>>1)*4 + (bid>>6);
    const void* h2 = ws + WS_H2; const unsigned* c = cnt + 32*(48 + rt);
    f32x16 a0,a1,a2,a3,a4,a5,a6;
    asm volatile(
"v_mbcnt_lo_u32_b32 v195, -1, 0\n"
"v_mbcnt_hi_u32_b32 v195, -1, v195\n"
"v_add_u32 v192, 0, v195\n"
"v_mul_u32_u24 v193, 0x124a, v192\n"
"v_lshrrev_b32 v193, 16, v193\n"
"v_mul_u32_u24 v194, 0xd20, v193\n"
"v_lshl_add_u32 v193, v192, 4, v194\n"
"v_add_u32 v200, 0xc00, v193\n"
"v_add_u32 v192, 64, v195\n"
"v_mul_u32_u24 v193, 0x124a, v192\n"
"v_lshrrev_b32 v193, 16, v193\n"
"v_mul_u32_u24 v194, 0xd20, v193\n"
"v_lshl_add_u32 v193, v192, 4, v194\n"
"v_add_u32 v201, 0x800, v193\n"
"v_add_u32 v192, 0x80, v195\n"
"v_mul_u32_u24 v193, 0x124a, v192\n"
"v_lshrrev_b32 v193, 16, v193\n"
"v_mul_u32_u24 v194, 0xd20, v193\n"
"v_lshl_add_u32 v193, v192, 4, v194\n"
"v_add_u32 v202, 0x400, v193\n"
"v_add_u32 v192, 0xc0, v195\n"
"v_mul_u32_u24 v193, 0x124a, v192\n"
"v_lshrrev_b32 v193, 16, v193\n"
"v_mul_u32_u24 v194, 0xd20, v193\n"
"v_lshl_add_u32 v193, v192, 4, v194\n"
"v_add_u32 v203, 0, v193\n"
"v_add_u32 v192, 0x100, v195\n"
"v_mul_u32_u24 v193, 0x124a, v192\n"
"v_lshrrev_b32 v193, 16, v193\n"
"v_mul_u32_u24 v194, 0xd20, v193\n"
"v_lshl_add_u32 v193, v192, 4, v194\n"
"v_add_u32 v204, 0xc00, v193\n"
"v_add_u32 v192, 0x140, v195\n"
"v_mul_u32_u24 v193, 0x124a, v192\n"
"v_lshrrev_b32 v193, 16, v193\n"
"v_mul_u32_u24 v194, 0xd20, v193\n"
"v_lshl_add_u32 v193, v192, 4, v194\n"
"v_add_u32 v205, 0x800, v193\n"
"v_add_u32 v192, 0x180, v195\n"
"v_mul_u32_u24 v193, 0x124a, v192\n"
"v_lshrrev_b32 v193, 16, v193\n"
"v_mul_u32_u24 v194, 0xd20, v193\n"
"v_lshl_add_u32 v193, v192, 4, v194\n"
"v_add_u32 v206, 0x400, v193\n"
"s_cmp_lt_u32 s17, 2\n"
"s_cselect_b32 s46, s8, s10\n"
"s_cselect_b32 s47, s9, s11\n"
"s_bfe_u32 s35, s16, 0x20001\n"
"s_lshl_b32 s35, s35, 2\n"
"s_lshr_b32 s36, s16, 6\n"
"s_add_u32 s35, s35, s36\n"
"s_mul_i32 s35, s35, 0xe0\n"
"s_sub_u32 s36, 0x1c0c00, s35\n"
"s_add_u32 s24, s46, s35\n"
"s_addc_u32 s25, s47, 0\n"
"s_sub_u32 s24, s24, 0xc00\n"
"s_subb_u32 s25, s25, 0\n"
"s_and_b32 s25, s25, 0xffff\n"
"s_mov_b32 s26, s36\n"
"s_mov_b32 s27, 0x20000\n"
"s_and_b32 s35, s17, 1\n"
"s_mul_i32 s30, s35, 0x1c000\n"
"s_add_u32 s30, s30, 0xa8000\n"
"s_mul_i32 s36, s35, 0x1c00\n"
"s_lshr_b32 s35, s17, 1\n"
"s_mul_i32 s35, s35, 0x3880\n"
"s_add_u32 s29, s36, s35\n"
"s_add_u32 s29, s29, s18\n"
"v_lshlrev_b32 v209, 4, v195\n"
"v_add_u32 v211, 0x4000, v209\n"
"s_and_b32 s35, s16, 1\n"
"s_lshl_b32 s35, s35, 3\n"
"s_bfe_u32 s36, s16, 0x30003\n"
"s_add_u32 s35, s35, s36\n"
"s_lshl_b32 s35, s35, 2\n"
"s_add_u32 s35, s35, s17\n"
"s_lshl_b32 s35, s35, 15\n"
"s_add_u32 s20, s4, s35\n"
"s_addc_u32 s21, s5, 0\n"
"s_and_b32 s21, s21, 0xffff\n"
"s_mov_b32 s22, 0x8000\n"
"s_mov_b32 s23, 0x20000\n"
"s_mov_b32 s31, 0\n"
"v_lshrrev_b32 v192, 2, v195\n"
"v_mul_u32_u24 v192, 0xe0, v192\n"
"v_and_b32 v194, 3, v195\n"
"v_lshl_add_u32 v192, v194, 3, v192\n"
"v_add_u32 v207, s18, v192\n"
"v_add_u32 v208, 0x10000, v207\n"
"v_add_u32 v210, 0x20000, v207\n"
"v_mbcnt_lo_u32_b32 v118, -1, 0\n"
"v_mbcnt_hi_u32_b32 v118, -1, v118\n"
"v_bfe_u32 v116, v118, 4, 1\n"
"v_lshlrev_b32 v116, 1, v116\n"
"v_and_b32 v117, 3, v118\n"
"v_xor_b32 v116, v116, v117\n"
"v_lshlrev_b32 v116, 4, v116\n"
"v_lshrrev_b32 v117, 5, v118\n"
"v_lshl_add_u32 v116, v117, 6, v116\n"
"v_bfe_u32 v117, v118, 2, 3\n"
"v_lshl_add_u32 v112, v117, 10, v116\n"
"v_add_u32 v113, 0x1c00, v112\n"
"v_add_u32 v114, 0x3800, v112\n"
"v_add_u32 v115, 0x5400, v112\n"
"s_mul_i32 s38, s17, 0x70000\n"
"s_bfe_u32 s39, s16, 0x30003\n"
"s_lshl_b32 s39, s39, 7\n"
"s_add_u32 s38, s38, s39\n"
"s_sub_u32 s39, 0x70000, s39\n"
"s_add_u32 s40, s6, s38\n"
"s_addc_u32 s41, s7, 0\n"
"s_and_b32 s41, s41, 0xffff\n"
"s_mov_b32 s42, s39\n"
"s_mov_b32 s43, 0x20000\n"
"s_mov_b32 s45, 0\n"
"s_mov_b32 s37, 0x8000\n"
"s_lshl_b32 s38, s17, 13\n"
"s_add_u32 s44, s18, s38\n"
"ds_read_b64_tr_b16 a[0:1], v207 offset:32768\n"
"ds_read_b64_tr_b16 a[2:3], v207 offset:36352\n"
"ds_read_b64_tr_b16 a[4:5], v207 offset:47232\n"
"ds_read_b64_tr_b16 a[6:7], v207 offset:50816\n"
"ds_read_b64_tr_b16 a[8:9], v207 offset:32800\n"
"ds_read_b64_tr_b16 a[10:11], v207 offset:36384\n"
"ds_read_b64_tr_b16 a[12:13], v207 offset:47264\n"
"ds_read_b64_tr_b16 a[14:15], v207 offset:50848\n"
"ds_read_b64_tr_b16 a[16:17], v207 offset:32832\n"
"ds_read_b64_tr_b16 a[18:19], v207 offset:36416\n"
"ds_read_b64_tr_b16 a[20:21], v207 offset:47296\n"
"ds_read_b64_tr_b16 a[22:23], v207 offset:50880\n"
"ds_read_b64_tr_b16 a[24:25], v207 offset:32864\n"
"ds_read_b64_tr_b16 a[26:27], v207 offset:36448\n"
"ds_read_b64_tr_b16 a[28:29], v207 offset:47328\n"
"ds_read_b64_tr_b16 a[30:31], v207 offset:50912\n"
"ds_read_b64_tr_b16 a[32:33], v207 offset:32896\n"
"ds_read_b64_tr_b16 a[34:35], v207 offset:36480\n"
"ds_read_b64_tr_b16 a[36:37], v207 offset:47360\n"
"ds_read_b64_tr_b16 a[38:39], v207 offset:50944\n"
"ds_read_b64_tr_b16 a[40:41], v207 offset:32928\n"
"ds_read_b64_tr_b16 a[42:43], v207 offset:36512\n"
"ds_read_b64_tr_b16 a[44:45], v207 offset:47392\n"
"ds_read_b64_tr_b16 a[46:47], v207 offset:50976\n"
"ds_read_b64_tr_b16 a[48:49], v207 offset:32960\n"
"ds_read_b64_tr_b16 a[50:51], v207 offset:36544\n"
"ds_read_b64_tr_b16 a[52:53], v207 offset:47424\n"
"ds_read_b64_tr_b16 a[54:55], v207 offset:51008\n"
"s_add_u32 m0, s29, 0x20000\n"
"s_nop 0\n"
"buffer_load_dwordx4 v200, s[24:27], s30 offen offset:0 lds\n"
"buffer_load_dwordx4 v201, s[24:27], s30 offen offset:1024 lds\n"
"buffer_load_dwordx4 v202, s[24:27], s30 offen offset:2048 lds\n"
"buffer_load_dwordx4 v203, s[24:27], s30 offen offset:3072 lds\n"
"s_add_u32 m0, s29, 0x21000\n"
"s_nop 0\n"
"buffer_load_dwordx4 v204, s[24:27], s30 offen offset:0 lds\n"
"buffer_load_dwordx4 v205, s[24:27], s30 offen offset:1024 lds\n"
"buffer_load_dwordx4 v206, s[24:27], s30 offen offset:2048 lds\n"
"s_add_u32 s30, s30, 0x38000\n"
"s_add_u32 m0, s29, 0\n"
"s_nop 0\n"
"buffer_load_dwordx4 v200, s[24:27], s30 offen offset:0 lds\n"
"buffer_load_dwordx4 v201, s[24:27], s30 offen offset:1024 lds\n"
"buffer_load_dwordx4 v202, s[24:27], s30 offen offset:2048 lds\n"
"buffer_load_dwordx4 v203, s[24:27], s30 offen offset:3072 lds\n"
"s_add_u32 m0, s29, 0x1000\n"
"s_nop 0\n"
"buffer_load_dwordx4 v204, s[24:27], s30 offen offset:0 lds\n"
"buffer_load_dwordx4 v205, s[24:27], s30 offen offset:1024 lds\n"
"buffer_load_dwordx4 v206, s[24:27], s30 offen offset:2048 lds\n"
"s_add_u32 s30, s30, 0x38000\n"
"s_cmp_lg_u32 s17, 0\n"
"s_cbranch_scc1 L_gu_done_%=\n"
"v_mov_b32 v213, 0\n"
"s_mov_b32 s36, 0x61a80\n"
"L_gu_poll_%=:\n"
"global_load_dword v212, v213, s[12:13] sc1\n"
"s_waitcnt vmcnt(0)\n"
"v_readfirstlane_b32 s35, v212\n"
"s_cmp_ge_u32 s35, 16\n"
"s_cbranch_scc1 L_gu_done_%=\n"
"s_sub_u32 s36, s36, 1\n"
"s_cmp_eq_u32 s36, 0\n"
"s_cbranch_scc1 L_gu_done_%=\n"
"s_sleep 1\n"
"s_branch L_gu_poll_%=\n"
"L_gu_done_%=:\n"
"s_barrier\n"
"buffer_load_dwordx4 a[112:115], v209, s[20:23], s31 offen offset:0 sc1\n"
"buffer_load_dwordx4 a[116:119], v211, s[20:23], s31 offen offset:0 sc1\n"
"buffer_load_dwordx4 a[120:123], v209, s[20:23], s31 offen offset:1024 sc1\n"
"buffer_load_dwordx4 a[124:127], v211, s[20:23], s31 offen offset:1024 sc1\n"
"s_add_u32 s31, s31, 0x800\n"
"buffer_load_dwordx4 a[128:131], v209, s[20:23], s31 offen offset:0 sc1\n"
"buffer_load_dwordx4 a[132:135], v211, s[20:23], s31 offen offset:0 sc1\n"
"buffer_load_dwordx4 a[136:139], v209, s[20:23], s31 offen offset:1024 sc1\n"
"buffer_load_dwordx4 a[140:143], v211, s[20:23], s31 offen offset:1024 sc1\n"
"s_add_u32 s31, s31, 0x800\n"
"buffer_load_dwordx4 a[144:147], v209, s[20:23], s31 offen offset:0 sc1\n"
"buffer_load_dwordx4 a[148:151], v211, s[20:23], s31 offen offset:0 sc1\n"
"buffer_load_dwordx4 a[152:155], v209, s[20:23], s31 offen offset:1024 sc1\n"
"buffer_load_dwordx4 a[156:159], v211, s[20:23], s31 offen offset:1024 sc1\n"
"s_add_u32 s31, s31, 0x800\n"
"s_waitcnt lgkmcnt(0)\n"
"s_waitcnt vmcnt(8)\n"
"v_mfma_f32_16x16x32_bf16 v[0:3], a[0:3], a[112:115], 0\n"
"ds_read_b64_tr_b16 a[56:57], v207 offset:39936\n"
"ds_read_b64_tr_b16 a[58:59], v207 offset:43520\n"
"v_mfma_f32_16x16x32_bf16 v[4:7], a[0:3], a[116:119], 0\n"
"ds_read_b64_tr_b16 a[60:61], v207 offset:54400\n"
"buffer_load_dwordx4 a[160:163], v209, s[20:23], s31 offen offset:0 sc1\n"
"v_mfma_f32_16x16x32_bf16 v[8:11], a[4:7], a[112:115], 0\n"
"ds_read_b64_tr_b16 a[62:63], v207 offset:57984\n"
"v_mfma_f32_16x16x32_bf16 v[12:15], a[4:7], a[116:119], 0\n"
"ds_read_b64_tr_b16 a[64:65], v207 offset:39968\n"
"v_mfma_f32_16x16x32_bf16 v[16:19], a[8:11], a[112:115], 0\n"
"ds_read_b64_tr_b16 a[66:67], v207 offset:43552\n"
"buffer_load_dwordx4 a[164:167], v211, s[20:23], s31 offen offset:0 sc1\n"
"v_mfma_f32_16x16x32_bf16 v[20:23], a[8:11], a[116:119], 0\n"
"ds_read_b64_tr_b16 a[68:69], v207 offset:54432\n"
"v_mfma_f32_16x16x32_bf16 v[24:27], a[12:15], a[112:115], 0\n"
"ds_read_b64_tr_b16 a[70:71], v207 offset:58016\n"
"ds_read_b64_tr_b16 a[72:73], v207 offset:40000\n"
"v_mfma_f32_16x16x32_bf16 v[28:31], a[12:15], a[116:119], 0\n"
"ds_read_b64_tr_b16 a[74:75], v207 offset:43584\n"
"buffer_load_dwordx4 a[168:171], v209, s[20:23], s31 offen offset:1024 sc1\n"
"v_mfma_f32_16x16x32_bf16 v[32:35], a[16:19], a[112:115], 0\n"
"ds_read_b64_tr_b16 a[76:77], v207 offset:54464\n"
"v_mfma_f32_16x16x32_bf16 v[36:39], a[16:19], a[116:119], 0\n"
"ds_read_b64_tr_b16 a[78:79], v207 offset:58048\n"
"v_mfma_f32_16x16x32_bf16 v[40:43], a[20:23], a[112:115], 0\n"
"ds_read_b64_tr_b16 a[80:81], v207 offset:40032\n"
"buffer_load_dwordx4 a[172:175], v211, s[20:23], s31 offen offset:1024 sc1\n"
"v_mfma_f32_16x16x32_bf16 v[44:47], a[20:23], a[116:119], 0\n"
"ds_read_b64_tr_b16 a[82:83], v207 offset:43616\n"
"s_add_u32 s31, s31, 0x800\n"
"v_mfma_f32_16x16x32_bf16 v[48:51], a[24:27], a[112:115], 0\n"
"ds_read_b64_tr_b16 a[84:85], v207 offset:54496\n"
"ds_read_b64_tr_b16 a[86:87], v207 offset:58080\n"
"v_mfma_f32_16x16x32_bf16 v[52:55], a[24:27], a[116:119], 0\n"
"ds_read_b64_tr_b16 a[88:89], v207 offset:40064\n"
"buffer_load_dwordx4 a[176:179], v209, s[20:23], s31 offen offset:0 sc1\n"
"v_mfma_f32_16x16x32_bf16 v[56:59], a[28:31], a[112:115], 0\n"
"ds_read_b64_tr_b16 a[90:91], v207 offset:43648\n"
"v_mfma_f32_16x16x32_bf16 v[60:63], a[28:31], a[116:119], 0\n"
"ds_read_b64_tr_b16 a[92:93], v207 offset:54528\n"
"v_mfma_f32_16x16x32_bf16 v[64:67], a[32:35], a[112:115], 0\n"
"ds_read_b64_tr_b16 a[94:95], v207 offset:58112\n"
"buffer_load_dwordx4 a[180:183], v211, s[20:23], s31 offen offset:0 sc1\n"
"v_mfma_f32_16x16x32_bf16 v[68:71], a[32:35], a[116:119], 0\n"
"ds_read_b64_tr_b16 a[96:97], v207 offset:40096\n"
"v_mfma_f32_16x16x32_bf16 v[72:75], a[36:39], a[112:115], 0\n"
"ds_read_b64_tr_b16 a[98:99], v207 offset:43680\n"
"ds_read_b64_tr_b16 a[100:101], v207 offset:54560\n"
"v_mfma_f32_16x16x32_bf16 v[76:79], a[36:39], a[116:119], 0\n"
"ds_read_b64_tr_b16 a[102:103], v207 offset:58144\n"
"buffer_load_dwordx4 a[184:187], v209, s[20:23], s31 offen offset:1024 sc1\n"
"v_mfma_f32_16x16x32_bf16 v[80:83], a[40:43], a[112:115], 0\n"
"ds_read_b64_tr_b16 a[104:105], v207 offset:40128\n"
"v_mfma_f32_16x16x32_bf16 v[84:87], a[40:43], a[116:119], 0\n"
"ds_read_b64_tr_b16 a[106:107], v207 offset:43712\n"
"v_mfma_f32_16x16x32_bf16 v[88:91], a[44:47], a[112:115], 0\n"
"ds_read_b64_tr_b16 a[108:109], v207 offset:54592\n"
"buffer_load_dwordx4 a[188:191], v211, s[20:23], s31 offen offset:1024 sc1\n"
"v_mfma_f32_16x16x32_bf16 v[92:95], a[44:47], a[116:119], 0\n"
"ds_read_b64_tr_b16 a[110:111], v207 offset:58176\n"
"s_add_u32 s31, s31, 0x800\n"
"v_mfma_f32_16x16x32_bf16 v[96:99], a[48:51], a[112:115], 0\n"
"v_mfma_f32_16x16x32_bf16 v[100:103], a[48:51], a[116:119], 0\n"
"v_mfma_f32_16x16x32_bf16 v[104:107], a[52:55], a[112:115], 0\n"
"v_mfma_f32_16x16x32_bf16 v[108:111], a[52:55], a[116:119], 0\n"
"s_waitcnt lgkmcnt(0)\n"
"v_mfma_f32_16x16x32_bf16 v[0:3], a[56:59], a[120:123], v[0:3]\n"
"s_barrier\n"
"ds_read_b64_tr_b16 a[0:1], v208 offset:0\n"
"ds_read_b64_tr_b16 a[2:3], v208 offset:3584\n"
"v_mfma_f32_16x16x32_bf16 v[4:7], a[56:59], a[124:127], v[4:7]\n"
"ds_read_b64_tr_b16 a[4:5], v208 offset:14464\n"
"buffer_load_dwordx4 a[192:195], v209, s[20:23], s31 offen offset:0 sc1\n"
"v_mfma_f32_16x16x32_bf16 v[8:11], a[60:63], a[120:123], v[8:11]\n"
"ds_read_b64_tr_b16 a[6:7], v208 offset:18048\n"
"v_mfma_f32_16x16x32_bf16 v[12:15], a[60:63], a[124:127], v[12:15]\n"
"ds_read_b64_tr_b16 a[8:9], v208 offset:32\n"
"v_mfma_f32_16x16x32_bf16 v[16:19], a[64:67], a[120:123], v[16:19]\n"
"ds_read_b64_tr_b16 a[10:11], v208 offset:3616\n"
"buffer_load_dwordx4 a[196:199], v211, s[20:23], s31 offen offset:0 sc1\n"
"v_mfma_f32_16x16x32_bf16 v[20:23], a[64:67], a[124:127], v[20:23]\n"
"ds_read_b64_tr_b16 a[12:13], v208 offset:14496\n"
"v_mfma_f32_16x16x32_bf16 v[24:27], a[68:71], a[120:123], v[24:27]\n"
"ds_read_b64_tr_b16 a[14:15], v208 offset:18080\n"
"ds_read_b64_tr_b16 a[16:17], v208 offset:64\n"
"v_mfma_f32_16x16x32_bf16 v[28:31], a[68:71], a[124:127], v[28:31]\n"
"ds_read_b64_tr_b16 a[18:19], v208 offset:3648\n"
"buffer_load_dwordx4 a[200:203], v209, s[20:23], s31 offen offset:1024 sc1\n"
"v_mfma_f32_16x16x32_bf16 v[32:35], a[72:75], a[120:123], v[32:35]\n"
"ds_read_b64_tr_b16 a[20:21], v208 offset:14528\n"
"v_mfma_f32_16x16x32_bf16 v[36:39], a[72:75], a[124:127], v[36:39]\n"
"ds_read_b64_tr_b16 a[22:23], v208 offset:18112\n"
"v_mfma_f32_16x16x32_bf16 v[40:43], a[76:79], a[120:123], v[40:43]\n"
"ds_read_b64_tr_b16 a[24:25], v208 offset:96\n"
"buffer_load_dwordx4 a[204:207], v211, s[20:23], s31 offen offset:1024 sc1\n"
"v_mfma_f32_16x16x32_bf16 v[44:47], a[76:79], a[124:127], v[44:47]\n"
"ds_read_b64_tr_b16 a[26:27], v208 offset:3680\n"
"s_add_u32 s31, s31, 0x800\n"
"v_mfma_f32_16x16x32_bf16 v[48:51], a[80:83], a[120:123], v[48:51]\n"
"ds_read_b64_tr_b16 a[28:29], v208 offset:14560\n"
"ds_read_b64_tr_b16 a[30:31], v208 offset:18144\n"
"v_mfma_f32_16x16x32_bf16 v[52:55], a[80:83], a[124:127], v[52:55]\n"
"ds_read_b64_tr_b16 a[32:33], v208 offset:128\n"
"buffer_load_dwordx4 a[208:211], v209, s[20:23], s31 offen offset:0 sc1\n"
"v_mfma_f32_16x16x32_bf16 v[56:59], a[84:87], a[120:123], v[56:59]\n"
"ds_read_b64_tr_b16 a[34:35], v208 offset:3712\n"
"v_mfma_f32_16x16x32_bf16 v[60:63], a[84:87], a[124:127], v[60:63]\n"
"ds_read_b64_tr_b16 a[36:37], v208 offset:14592\n"
"v_mfma_f32_16x16x32_bf16 v[64:67], a[88:91], a[120:123], v[64:67]\n"
"ds_read_b64_tr_b16 a[38:39], v208 offset:18176\n"
"buffer_load_dwordx4 a[212:215], v211, s[20:23], s31 offen offset:0 sc1\n"
"v_mfma_f32_16x16x32_bf16 v[68:71], a[88:91], a[124:127], v[68:71]\n"
"ds_read_b64_tr_b16 a[40:41], v208 offset:160\n"
"v_mfma_f32_16x16x32_bf16 v[72:75], a[92:95], a[120:123], v[72:75]\n"
"ds_read_b64_tr_b16 a[42:43], v208 offset:3744\n"
"ds_read_b64_tr_b16 a[44:45], v208 offset:14624\n"
"v_mfma_f32_16x16x32_bf16 v[76:79], a[92:95], a[124:127], v[76:79]\n"
"ds_read_b64_tr_b16 a[46:47], v208 offset:18208\n"
"buffer_load_dwordx4 a[216:219], v209, s[20:23], s31 offen offset:1024 sc1\n"
"v_mfma_f32_16x16x32_bf16 v[80:83], a[96:99], a[120:123], v[80:83]\n"
"ds_read_b64_tr_b16 a[48:49], v208 offset:192\n"
"v_mfma_f32_16x16x32_bf16 v[84:87], a[96:99], a[124:127], v[84:87]\n"
"ds_read_b64_tr_b16 a[50:51], v208 offset:3776\n"
"v_mfma_f32_16x16x32_bf16 v[88:91], a[100:103], a[120:123], v[88:91]\n"
"ds_read_b64_tr_b16 a[52:53], v208 offset:14656\n"
"buffer_load_dwordx4 a[220:223], v211, s[20:23], s31 offen offset:1024 sc1\n"
"v_mfma_f32_16x16x32_bf16 v[92:95], a[100:103], a[124:127], v[92:95]\n"
"ds_read_b64_tr_b16 a[54:55], v208 offset:18240\n"
"s_add_u32 s31, s31, 0x800\n"
"v_mfma_f32_16x16x32_bf16 v[96:99], a[104:107], a[120:123], v[96:99]\n"
"v_mfma_f32_16x16x32_bf16 v[100:103], a[104:107], a[124:127], v[100:103]\n"
"v_mfma_f32_16x16x32_bf16 v[104:107], a[108:111], a[120:123], v[104:107]\n"
"v_mfma_f32_16x16x32_bf16 v[108:111], a[108:111], a[124:127], v[108:111]\n"
"s_waitcnt lgkmcnt(0)\n"
"s_waitcnt vmcnt(20)\n"
"v_mfma_f32_16x16x32_bf16 v[0:3], a[0:3], a[128:131], v[0:3]\n"
"ds_read_b64_tr_b16 a[56:57], v208 offset:7168\n"
"ds_read_b64_tr_b16 a[58:59], v208 offset:10752\n"
"v_mfma_f32_16x16x32_bf16 v[4:7], a[0:3], a[132:135], v[4:7]\n"
"ds_read_b64_tr_b16 a[60:61], v208 offset:21632\n"
"buffer_load_dwordx4 a[224:227], v209, s[20:23], s31 offen offset:0 sc1\n"
"v_mfma_f32_16x16x32_bf16 v[8:11], a[4:7], a[128:131], v[8:11]\n"
"ds_read_b64_tr_b16 a[62:63], v208 offset:25216\n"
"v_mfma_f32_16x16x32_bf16 v[12:15], a[4:7], a[132:135], v[12:15]\n"
"ds_read_b64_tr_b16 a[64:65], v208 offset:7200\n"
"v_mfma_f32_16x16x32_bf16 v[16:19], a[8:11], a[128:131], v[16:19]\n"
"ds_read_b64_tr_b16 a[66:67], v208 offset:10784\n"
"buffer_load_dwordx4 a[228:231], v211, s[20:23], s31 offen offset:0 sc1\n"
"v_mfma_f32_16x16x32_bf16 v[20:23], a[8:11], a[132:135], v[20:23]\n"
"ds_read_b64_tr_b16 a[68:69], v208 offset:21664\n"
"v_mfma_f32_16x16x32_bf16 v[24:27], a[12:15], a[128:131], v[24:27]\n"
"ds_read_b64_tr_b16 a[70:71], v208 offset:25248\n"
"ds_read_b64_tr_b16 a[72:73], v208 offset:7232\n"
"v_mfma_f32_16x16x32_bf16 v[28:31], a[12:15], a[132:135], v[28:31]\n"
"ds_read_b64_tr_b16 a[74:75], v208 offset:10816\n"
"buffer_load_dwordx4 a[232:235], v209, s[20:23], s31 offen offset:1024 sc1\n"
"v_mfma_f32_16x16x32_bf16 v[32:35], a[16:19], a[128:131], v[32:35]\n"
"ds_read_b64_tr_b16 a[76:77], v208 offset:21696\n"
"v_mfma_f32_16x16x32_bf16 v[36:39], a[16:19], a[132:135], v[36:39]\n"
"ds_read_b64_tr_b16 a[78:79], v208 offset:25280\n"
"v_mfma_f32_16x16x32_bf16 v[40:43], a[20:23], a[128:131], v[40:43]\n"
"ds_read_b64_tr_b16 a[80:81], v208 offset:7264\n"
"buffer_load_dwordx4 a[236:239], v211, s[20:23], s31 offen offset:1024 sc1\n"
"v_mfma_f32_16x16x32_bf16 v[44:47], a[20:23], a[132:135], v[44:47]\n"
"ds_read_b64_tr_b16 a[82:83], v208 offset:10848\n"
"s_add_u32 s31, s31, 0x800\n"
"v_mfma_f32_16x16x32_bf16 v[48:51], a[24:27], a[128:131], v[48:51]\n"
"ds_read_b64_tr_b16 a[84:85], v208 offset:21728\n"
"ds_read_b64_tr_b16 a[86:87], v208 offset:25312\n"
"v_mfma_f32_16x16x32_bf16 v[52:55], a[24:27], a[132:135], v[52:55]\n"
"ds_read_b64_tr_b16 a[88:89], v208 offset:7296\n"
"v_mfma_f32_16x16x32_bf16 v[56:59], a[28:31], a[128:131], v[56:59]\n"
"ds_read_b64_tr_b16 a[90:91], v208 offset:10880\n"
"v_mfma_f32_16x16x32_bf16 v[60:63], a[28:31], a[132:135], v[60:63]\n"
"ds_read_b64_tr_b16 a[92:93], v208 offset:21760\n"
"v_mfma_f32_16x16x32_bf16 v[64:67], a[32:35], a[128:131], v[64:67]\n"
"ds_read_b64_tr_b16 a[94:95], v208 offset:25344\n"
"v_mfma_f32_16x16x32_bf16 v[68:71], a[32:35], a[132:135], v[68:71]\n"
"ds_read_b64_tr_b16 a[96:97], v208 offset:7328\n"
"v_mfma_f32_16x16x32_bf16 v[72:75], a[36:39], a[128:131], v[72:75]\n"
"ds_read_b64_tr_b16 a[98:99], v208 offset:10912\n"
"ds_read_b64_tr_b16 a[100:101], v208 offset:21792\n"
"v_mfma_f32_16x16x32_bf16 v[76:79], a[36:39], a[132:135], v[76:79]\n"
"ds_read_b64_tr_b16 a[102:103], v208 offset:25376\n"
"v_mfma_f32_16x16x32_bf16 v[80:83], a[40:43], a[128:131], v[80:83]\n"
"ds_read_b64_tr_b16 a[104:105], v208 offset:7360\n"
"v_mfma_f32_16x16x32_bf16 v[84:87], a[40:43], a[132:135], v[84:87]\n"
"ds_read_b64_tr_b16 a[106:107], v208 offset:10944\n"
"v_mfma_f32_16x16x32_bf16 v[88:91], a[44:47], a[128:131], v[88:91]\n"
"ds_read_b64_tr_b16 a[108:109], v208 offset:21824\n"
"v_mfma_f32_16x16x32_bf16 v[92:95], a[44:47], a[132:135], v[92:95]\n"
"ds_read_b64_tr_b16 a[110:111], v208 offset:25408\n"
"v_mfma_f32_16x16x32_bf16 v[96:99], a[48:51], a[128:131], v[96:99]\n"
"v_mfma_f32_16x16x32_bf16 v[100:103], a[48:51], a[132:135], v[100:103]\n"
"v_mfma_f32_16x16x32_bf16 v[104:107], a[52:55], a[128:131], v[104:107]\n"
"v_mfma_f32_16x16x32_bf16 v[108:111], a[52:55], a[132:135], v[108:111]\n"
"s_waitcnt lgkmcnt(0)\n"
"v_mfma_f32_16x16x32_bf16 v[0:3], a[56:59], a[136:139], v[0:3]\n"
"s_barrier\n"
"ds_read_b64_tr_b16 a[0:1], v208 offset:32768\n"
"ds_read_b64_tr_b16 a[2:3], v208 offset:36352\n"
"v_mfma_f32_16x16x32_bf16 v[4:7], a[56:59], a[140:143], v[4:7]\n"
"ds_read_b64_tr_b16 a[4:5], v208 offset:47232\n"
"s_add_u32 m0, s29, 0x8000\n"
"v_mfma_f32_16x16x32_bf16 v[8:11], a[60:63], a[136:139], v[8:11]\n"
"ds_read_b64_tr_b16 a[6:7], v208 offset:50816\n"
"buffer_load_dwordx4 v200, s[24:27], s30 offen offset:0 lds\n"
"v_mfma_f32_16x16x32_bf16 v[12:15], a[60:63], a[140:143], v[12:15]\n"
"ds_read_b64_tr_b16 a[8:9], v208 offset:32800\n"
"v_mfma_f32_16x16x32_bf16 v[16:19], a[64:67], a[136:139], v[16:19]\n"
"ds_read_b64_tr_b16 a[10:11], v208 offset:36384\n"
"v_mfma_f32_16x16x32_bf16 v[20:23], a[64:67], a[140:143], v[20:23]\n"
"ds_read_b64_tr_b16 a[12:13], v208 offset:47264\n"
"buffer_load_dwordx4 v201, s[24:27], s30 offen offset:1024 lds\n"
"v_mfma_f32_16x16x32_bf16 v[24:27], a[68:71], a[136:139], v[24:27]\n"
"ds_read_b64_tr_b16 a[14:15], v208 offset:50848\n"
"ds_read_b64_tr_b16 a[16:17], v208 offset:32832\n"
"v_mfma_f32_16x16x32_bf16 v[28:31], a[68:71], a[140:143], v[28:31]\n"
"ds_read_b64_tr_b16 a[18:19], v208 offset:36416\n"
"v_mfma_f32_16x16x32_bf16 v[32:35], a[72:75], a[136:139], v[32:35]\n"
"ds_read_b64_tr_b16 a[20:21], v208 offset:47296\n"
"buffer_load_dwordx4 v202, s[24:27], s30 offen offset:2048 lds\n"
"v_mfma_f32_16x16x32_bf16 v[36:39], a[72:75], a[140:143], v[36:39]\n"
"ds_read_b64_tr_b16 a[22:23], v208 offset:50880\n"
"v_mfma_f32_16x16x32_bf16 v[40:43], a[76:79], a[136:139], v[40:43]\n"
"ds_read_b64_tr_b16 a[24:25], v208 offset:32864\n"
"v_mfma_f32_16x16x32_bf16 v[44:47], a[76:79], a[140:143], v[44:47]\n"
"ds_read_b64_tr_b16 a[26:27], v208 offset:36448\n"
"buffer_load_dwordx4 v203, s[24:27], s30 offen offset:3072 lds\n"
"v_mfma_f32_16x16x32_bf16 v[48:51], a[80:83], a[136:139], v[48:51]\n"
"ds_read_b64_tr_b16 a[28:29], v208 offset:47328\n"
"ds_read_b64_tr_b16 a[30:31], v208 offset:50912\n"
"v_mfma_f32_16x16x32_bf16 v[52:55], a[80:83], a[140:143], v[52:55]\n"
"ds_read_b64_tr_b16 a[32:33], v208 offset:32896\n"
"s_add_u32 m0, s29, 0x9000\n"
"v_mfma_f32_16x16x32_bf16 v[56:59], a[84:87], a[136:139], v[56:59]\n"
"ds_read_b64_tr_b16 a[34:35], v208 offset:36480\n"
"buffer_load_dwordx4 v204, s[24:27], s30 offen offset:0 lds\n"
"v_mfma_f32_16x16x32_bf16 v[60:63], a[84:87], a[140:143], v[60:63]\n"
"ds_read_b64_tr_b16 a[36:37], v208 offset:47360\n"
"v_mfma_f32_16x16x32_bf16 v[64:67], a[88:91], a[136:139], v[64:67]\n"
"ds_read_b64_tr_b16 a[38:39], v208 offset:50944\n"
"v_mfma_f32_16x16x32_bf16 v[68:71], a[88:91], a[140:143], v[68:71]\n"
"ds_read_b64_tr_b16 a[40:41], v208 offset:32928\n"
"buffer_load_dwordx4 v205, s[24:27], s30 offen offset:1024 lds\n"
"v_mfma_f32_16x16x32_bf16 v[72:75], a[92:95], a[136:139], v[72:75]\n"
"ds_read_b64_tr_b16 a[42:43], v208 offset:36512\n"
"ds_read_b64_tr_b16 a[44:45], v208 offset:47392\n"
"v_mfma_f32_16x16x32_bf16 v[76:79], a[92:95], a[140:143], v[76:79]\n"
"ds_read_b64_tr_b16 a[46:47], v208 offset:50976\n"
"v_mfma_f32_16x16x32_bf16 v[80:83], a[96:99], a[136:139], v[80:83]\n"
"ds_read_b64_tr_b16 a[48:49], v208 offset:32960\n"
"buffer_load_dwordx4 v206, s[24:27], s30 offen offset:2048 lds\n"
"v_mfma_f32_16x16x32_bf16 v[84:87], a[96:99], a[140:143], v[84:87]\n"
"ds_read_b64_tr_b16 a[50:51], v208 offset:36544\n"
"v_mfma_f32_16x16x32_bf16 v[88:91], a[100:103], a[136:139], v[88:91]\n"
"ds_read_b64_tr_b16 a[52:53], v208 offset:47424\n"
"v_mfma_f32_16x16x32_bf16 v[92:95], a[100:103], a[140:143], v[92:95]\n"
"ds_read_b64_tr_b16 a[54:55], v208 offset:51008\n"
"v_mfma_f32_16x16x32_bf16 v[96:99], a[104:107], a[136:139], v[96:99]\n"
"s_add_u32 s30, s30, 0x38000\n"
"v_mfma_f32_16x16x32_bf16 v[100:103], a[104:107], a[140:143], v[100:103]\n"
"v_mfma_f32_16x16x32_bf16 v[104:107], a[108:111], a[136:139], v[104:107]\n"
"v_mfma_f32_16x16x32_bf16 v[108:111], a[108:111], a[140:143], v[108:111]\n"
"s_waitcnt lgkmcnt(0)\n"
"s_waitcnt vmcnt(27)\n"
"v_mfma_f32_16x16x32_bf16 v[0:3], a[0:3], a[144:147], v[0:3]\n"
"ds_read_b64_tr_b16 a[56:57], v208 offset:39936\n"
"ds_read_b64_tr_b16 a[58:59], v208 offset:43520\n"
"v_mfma_f32_16x16x32_bf16 v[4:7], a[0:3], a[148:151], v[4:7]\n"
"ds_read_b64_tr_b16 a[60:61], v208 offset:54400\n"
"v_mfma_f32_16x16x32_bf16 v[8:11], a[4:7], a[144:147], v[8:11]\n"
"ds_read_b64_tr_b16 a[62:63], v208 offset:57984\n"
"v_mfma_f32_16x16x32_bf16 v[12:15], a[4:7], a[148:151], v[12:15]\n"
"ds_read_b64_tr_b16 a[64:65], v208 offset:39968\n"
"v_mfma_f32_16x16x32_bf16 v[16:19], a[8:11], a[144:147], v[16:19]\n"
"ds_read_b64_tr_b16 a[66:67], v208 offset:43552\n"
"v_mfma_f32_16x16x32_bf16 v[20:23], a[8:11], a[148:151], v[20:23]\n"
"ds_read_b64_tr_b16 a[68:69], v208 offset:54432\n"
"v_mfma_f32_16x16x32_bf16 v[24:27], a[12:15], a[144:147], v[24:27]\n"
"ds_read_b64_tr_b16 a[70:71], v208 offset:58016\n"
"ds_read_b64_tr_b16 a[72:73], v208 offset:40000\n"
"v_mfma_f32_16x16x32_bf16 v[28:31], a[12:15], a[148:151], v[28:31]\n"
"ds_read_b64_tr_b16 a[74:75], v208 offset:43584\n"
"v_mfma_f32_16x16x32_bf16 v[32:35], a[16:19], a[144:147], v[32:35]\n"
"ds_read_b64_tr_b16 a[76:77], v208 offset:54464\n"
"v_mfma_f32_16x16x32_bf16 v[36:39], a[16:19], a[148:151], v[36:39]\n"
"ds_read_b64_tr_b16 a[78:79], v208 offset:58048\n"
"v_mfma_f32_16x16x32_bf16 v[40:43], a[20:23], a[144:147], v[40:43]\n"
"ds_read_b64_tr_b16 a[80:81], v208 offset:40032\n"
"v_mfma_f32_16x16x32_bf16 v[44:47], a[20:23], a[148:151], v[44:47]\n"
"ds_read_b64_tr_b16 a[82:83], v208 offset:43616\n"
"v_mfma_f32_16x16x32_bf16 v[48:51], a[24:27], a[144:147], v[48:51]\n"
"ds_read_b64_tr_b16 a[84:85], v208 offset:54496\n"
"ds_read_b64_tr_b16 a[86:87], v208 offset:58080\n"
"v_mfma_f32_16x16x32_bf16 v[52:55], a[24:27], a[148:151], v[52:55]\n"
"ds_read_b64_tr_b16 a[88:89], v208 offset:40064\n"
"v_mfma_f32_16x16x32_bf16 v[56:59], a[28:31], a[144:147], v[56:59]\n"
"ds_read_b64_tr_b16 a[90:91], v208 offset:43648\n"
"v_mfma_f32_16x16x32_bf16 v[60:63], a[28:31], a[148:151], v[60:63]\n"
"ds_read_b64_tr_b16 a[92:93], v208 offset:54528\n"
"v_mfma_f32_16x16x32_bf16 v[64:67], a[32:35], a[144:147], v[64:67]\n"
"ds_read_b64_tr_b16 a[94:95], v208 offset:58112\n"
"v_mfma_f32_16x16x32_bf16 v[68:71], a[32:35], a[148:151], v[68:71]\n"
"ds_read_b64_tr_b16 a[96:97], v208 offset:40096\n"
"v_mfma_f32_16x16x32_bf16 v[72:75], a[36:39], a[144:147], v[72:75]\n"
"ds_read_b64_tr_b16 a[98:99], v208 offset:43680\n"
"ds_read_b64_tr_b16 a[100:101], v208 offset:54560\n"
"v_mfma_f32_16x16x32_bf16 v[76:79], a[36:39], a[148:151], v[76:79]\n"
"ds_read_b64_tr_b16 a[102:103], v208 offset:58144\n"
"v_mfma_f32_16x16x32_bf16 v[80:83], a[40:43], a[144:147], v[80:83]\n"
"ds_read_b64_tr_b16 a[104:105], v208 offset:40128\n"
"v_mfma_f32_16x16x32_bf16 v[84:87], a[40:43], a[148:151], v[84:87]\n"
"ds_read_b64_tr_b16 a[106:107], v208 offset:43712\n"
"v_mfma_f32_16x16x32_bf16 v[88:91], a[44:47], a[144:147], v[88:91]\n"
"ds_read_b64_tr_b16 a[108:109], v208 offset:54592\n"
"v_mfma_f32_16x16x32_bf16 v[92:95], a[44:47], a[148:151], v[92:95]\n"
"ds_read_b64_tr_b16 a[110:111], v208 offset:58176\n"
"v_mfma_f32_16x16x32_bf16 v[96:99], a[48:51], a[144:147], v[96:99]\n"
"v_mfma_f32_16x16x32_bf16 v[100:103], a[48:51], a[148:151], v[100:103]\n"
"v_mfma_f32_16x16x32_bf16 v[104:107], a[52:55], a[144:147], v[104:107]\n"
"v_mfma_f32_16x16x32_bf16 v[108:111], a[52:55], a[148:151], v[108:111]\n"
"s_waitcnt lgkmcnt(0)\n"
"v_mfma_f32_16x16x32_bf16 v[0:3], a[56:59], a[152:155], v[0:3]\n"
"s_barrier\n"
"ds_read_b64_tr_b16 a[0:1], v210 offset:0\n"
"ds_read_b64_tr_b16 a[2:3], v210 offset:3584\n"
"v_mfma_f32_16x16x32_bf16 v[4:7], a[56:59], a[156:159], v[4:7]\n"
"ds_read_b64_tr_b16 a[4:5], v210 offset:14464\n"
"s_add_u32 m0, s29, 0x10000\n"
"v_mfma_f32_16x16x32_bf16 v[8:11], a[60:63], a[152:155], v[8:11]\n"
"ds_read_b64_tr_b16 a[6:7], v210 offset:18048\n"
"buffer_load_dwordx4 v200, s[24:27], s30 offen offset:0 lds\n"
"v_mfma_f32_16x16x32_bf16 v[12:15], a[60:63], a[156:159], v[12:15]\n"
"ds_read_b64_tr_b16 a[8:9], v210 offset:32\n"
"v_mfma_f32_16x16x32_bf16 v[16:19], a[64:67], a[152:155], v[16:19]\n"
"ds_read_b64_tr_b16 a[10:11], v210 offset:3616\n"
"v_mfma_f32_16x16x32_bf16 v[20:23], a[64:67], a[156:159], v[20:23]\n"
"ds_read_b64_tr_b16 a[12:13], v210 offset:14496\n"
"buffer_load_dwordx4 v201, s[24:27], s30 offen offset:1024 lds\n"
"v_mfma_f32_16x16x32_bf16 v[24:27], a[68:71], a[152:155], v[24:27]\n"
"ds_read_b64_tr_b16 a[14:15], v210 offset:18080\n"
"ds_read_b64_tr_b16 a[16:17], v210 offset:64\n"
"v_mfma_f32_16x16x32_bf16 v[28:31], a[68:71], a[156:159], v[28:31]\n"
"ds_read_b64_tr_b16 a[18:19], v210 offset:3648\n"
"v_mfma_f32_16x16x32_bf16 v[32:35], a[72:75], a[152:155], v[32:35]\n"
"ds_read_b64_tr_b16 a[20:21], v210 offset:14528\n"
"buffer_load_dwordx4 v202, s[24:27], s30 offen offset:2048 lds\n"
"v_mfma_f32_16x16x32_bf16 v[36:39], a[72:75], a[156:159], v[36:39]\n"
"ds_read_b64_tr_b16 a[22:23], v210 offset:18112\n"
"v_mfma_f32_16x16x32_bf16 v[40:43], a[76:79], a[152:155], v[40:43]\n"
"ds_read_b64_tr_b16 a[24:25], v210 offset:96\n"
"v_mfma_f32_16x16x32_bf16 v[44:47], a[76:79], a[156:159], v[44:47]\n"
"ds_read_b64_tr_b16 a[26:27], v210 offset:3680\n"
"buffer_load_dwordx4 v203, s[24:27], s30 offen offset:3072 lds\n"
"v_mfma_f32_16x16x32_bf16 v[48:51], a[80:83], a[152:155], v[48:51]\n"
"ds_read_b64_tr_b16 a[28:29], v210 offset:14560\n"
"ds_read_b64_tr_b16 a[30:31], v210 offset:18144\n"
"v_mfma_f32_16x16x32_bf16 v[52:55], a[80:83], a[156:159], v[52:55]\n"
"ds_read_b64_tr_b16 a[32:33], v210 offset:128\n"
"s_add_u32 m0, s29, 0x11000\n"
"v_mfma_f32_16x16x32_bf16 v[56:59], a[84:87], a[152:155], v[56:59]\n"
"ds_read_b64_tr_b16 a[34:35], v210 offset:3712\n"
"buffer_load_dwordx4 v204, s[24:27], s30 offen offset:0 lds\n"
"v_mfma_f32_16x16x32_bf16 v[60:63], a[84:87], a[156:159], v[60:63]\n"
"ds_read_b64_tr_b16 a[36:37], v210 offset:14592\n"
"v_mfma_f32_16x16x32_bf16 v[64:67], a[88:91], a[152:155], v[64:67]\n"
"ds_read_b64_tr_b16 a[38:39], v210 offset:18176\n"
"v_mfma_f32_16x16x32_bf16 v[68:71], a[88:91], a[156:159], v[68:71]\n"
"ds_read_b64_tr_b16 a[40:41], v210 offset:160\n"
"buffer_load_dwordx4 v205, s[24:27], s30 offen offset:1024 lds\n"
"v_mfma_f32_16x16x32_bf16 v[72:75], a[92:95], a[152:155], v[72:75]\n"
"ds_read_b64_tr_b16 a[42:43], v210 offset:3744\n"
"ds_read_b64_tr_b16 a[44:45], v210 offset:14624\n"
"v_mfma_f32_16x16x32_bf16 v[76:79], a[92:95], a[156:159], v[76:79]\n"
"ds_read_b64_tr_b16 a[46:47], v210 offset:18208\n"
"v_mfma_f32_16x16x32_bf16 v[80:83], a[96:99], a[152:155], v[80:83]\n"
"ds_read_b64_tr_b16 a[48:49], v210 offset:192\n"
"buffer_load_dwordx4 v206, s[24:27], s30 offen offset:2048 lds\n"
"v_mfma_f32_16x16x32_bf16 v[84:87], a[96:99], a[156:159], v[84:87]\n"
"ds_read_b64_tr_b16 a[50:51], v210 offset:3776\n"
"v_mfma_f32_16x16x32_bf16 v[88:91], a[100:103], a[152:155], v[88:91]\n"
"ds_read_b64_tr_b16 a[52:53], v210 offset:14656\n"
"v_mfma_f32_16x16x32_bf16 v[92:95], a[100:103], a[156:159], v[92:95]\n"
"ds_read_b64_tr_b16 a[54:55], v210 offset:18240\n"
"v_mfma_f32_16x16x32_bf16 v[96:99], a[104:107], a[152:155], v[96:99]\n"
"s_add_u32 s30, s30, 0x38000\n"
"v_mfma_f32_16x16x32_bf16 v[100:103], a[104:107], a[156:159], v[100:103]\n"
"v_mfma_f32_16x16x32_bf16 v[104:107], a[108:111], a[152:155], v[104:107]\n"
"v_mfma_f32_16x16x32_bf16 v[108:111], a[108:111], a[156:159], v[108:111]\n"
"s_waitcnt lgkmcnt(0)\n"
"s_waitcnt vmcnt(30)\n"
"v_mfma_f32_16x16x32_bf16 v[0:3], a[0:3], a[160:163], v[0:3]\n"
"ds_read_b64_tr_b16 a[56:57], v210 offset:7168\n"
"ds_read_b64_tr_b16 a[58:59], v210 offset:10752\n"
"v_mfma_f32_16x16x32_bf16 v[4:7], a[0:3], a[164:167], v[4:7]\n"
"ds_read_b64_tr_b16 a[60:61], v210 offset:21632\n"
"v_mfma_f32_16x16x32_bf16 v[8:11], a[4:7], a[160:163], v[8:11]\n"
"ds_read_b64_tr_b16 a[62:63], v210 offset:25216\n"
"v_mfma_f32_16x16x32_bf16 v[12:15], a[4:7], a[164:167], v[12:15]\n"
"ds_read_b64_tr_b16 a[64:65], v210 offset:7200\n"
"v_mfma_f32_16x16x32_bf16 v[16:19], a[8:11], a[160:163], v[16:19]\n"
"ds_read_b64_tr_b16 a[66:67], v210 offset:10784\n"
"v_mfma_f32_16x16x32_bf16 v[20:23], a[8:11], a[164:167], v[20:23]\n"
"ds_read_b64_tr_b16 a[68:69], v210 offset:21664\n"
"v_mfma_f32_16x16x32_bf16 v[24:27], a[12:15], a[160:163], v[24:27]\n"
"ds_read_b64_tr_b16 a[70:71], v210 offset:25248\n"
"ds_read_b64_tr_b16 a[72:73], v210 offset:7232\n"
"v_mfma_f32_16x16x32_bf16 v[28:31], a[12:15], a[164:167], v[28:31]\n"
"ds_read_b64_tr_b16 a[74:75], v210 offset:10816\n"
"v_mfma_f32_16x16x32_bf16 v[32:35], a[16:19], a[160:163], v[32:35]\n"
"ds_read_b64_tr_b16 a[76:77], v210 offset:21696\n"
"v_mfma_f32_16x16x32_bf16 v[36:39], a[16:19], a[164:167], v[36:39]\n"
"ds_read_b64_tr_b16 a[78:79], v210 offset:25280\n"
"v_mfma_f32_16x16x32_bf16 v[40:43], a[20:23], a[160:163], v[40:43]\n"
"ds_read_b64_tr_b16 a[80:81], v210 offset:7264\n"
"v_mfma_f32_16x16x32_bf16 v[44:47], a[20:23], a[164:167], v[44:47]\n"
"ds_read_b64_tr_b16 a[82:83], v210 offset:10848\n"
"v_mfma_f32_16x16x32_bf16 v[48:51], a[24:27], a[160:163], v[48:51]\n"
"ds_read_b64_tr_b16 a[84:85], v210 offset:21728\n"
"ds_read_b64_tr_b16 a[86:87], v210 offset:25312\n"
"v_mfma_f32_16x16x32_bf16 v[52:55], a[24:27], a[164:167], v[52:55]\n"
"ds_read_b64_tr_b16 a[88:89], v210 offset:7296\n"
"v_mfma_f32_16x16x32_bf16 v[56:59], a[28:31], a[160:163], v[56:59]\n"
"ds_read_b64_tr_b16 a[90:91], v210 offset:10880\n"
"v_mfma_f32_16x16x32_bf16 v[60:63], a[28:31], a[164:167], v[60:63]\n"
"ds_read_b64_tr_b16 a[92:93], v210 offset:21760\n"
"v_mfma_f32_16x16x32_bf16 v[64:67], a[32:35], a[160:163], v[64:67]\n"
"ds_read_b64_tr_b16 a[94:95], v210 offset:25344\n"
"v_mfma_f32_16x16x32_bf16 v[68:71], a[32:35], a[164:167], v[68:71]\n"
"ds_read_b64_tr_b16 a[96:97], v210 offset:7328\n"
"v_mfma_f32_16x16x32_bf16 v[72:75], a[36:39], a[160:163], v[72:75]\n"
"ds_read_b64_tr_b16 a[98:99], v210 offset:10912\n"
"ds_read_b64_tr_b16 a[100:101], v210 offset:21792\n"
"v_mfma_f32_16x16x32_bf16 v[76:79], a[36:39], a[164:167], v[76:79]\n"
"ds_read_b64_tr_b16 a[102:103], v210 offset:25376\n"
"v_mfma_f32_16x16x32_bf16 v[80:83], a[40:43], a[160:163], v[80:83]\n"
"ds_read_b64_tr_b16 a[104:105], v210 offset:7360\n"
"v_mfma_f32_16x16x32_bf16 v[84:87], a[40:43], a[164:167], v[84:87]\n"
"ds_read_b64_tr_b16 a[106:107], v210 offset:10944\n"
"v_mfma_f32_16x16x32_bf16 v[88:91], a[44:47], a[160:163], v[88:91]\n"
"ds_read_b64_tr_b16 a[108:109], v210 offset:21824\n"
"v_mfma_f32_16x16x32_bf16 v[92:95], a[44:47], a[164:167], v[92:95]\n"
"ds_read_b64_tr_b16 a[110:111], v210 offset:25408\n"
"v_mfma_f32_16x16x32_bf16 v[96:99], a[48:51], a[160:163], v[96:99]\n"
"v_mfma_f32_16x16x32_bf16 v[100:103], a[48:51], a[164:167], v[100:103]\n"
"v_mfma_f32_16x16x32_bf16 v[104:107], a[52:55], a[160:163], v[104:107]\n"
"v_mfma_f32_16x16x32_bf16 v[108:111], a[52:55], a[164:167], v[108:111]\n"
"s_waitcnt lgkmcnt(0)\n"
"v_mfma_f32_16x16x32_bf16 v[0:3], a[56:59], a[168:171], v[0:3]\n"
"s_barrier\n"
"ds_read_b64_tr_b16 a[0:1], v207 offset:0\n"
"ds_read_b64_tr_b16 a[2:3], v207 offset:3584\n"
"v_mfma_f32_16x16x32_bf16 v[4:7], a[56:59], a[172:175], v[4:7]\n"
"ds_read_b64_tr_b16 a[4:5], v207 offset:14464\n"
"s_add_u32 m0, s29, 0x18000\n"
"v_mfma_f32_16x16x32_bf16 v[8:11], a[60:63], a[168:171], v[8:11]\n"
"ds_read_b64_tr_b16 a[6:7], v207 offset:18048\n"
"buffer_load_dwordx4 v200, s[24:27], s30 offen offset:0 lds\n"
"v_mfma_f32_16x16x32_bf16 v[12:15], a[60:63], a[172:175], v[12:15]\n"
"ds_read_b64_tr_b16 a[8:9], v207 offset:32\n"
"v_mfma_f32_16x16x32_bf16 v[16:19], a[64:67], a[168:171], v[16:19]\n"
"ds_read_b64_tr_b16 a[10:11], v207 offset:3616\n"
"v_mfma_f32_16x16x32_bf16 v[20:23], a[64:67], a[172:175], v[20:23]\n"
"ds_read_b64_tr_b16 a[12:13], v207 offset:14496\n"
"buffer_load_dwordx4 v201, s[24:27], s30 offen offset:1024 lds\n"
"v_mfma_f32_16x16x32_bf16 v[24:27], a[68:71], a[168:171], v[24:27]\n"
"ds_read_b64_tr_b16 a[14:15], v207 offset:18080\n"
"ds_read_b64_tr_b16 a[16:17], v207 offset:64\n"
"v_mfma_f32_16x16x32_bf16 v[28:31], a[68:71], a[172:175], v[28:31]\n"
"ds_read_b64_tr_b16 a[18:19], v207 offset:3648\n"
"v_mfma_f32_16x16x32_bf16 v[32:35], a[72:75], a[168:171], v[32:35]\n"
"ds_read_b64_tr_b16 a[20:21], v207 offset:14528\n"
"buffer_load_dwordx4 v202, s[24:27], s30 offen offset:2048 lds\n"
"v_mfma_f32_16x16x32_bf16 v[36:39], a[72:75], a[172:175], v[36:39]\n"
"ds_read_b64_tr_b16 a[22:23], v207 offset:18112\n"
"v_mfma_f32_16x16x32_bf16 v[40:43], a[76:79], a[168:171], v[40:43]\n"
"ds_read_b64_tr_b16 a[24:25], v207 offset:96\n"
"v_mfma_f32_16x16x32_bf16 v[44:47], a[76:79], a[172:175], v[44:47]\n"
"ds_read_b64_tr_b16 a[26:27], v207 offset:3680\n"
"buffer_load_dwordx4 v203, s[24:27], s30 offen offset:3072 lds\n"
"v_mfma_f32_16x16x32_bf16 v[48:51], a[80:83], a[168:171], v[48:51]\n"
"ds_read_b64_tr_b16 a[28:29], v207 offset:14560\n"
"ds_read_b64_tr_b16 a[30:31], v207 offset:18144\n"
"v_mfma_f32_16x16x32_bf16 v[52:55], a[80:83], a[172:175], v[52:55]\n"
"ds_read_b64_tr_b16 a[32:33], v207 offset:128\n"
"s_add_u32 m0, s29, 0x19000\n"
"v_mfma_f32_16x16x32_bf16 v[56:59], a[84:87], a[168:171], v[56:59]\n"
"ds_read_b64_tr_b16 a[34:35], v207 offset:3712\n"
"buffer_load_dwordx4 v204, s[24:27], s30 offen offset:0 lds\n"
"v_mfma_f32_16x16x32_bf16 v[60:63], a[84:87], a[172:175], v[60:63]\n"
"ds_read_b64_tr_b16 a[36:37], v207 offset:14592\n"
"v_mfma_f32_16x16x32_bf16 v[64:67], a[88:91], a[168:171], v[64:67]\n"
"ds_read_b64_tr_b16 a[38:39], v207 offset:18176\n"
"v_mfma_f32_16x16x32_bf16 v[68:71], a[88:91], a[172:175], v[68:71]\n"
"ds_read_b64_tr_b16 a[40:41], v207 offset:160\n"
"buffer_load_dwordx4 v205, s[24:27], s30 offen offset:1024 lds\n"
"v_mfma_f32_16x16x32_bf16 v[72:75], a[92:95], a[168:171], v[72:75]\n"
"ds_read_b64_tr_b16 a[42:43], v207 offset:3744\n"
"ds_read_b64_tr_b16 a[44:45], v207 offset:14624\n"
"v_mfma_f32_16x16x32_bf16 v[76:79], a[92:95], a[172:175], v[76:79]\n"
"ds_read_b64_tr_b16 a[46:47], v207 offset:18208\n"
"v_mfma_f32_16x16x32_bf16 v[80:83], a[96:99], a[168:171], v[80:83]\n"
"ds_read_b64_tr_b16 a[48:49], v207 offset:192\n"
"buffer_load_dwordx4 v206, s[24:27], s30 offen offset:2048 lds\n"
"v_mfma_f32_16x16x32_bf16 v[84:87], a[96:99], a[172:175], v[84:87]\n"
"ds_read_b64_tr_b16 a[50:51], v207 offset:3776\n"
"v_mfma_f32_16x16x32_bf16 v[88:91], a[100:103], a[168:171], v[88:91]\n"
"ds_read_b64_tr_b16 a[52:53], v207 offset:14656\n"
"v_mfma_f32_16x16x32_bf16 v[92:95], a[100:103], a[172:175], v[92:95]\n"
"ds_read_b64_tr_b16 a[54:55], v207 offset:18240\n"
"v_mfma_f32_16x16x32_bf16 v[96:99], a[104:107], a[168:171], v[96:99]\n"
"s_add_u32 s30, s30, 0x38000\n"
"v_mfma_f32_16x16x32_bf16 v[100:103], a[104:107], a[172:175], v[100:103]\n"
"v_mfma_f32_16x16x32_bf16 v[104:107], a[108:111], a[168:171], v[104:107]\n"
"v_mfma_f32_16x16x32_bf16 v[108:111], a[108:111], a[172:175], v[108:111]\n"
"s_waitcnt lgkmcnt(0)\n"
"s_waitcnt vmcnt(33)\n"
"v_mfma_f32_16x16x32_bf16 v[0:3], a[0:3], a[176:179], v[0:3]\n"
"s_add_u32 m0, s44, 0x20000\n"
"ds_read_b64_tr_b16 a[56:57], v207 offset:7168\n"
"ds_read_b64_tr_b16 a[58:59], v207 offset:10752\n"
"v_mfma_f32_16x16x32_bf16 v[4:7], a[0:3], a[180:183], v[4:7]\n"
"buffer_load_dwordx4 v112, s[40:43], s45 offen offset:0 lds\n"
"ds_read_b64_tr_b16 a[60:61], v207 offset:21632\n"
"v_mfma_f32_16x16x32_bf16 v[8:11], a[4:7], a[176:179], v[8:11]\n"
"ds_read_b64_tr_b16 a[62:63], v207 offset:25216\n"
"v_mfma_f32_16x16x32_bf16 v[12:15], a[4:7], a[180:183], v[12:15]\n"
"ds_read_b64_tr_b16 a[64:65], v207 offset:7200\n"
"v_mfma_f32_16x16x32_bf16 v[16:19], a[8:11], a[176:179], v[16:19]\n"
"buffer_load_dwordx4 v113, s[40:43], s45 offen offset:1024 lds\n"
"ds_read_b64_tr_b16 a[66:67], v207 offset:10784\n"
"v_mfma_f32_16x16x32_bf16 v[20:23], a[8:11], a[180:183], v[20:23]\n"
"ds_read_b64_tr_b16 a[68:69], v207 offset:21664\n"
"v_mfma_f32_16x16x32_bf16 v[24:27], a[12:15], a[176:179], v[24:27]\n"
"ds_read_b64_tr_b16 a[70:71], v207 offset:25248\n"
"ds_read_b64_tr_b16 a[72:73], v207 offset:7232\n"
"v_mfma_f32_16x16x32_bf16 v[28:31], a[12:15], a[180:183], v[28:31]\n"
"buffer_load_dwordx4 v114, s[40:43], s45 offen offset:2048 lds\n"
"ds_read_b64_tr_b16 a[74:75], v207 offset:10816\n"
"v_mfma_f32_16x16x32_bf16 v[32:35], a[16:19], a[176:179], v[32:35]\n"
"ds_read_b64_tr_b16 a[76:77], v207 offset:21696\n"
"v_mfma_f32_16x16x32_bf16 v[36:39], a[16:19], a[180:183], v[36:39]\n"
"ds_read_b64_tr_b16 a[78:79], v207 offset:25280\n"
"v_mfma_f32_16x16x32_bf16 v[40:43], a[20:23], a[176:179], v[40:43]\n"
"buffer_load_dwordx4 v115, s[40:43], s45 offen offset:3072 lds\n"
"ds_read_b64_tr_b16 a[80:81], v207 offset:7264\n"
"v_mfma_f32_16x16x32_bf16 v[44:47], a[20:23], a[180:183], v[44:47]\n"
"ds_read_b64_tr_b16 a[82:83], v207 offset:10848\n"
"v_mfma_f32_16x16x32_bf16 v[48:51], a[24:27], a[176:179], v[48:51]\n"
"s_add_u32 m0, s44, 0x21000\n"
"ds_read_b64_tr_b16 a[84:85], v207 offset:21728\n"
"ds_read_b64_tr_b16 a[86:87], v207 offset:25312\n"
"v_mfma_f32_16x16x32_bf16 v[52:55], a[24:27], a[180:183], v[52:55]\n"
"buffer_load_dwordx4 v112, s[40:43], s37 offen offset:0 lds\n"
"ds_read_b64_tr_b16 a[88:89], v207 offset:7296\n"
"v_mfma_f32_16x16x32_bf16 v[56:59], a[28:31], a[176:179], v[56:59]\n"
"ds_read_b64_tr_b16 a[90:91], v207 offset:10880\n"
"v_mfma_f32_16x16x32_bf16 v[60:63], a[28:31], a[180:183], v[60:63]\n"
"ds_read_b64_tr_b16 a[92:93], v207 offset:21760\n"
"v_mfma_f32_16x16x32_bf16 v[64:67], a[32:35], a[176:179], v[64:67]\n"
"buffer_load_dwordx4 v113, s[40:43], s37 offen offset:1024 lds\n"
"ds_read_b64_tr_b16 a[94:95], v207 offset:25344\n"
"v_mfma_f32_16x16x32_bf16 v[68:71], a[32:35], a[180:183], v[68:71]\n"
"ds_read_b64_tr_b16 a[96:97], v207 offset:7328\n"
"v_mfma_f32_16x16x32_bf16 v[72:75], a[36:39], a[176:179], v[72:75]\n"
"ds_read_b64_tr_b16 a[98:99], v207 offset:10912\n"
"ds_read_b64_tr_b16 a[100:101], v207 offset:21792\n"
"v_mfma_f32_16x16x32_bf16 v[76:79], a[36:39], a[180:183], v[76:79]\n"
"buffer_load_dwordx4 v114, s[40:43], s37 offen offset:2048 lds\n"
"ds_read_b64_tr_b16 a[102:103], v207 offset:25376\n"
"v_mfma_f32_16x16x32_bf16 v[80:83], a[40:43], a[176:179], v[80:83]\n"
"ds_read_b64_tr_b16 a[104:105], v207 offset:7360\n"
"v_mfma_f32_16x16x32_bf16 v[84:87], a[40:43], a[180:183], v[84:87]\n"
"ds_read_b64_tr_b16 a[106:107], v207 offset:10944\n"
"v_mfma_f32_16x16x32_bf16 v[88:91], a[44:47], a[176:179], v[88:91]\n"
"buffer_load_dwordx4 v115, s[40:43], s37 offen offset:3072 lds\n"
"ds_read_b64_tr_b16 a[108:109], v207 offset:21824\n"
"v_mfma_f32_16x16x32_bf16 v[92:95], a[44:47], a[180:183], v[92:95]\n"
"ds_read_b64_tr_b16 a[110:111], v207 offset:25408\n"
"v_mfma_f32_16x16x32_bf16 v[96:99], a[48:51], a[176:179], v[96:99]\n"
"v_mfma_f32_16x16x32_bf16 v[100:103], a[48:51], a[180:183], v[100:103]\n"
"s_add_u32 s45, s45, 0x10000\n"
"s_add_u32 s37, s37, 0x10000\n"
"v_mfma_f32_16x16x32_bf16 v[104:107], a[52:55], a[176:179], v[104:107]\n"
"v_mfma_f32_16x16x32_bf16 v[108:111], a[52:55], a[180:183], v[108:111]\n"
"s_waitcnt lgkmcnt(0)\n"
"v_mfma_f32_16x16x32_bf16 v[0:3], a[56:59], a[184:187], v[0:3]\n"
"s_waitcnt vmcnt(22)\n"
"s_barrier\n"
"ds_read_b64_tr_b16 a[0:1], v207 offset:32768\n"
"ds_read_b64_tr_b16 a[2:3], v207 offset:36352\n"
"v_mfma_f32_16x16x32_bf16 v[4:7], a[56:59], a[188:191], v[4:7]\n"
"ds_read_b64_tr_b16 a[4:5], v207 offset:47232\n"
"v_mfma_f32_16x16x32_bf16 v[8:11], a[60:63], a[184:187], v[8:11]\n"
"ds_read_b64_tr_b16 a[6:7], v207 offset:50816\n"
"v_mfma_f32_16x16x32_bf16 v[12:15], a[60:63], a[188:191], v[12:15]\n"
"ds_read_b64_tr_b16 a[8:9], v207 offset:32800\n"
"v_mfma_f32_16x16x32_bf16 v[16:19], a[64:67], a[184:187], v[16:19]\n"
"ds_read_b64_tr_b16 a[10:11], v207 offset:36384\n"
"v_mfma_f32_16x16x32_bf16 v[20:23], a[64:67], a[188:191], v[20:23]\n"
"ds_read_b64_tr_b16 a[12:13], v207 offset:47264\n"
"v_mfma_f32_16x16x32_bf16 v[24:27], a[68:71], a[184:187], v[24:27]\n"
"ds_read_b64_tr_b16 a[14:15], v207 offset:50848\n"
"ds_read_b64_tr_b16 a[16:17], v207 offset:32832\n"
"v_mfma_f32_16x16x32_bf16 v[28:31], a[68:71], a[188:191], v[28:31]\n"
"ds_read_b64_tr_b16 a[18:19], v207 offset:36416\n"
"v_mfma_f32_16x16x32_bf16 v[32:35], a[72:75], a[184:187], v[32:35]\n"
"ds_read_b64_tr_b16 a[20:21], v207 offset:47296\n"
"v_mfma_f32_16x16x32_bf16 v[36:39], a[72:75], a[188:191], v[36:39]\n"
"ds_read_b64_tr_b16 a[22:23], v207 offset:50880\n"
"v_mfma_f32_16x16x32_bf16 v[40:43], a[76:79], a[184:187], v[40:43]\n"
"ds_read_b64_tr_b16 a[24:25], v207 offset:32864\n"
"v_mfma_f32_16x16x32_bf16 v[44:47], a[76:79], a[188:191], v[44:47]\n"
"ds_read_b64_tr_b16 a[26:27], v207 offset:36448\n"
"v_mfma_f32_16x16x32_bf16 v[48:51], a[80:83], a[184:187], v[48:51]\n"
"ds_read_b64_tr_b16 a[28:29], v207 offset:47328\n"
"ds_read_b64_tr_b16 a[30:31], v207 offset:50912\n"
"v_mfma_f32_16x16x32_bf16 v[52:55], a[80:83], a[188:191], v[52:55]\n"
"ds_read_b64_tr_b16 a[32:33], v207 offset:32896\n"
"v_mfma_f32_16x16x32_bf16 v[56:59], a[84:87], a[184:187], v[56:59]\n"
"ds_read_b64_tr_b16 a[34:35], v207 offset:36480\n"
"v_mfma_f32_16x16x32_bf16 v[60:63], a[84:87], a[188:191], v[60:63]\n"
"ds_read_b64_tr_b16 a[36:37], v207 offset:47360\n"
"v_mfma_f32_16x16x32_bf16 v[64:67], a[88:91], a[184:187], v[64:67]\n"
"ds_read_b64_tr_b16 a[38:39], v207 offset:50944\n"
"v_mfma_f32_16x16x32_bf16 v[68:71], a[88:91], a[188:191], v[68:71]\n"
"ds_read_b64_tr_b16 a[40:41], v207 offset:32928\n"
"v_mfma_f32_16x16x32_bf16 v[72:75], a[92:95], a[184:187], v[72:75]\n"
"ds_read_b64_tr_b16 a[42:43], v207 offset:36512\n"
"ds_read_b64_tr_b16 a[44:45], v207 offset:47392\n"
"v_mfma_f32_16x16x32_bf16 v[76:79], a[92:95], a[188:191], v[76:79]\n"
"ds_read_b64_tr_b16 a[46:47], v207 offset:50976\n"
"v_mfma_f32_16x16x32_bf16 v[80:83], a[96:99], a[184:187], v[80:83]\n"
"ds_read_b64_tr_b16 a[48:49], v207 offset:32960\n"
"v_mfma_f32_16x16x32_bf16 v[84:87], a[96:99], a[188:191], v[84:87]\n"
"ds_read_b64_tr_b16 a[50:51], v207 offset:36544\n"
"v_mfma_f32_16x16x32_bf16 v[88:91], a[100:103], a[184:187], v[88:91]\n"
"ds_read_b64_tr_b16 a[52:53], v207 offset:47424\n"
"v_mfma_f32_16x16x32_bf16 v[92:95], a[100:103], a[188:191], v[92:95]\n"
"ds_read_b64_tr_b16 a[54:55], v207 offset:51008\n"
"v_mfma_f32_16x16x32_bf16 v[96:99], a[104:107], a[184:187], v[96:99]\n"
"v_mfma_f32_16x16x32_bf16 v[100:103], a[104:107], a[188:191], v[100:103]\n"
"v_mfma_f32_16x16x32_bf16 v[104:107], a[108:111], a[184:187], v[104:107]\n"
"v_mfma_f32_16x16x32_bf16 v[108:111], a[108:111], a[188:191], v[108:111]\n"
"s_waitcnt lgkmcnt(0)\n"
"v_mfma_f32_16x16x32_bf16 v[0:3], a[0:3], a[192:195], v[0:3]\n"
"s_add_u32 m0, s44, 0\n"
"ds_read_b64_tr_b16 a[56:57], v207 offset:39936\n"
"ds_read_b64_tr_b16 a[58:59], v207 offset:43520\n"
"v_mfma_f32_16x16x32_bf16 v[4:7], a[0:3], a[196:199], v[4:7]\n"
"buffer_load_dwordx4 v112, s[40:43], s45 offen offset:0 lds\n"
"ds_read_b64_tr_b16 a[60:61], v207 offset:54400\n"
"v_mfma_f32_16x16x32_bf16 v[8:11], a[4:7], a[192:195], v[8:11]\n"
"ds_read_b64_tr_b16 a[62:63], v207 offset:57984\n"
"v_mfma_f32_16x16x32_bf16 v[12:15], a[4:7], a[196:199], v[12:15]\n"
"ds_read_b64_tr_b16 a[64:65], v207 offset:39968\n"
"v_mfma_f32_16x16x32_bf16 v[16:19], a[8:11], a[192:195], v[16:19]\n"
"buffer_load_dwordx4 v113, s[40:43], s45 offen offset:1024 lds\n"
"ds_read_b64_tr_b16 a[66:67], v207 offset:43552\n"
"v_mfma_f32_16x16x32_bf16 v[20:23], a[8:11], a[196:199], v[20:23]\n"
"ds_read_b64_tr_b16 a[68:69], v207 offset:54432\n"
"v_mfma_f32_16x16x32_bf16 v[24:27], a[12:15], a[192:195], v[24:27]\n"
"ds_read_b64_tr_b16 a[70:71], v207 offset:58016\n"
"ds_read_b64_tr_b16 a[72:73], v207 offset:40000\n"
"v_mfma_f32_16x16x32_bf16 v[28:31], a[12:15], a[196:199], v[28:31]\n"
"buffer_load_dwordx4 v114, s[40:43], s45 offen offset:2048 lds\n"
"ds_read_b64_tr_b16 a[74:75], v207 offset:43584\n"
"v_mfma_f32_16x16x32_bf16 v[32:35], a[16:19], a[192:195], v[32:35]\n"
"ds_read_b64_tr_b16 a[76:77], v207 offset:54464\n"
"v_mfma_f32_16x16x32_bf16 v[36:39], a[16:19], a[196:199], v[36:39]\n"
"ds_read_b64_tr_b16 a[78:79], v207 offset:58048\n"
"v_mfma_f32_16x16x32_bf16 v[40:43], a[20:23], a[192:195], v[40:43]\n"
"buffer_load_dwordx4 v115, s[40:43], s45 offen offset:3072 lds\n"
"ds_read_b64_tr_b16 a[80:81], v207 offset:40032\n"
"v_mfma_f32_16x16x32_bf16 v[44:47], a[20:23], a[196:199], v[44:47]\n"
"ds_read_b64_tr_b16 a[82:83], v207 offset:43616\n"
"v_mfma_f32_16x16x32_bf16 v[48:51], a[24:27], a[192:195], v[48:51]\n"
"s_add_u32 m0, s44, 0x1000\n"
"ds_read_b64_tr_b16 a[84:85], v207 offset:54496\n"
"ds_read_b64_tr_b16 a[86:87], v207 offset:58080\n"
"v_mfma_f32_16x16x32_bf16 v[52:55], a[24:27], a[196:199], v[52:55]\n"
"buffer_load_dwordx4 v112, s[40:43], s37 offen offset:0 lds\n"
"ds_read_b64_tr_b16 a[88:89], v207 offset:40064\n"
"v_mfma_f32_16x16x32_bf16 v[56:59], a[28:31], a[192:195], v[56:59]\n"
"ds_read_b64_tr_b16 a[90:91], v207 offset:43648\n"
"v_mfma_f32_16x16x32_bf16 v[60:63], a[28:31], a[196:199], v[60:63]\n"
"ds_read_b64_tr_b16 a[92:93], v207 offset:54528\n"
"v_mfma_f32_16x16x32_bf16 v[64:67], a[32:35], a[192:195], v[64:67]\n"
"buffer_load_dwordx4 v113, s[40:43], s37 offen offset:1024 lds\n"
"ds_read_b64_tr_b16 a[94:95], v207 offset:58112\n"
"v_mfma_f32_16x16x32_bf16 v[68:71], a[32:35], a[196:199], v[68:71]\n"
"ds_read_b64_tr_b16 a[96:97], v207 offset:40096\n"
"v_mfma_f32_16x16x32_bf16 v[72:75], a[36:39], a[192:195], v[72:75]\n"
"ds_read_b64_tr_b16 a[98:99], v207 offset:43680\n"
"ds_read_b64_tr_b16 a[100:101], v207 offset:54560\n"
"v_mfma_f32_16x16x32_bf16 v[76:79], a[36:39], a[196:199], v[76:79]\n"
"buffer_load_dwordx4 v114, s[40:43], s37 offen offset:2048 lds\n"
"ds_read_b64_tr_b16 a[102:103], v207 offset:58144\n"
"v_mfma_f32_16x16x32_bf16 v[80:83], a[40:43], a[192:195], v[80:83]\n"
"ds_read_b64_tr_b16 a[104:105], v207 offset:40128\n"
"v_mfma_f32_16x16x32_bf16 v[84:87], a[40:43], a[196:199], v[84:87]\n"
"ds_read_b64_tr_b16 a[106:107], v207 offset:43712\n"
"v_mfma_f32_16x16x32_bf16 v[88:91], a[44:47], a[192:195], v[88:91]\n"
"buffer_load_dwordx4 v115, s[40:43], s37 offen offset:3072 lds\n"
"ds_read_b64_tr_b16 a[108:109], v207 offset:54592\n"
"v_mfma_f32_16x16x32_bf16 v[92:95], a[44:47], a[196:199], v[92:95]\n"
"ds_read_b64_tr_b16 a[110:111], v207 offset:58176\n"
"v_mfma_f32_16x16x32_bf16 v[96:99], a[48:51], a[192:195], v[96:99]\n"
"v_mfma_f32_16x16x32_bf16 v[100:103], a[48:51], a[196:199], v[100:103]\n"
"s_add_u32 s45, s45, 0x10000\n"
"s_add_u32 s37, s37, 0x10000\n"
"v_mfma_f32_16x16x32_bf16 v[104:107], a[52:55], a[192:195], v[104:107]\n"
"v_mfma_f32_16x16x32_bf16 v[108:111], a[52:55], a[196:199], v[108:111]\n"
"s_waitcnt lgkmcnt(0)\n"
"v_mfma_f32_16x16x32_bf16 v[0:3], a[56:59], a[200:203], v[0:3]\n"
"s_waitcnt vmcnt(23)\n"
"s_barrier\n"
"ds_read_b64_tr_b16 a[0:1], v208 offset:0\n"
"ds_read_b64_tr_b16 a[2:3], v208 offset:3584\n"
"v_mfma_f32_16x16x32_bf16 v[4:7], a[56:59], a[204:207], v[4:7]\n"
"ds_read_b64_tr_b16 a[4:5], v208 offset:14464\n"
"v_mfma_f32_16x16x32_bf16 v[8:11], a[60:63], a[200:203], v[8:11]\n"
"ds_read_b64_tr_b16 a[6:7], v208 offset:18048\n"
"v_mfma_f32_16x16x32_bf16 v[12:15], a[60:63], a[204:207], v[12:15]\n"
"ds_read_b64_tr_b16 a[8:9], v208 offset:32\n"
"v_mfma_f32_16x16x32_bf16 v[16:19], a[64:67], a[200:203], v[16:19]\n"
"ds_read_b64_tr_b16 a[10:11], v208 offset:3616\n"
"v_mfma_f32_16x16x32_bf16 v[20:23], a[64:67], a[204:207], v[20:23]\n"
"ds_read_b64_tr_b16 a[12:13], v208 offset:14496\n"
"v_mfma_f32_16x16x32_bf16 v[24:27], a[68:71], a[200:203], v[24:27]\n"
"ds_read_b64_tr_b16 a[14:15], v208 offset:18080\n"
"ds_read_b64_tr_b16 a[16:17], v208 offset:64\n"
"v_mfma_f32_16x16x32_bf16 v[28:31], a[68:71], a[204:207], v[28:31]\n"
"ds_read_b64_tr_b16 a[18:19], v208 offset:3648\n"
"v_mfma_f32_16x16x32_bf16 v[32:35], a[72:75], a[200:203], v[32:35]\n"
"ds_read_b64_tr_b16 a[20:21], v208 offset:14528\n"
"v_mfma_f32_16x16x32_bf16 v[36:39], a[72:75], a[204:207], v[36:39]\n"
"ds_read_b64_tr_b16 a[22:23], v208 offset:18112\n"
"v_mfma_f32_16x16x32_bf16 v[40:43], a[76:79], a[200:203], v[40:43]\n"
"ds_read_b64_tr_b16 a[24:25], v208 offset:96\n"
"v_mfma_f32_16x16x32_bf16 v[44:47], a[76:79], a[204:207], v[44:47]\n"
"ds_read_b64_tr_b16 a[26:27], v208 offset:3680\n"
"v_mfma_f32_16x16x32_bf16 v[48:51], a[80:83], a[200:203], v[48:51]\n"
"ds_read_b64_tr_b16 a[28:29], v208 offset:14560\n"
"ds_read_b64_tr_b16 a[30:31], v208 offset:18144\n"
"v_mfma_f32_16x16x32_bf16 v[52:55], a[80:83], a[204:207], v[52:55]\n"
"ds_read_b64_tr_b16 a[32:33], v208 offset:128\n"
"v_mfma_f32_16x16x32_bf16 v[56:59], a[84:87], a[200:203], v[56:59]\n"
"ds_read_b64_tr_b16 a[34:35], v208 offset:3712\n"
"v_mfma_f32_16x16x32_bf16 v[60:63], a[84:87], a[204:207], v[60:63]\n"
"ds_read_b64_tr_b16 a[36:37], v208 offset:14592\n"
"v_mfma_f32_16x16x32_bf16 v[64:67], a[88:91], a[200:203], v[64:67]\n"
"ds_read_b64_tr_b16 a[38:39], v208 offset:18176\n"
"v_mfma_f32_16x16x32_bf16 v[68:71], a[88:91], a[204:207], v[68:71]\n"
"ds_read_b64_tr_b16 a[40:41], v208 offset:160\n"
"v_mfma_f32_16x16x32_bf16 v[72:75], a[92:95], a[200:203], v[72:75]\n"
"ds_read_b64_tr_b16 a[42:43], v208 offset:3744\n"
"ds_read_b64_tr_b16 a[44:45], v208 offset:14624\n"
"v_mfma_f32_16x16x32_bf16 v[76:79], a[92:95], a[204:207], v[76:79]\n"
"ds_read_b64_tr_b16 a[46:47], v208 offset:18208\n"
"v_mfma_f32_16x16x32_bf16 v[80:83], a[96:99], a[200:203], v[80:83]\n"
"ds_read_b64_tr_b16 a[48:49], v208 offset:192\n"
"v_mfma_f32_16x16x32_bf16 v[84:87], a[96:99], a[204:207], v[84:87]\n"
"ds_read_b64_tr_b16 a[50:51], v208 offset:3776\n"
"v_mfma_f32_16x16x32_bf16 v[88:91], a[100:103], a[200:203], v[88:91]\n"
"ds_read_b64_tr_b16 a[52:53], v208 offset:14656\n"
"v_mfma_f32_16x16x32_bf16 v[92:95], a[100:103], a[204:207], v[92:95]\n"
"ds_read_b64_tr_b16 a[54:55], v208 offset:18240\n"
"v_mfma_f32_16x16x32_bf16 v[96:99], a[104:107], a[200:203], v[96:99]\n"
"v_mfma_f32_16x16x32_bf16 v[100:103], a[104:107], a[204:207], v[100:103]\n"
"v_mfma_f32_16x16x32_bf16 v[104:107], a[108:111], a[200:203], v[104:107]\n"
"v_mfma_f32_16x16x32_bf16 v[108:111], a[108:111], a[204:207], v[108:111]\n"
"s_waitcnt lgkmcnt(0)\n"
"v_mfma_f32_16x16x32_bf16 v[0:3], a[0:3], a[208:211], v[0:3]\n"
"s_add_u32 m0, s44, 0x8000\n"
"ds_read_b64_tr_b16 a[56:57], v208 offset:7168\n"
"ds_read_b64_tr_b16 a[58:59], v208 offset:10752\n"
"v_mfma_f32_16x16x32_bf16 v[4:7], a[0:3], a[212:215], v[4:7]\n"
"buffer_load_dwordx4 v112, s[40:43], s45 offen offset:0 lds\n"
"ds_read_b64_tr_b16 a[60:61], v208 offset:21632\n"
"v_mfma_f32_16x16x32_bf16 v[8:11], a[4:7], a[208:211], v[8:11]\n"
"ds_read_b64_tr_b16 a[62:63], v208 offset:25216\n"
"v_mfma_f32_16x16x32_bf16 v[12:15], a[4:7], a[212:215], v[12:15]\n"
"ds_read_b64_tr_b16 a[64:65], v208 offset:7200\n"
"v_mfma_f32_16x16x32_bf16 v[16:19], a[8:11], a[208:211], v[16:19]\n"
"buffer_load_dwordx4 v113, s[40:43], s45 offen offset:1024 lds\n"
"ds_read_b64_tr_b16 a[66:67], v208 offset:10784\n"
"v_mfma_f32_16x16x32_bf16 v[20:23], a[8:11], a[212:215], v[20:23]\n"
"ds_read_b64_tr_b16 a[68:69], v208 offset:21664\n"
"v_mfma_f32_16x16x32_bf16 v[24:27], a[12:15], a[208:211], v[24:27]\n"
"ds_read_b64_tr_b16 a[70:71], v208 offset:25248\n"
"ds_read_b64_tr_b16 a[72:73], v208 offset:7232\n"
"v_mfma_f32_16x16x32_bf16 v[28:31], a[12:15], a[212:215], v[28:31]\n"
"buffer_load_dwordx4 v114, s[40:43], s45 offen offset:2048 lds\n"
"ds_read_b64_tr_b16 a[74:75], v208 offset:10816\n"
"v_mfma_f32_16x16x32_bf16 v[32:35], a[16:19], a[208:211], v[32:35]\n"
"ds_read_b64_tr_b16 a[76:77], v208 offset:21696\n"
"v_mfma_f32_16x16x32_bf16 v[36:39], a[16:19], a[212:215], v[36:39]\n"
"ds_read_b64_tr_b16 a[78:79], v208 offset:25280\n"
"v_mfma_f32_16x16x32_bf16 v[40:43], a[20:23], a[208:211], v[40:43]\n"
"buffer_load_dwordx4 v115, s[40:43], s45 offen offset:3072 lds\n"
"ds_read_b64_tr_b16 a[80:81], v208 offset:7264\n"
"v_mfma_f32_16x16x32_bf16 v[44:47], a[20:23], a[212:215], v[44:47]\n"
"ds_read_b64_tr_b16 a[82:83], v208 offset:10848\n"
"v_mfma_f32_16x16x32_bf16 v[48:51], a[24:27], a[208:211], v[48:51]\n"
"s_add_u32 m0, s44, 0x9000\n"
"ds_read_b64_tr_b16 a[84:85], v208 offset:21728\n"
"ds_read_b64_tr_b16 a[86:87], v208 offset:25312\n"
"v_mfma_f32_16x16x32_bf16 v[52:55], a[24:27], a[212:215], v[52:55]\n"
"buffer_load_dwordx4 v112, s[40:43], s37 offen offset:0 lds\n"
"ds_read_b64_tr_b16 a[88:89], v208 offset:7296\n"
"v_mfma_f32_16x16x32_bf16 v[56:59], a[28:31], a[208:211], v[56:59]\n"
"ds_read_b64_tr_b16 a[90:91], v208 offset:10880\n"
"v_mfma_f32_16x16x32_bf16 v[60:63], a[28:31], a[212:215], v[60:63]\n"
"ds_read_b64_tr_b16 a[92:93], v208 offset:21760\n"
"v_mfma_f32_16x16x32_bf16 v[64:67], a[32:35], a[208:211], v[64:67]\n"
"buffer_load_dwordx4 v113, s[40:43], s37 offen offset:1024 lds\n"
"ds_read_b64_tr_b16 a[94:95], v208 offset:25344\n"
"v_mfma_f32_16x16x32_bf16 v[68:71], a[32:35], a[212:215], v[68:71]\n"
"ds_read_b64_tr_b16 a[96:97], v208 offset:7328\n"
"v_mfma_f32_16x16x32_bf16 v[72:75], a[36:39], a[208:211], v[72:75]\n"
"ds_read_b64_tr_b16 a[98:99], v208 offset:10912\n"
"ds_read_b64_tr_b16 a[100:101], v208 offset:21792\n"
"v_mfma_f32_16x16x32_bf16 v[76:79], a[36:39], a[212:215], v[76:79]\n"
"buffer_load_dwordx4 v114, s[40:43], s37 offen offset:2048 lds\n"
"ds_read_b64_tr_b16 a[102:103], v208 offset:25376\n"
"v_mfma_f32_16x16x32_bf16 v[80:83], a[40:43], a[208:211], v[80:83]\n"
"ds_read_b64_tr_b16 a[104:105], v208 offset:7360\n"
"v_mfma_f32_16x16x32_bf16 v[84:87], a[40:43], a[212:215], v[84:87]\n"
"ds_read_b64_tr_b16 a[106:107], v208 offset:10944\n"
"v_mfma_f32_16x16x32_bf16 v[88:91], a[44:47], a[208:211], v[88:91]\n"
"buffer_load_dwordx4 v115, s[40:43], s37 offen offset:3072 lds\n"
"ds_read_b64_tr_b16 a[108:109], v208 offset:21824\n"
"v_mfma_f32_16x16x32_bf16 v[92:95], a[44:47], a[212:215], v[92:95]\n"
"ds_read_b64_tr_b16 a[110:111], v208 offset:25408\n"
"v_mfma_f32_16x16x32_bf16 v[96:99], a[48:51], a[208:211], v[96:99]\n"
"v_mfma_f32_16x16x32_bf16 v[100:103], a[48:51], a[212:215], v[100:103]\n"
"s_add_u32 s45, s45, 0x10000\n"
"s_add_u32 s37, s37, 0x10000\n"
"v_mfma_f32_16x16x32_bf16 v[104:107], a[52:55], a[208:211], v[104:107]\n"
"v_mfma_f32_16x16x32_bf16 v[108:111], a[52:55], a[212:215], v[108:111]\n"
"s_waitcnt lgkmcnt(0)\n"
"v_mfma_f32_16x16x32_bf16 v[0:3], a[56:59], a[216:219], v[0:3]\n"
"s_waitcnt vmcnt(24)\n"
"s_barrier\n"
"ds_read_b64_tr_b16 a[0:1], v208 offset:32768\n"
"ds_read_b64_tr_b16 a[2:3], v208 offset:36352\n"
"v_mfma_f32_16x16x32_bf16 v[4:7], a[56:59], a[220:223], v[4:7]\n"
"ds_read_b64_tr_b16 a[4:5], v208 offset:47232\n"
"v_mfma_f32_16x16x32_bf16 v[8:11], a[60:63], a[216:219], v[8:11]\n"
"ds_read_b64_tr_b16 a[6:7], v208 offset:50816\n"
"v_mfma_f32_16x16x32_bf16 v[12:15], a[60:63], a[220:223], v[12:15]\n"
"ds_read_b64_tr_b16 a[8:9], v208 offset:32800\n"
"v_mfma_f32_16x16x32_bf16 v[16:19], a[64:67], a[216:219], v[16:19]\n"
"ds_read_b64_tr_b16 a[10:11], v208 offset:36384\n"
"v_mfma_f32_16x16x32_bf16 v[20:23], a[64:67], a[220:223], v[20:23]\n"
"ds_read_b64_tr_b16 a[12:13], v208 offset:47264\n"
"v_mfma_f32_16x16x32_bf16 v[24:27], a[68:71], a[216:219], v[24:27]\n"
"ds_read_b64_tr_b16 a[14:15], v208 offset:50848\n"
"ds_read_b64_tr_b16 a[16:17], v208 offset:32832\n"
"v_mfma_f32_16x16x32_bf16 v[28:31], a[68:71], a[220:223], v[28:31]\n"
"ds_read_b64_tr_b16 a[18:19], v208 offset:36416\n"
"v_mfma_f32_16x16x32_bf16 v[32:35], a[72:75], a[216:219], v[32:35]\n"
"ds_read_b64_tr_b16 a[20:21], v208 offset:47296\n"
"v_mfma_f32_16x16x32_bf16 v[36:39], a[72:75], a[220:223], v[36:39]\n"
"ds_read_b64_tr_b16 a[22:23], v208 offset:50880\n"
"v_mfma_f32_16x16x32_bf16 v[40:43], a[76:79], a[216:219], v[40:43]\n"
"ds_read_b64_tr_b16 a[24:25], v208 offset:32864\n"
"v_mfma_f32_16x16x32_bf16 v[44:47], a[76:79], a[220:223], v[44:47]\n"
"ds_read_b64_tr_b16 a[26:27], v208 offset:36448\n"
"v_mfma_f32_16x16x32_bf16 v[48:51], a[80:83], a[216:219], v[48:51]\n"
"ds_read_b64_tr_b16 a[28:29], v208 offset:47328\n"
"ds_read_b64_tr_b16 a[30:31], v208 offset:50912\n"
"v_mfma_f32_16x16x32_bf16 v[52:55], a[80:83], a[220:223], v[52:55]\n"
"ds_read_b64_tr_b16 a[32:33], v208 offset:32896\n"
"v_mfma_f32_16x16x32_bf16 v[56:59], a[84:87], a[216:219], v[56:59]\n"
"ds_read_b64_tr_b16 a[34:35], v208 offset:36480\n"
"v_mfma_f32_16x16x32_bf16 v[60:63], a[84:87], a[220:223], v[60:63]\n"
"ds_read_b64_tr_b16 a[36:37], v208 offset:47360\n"
"v_mfma_f32_16x16x32_bf16 v[64:67], a[88:91], a[216:219], v[64:67]\n"
"ds_read_b64_tr_b16 a[38:39], v208 offset:50944\n"
"v_mfma_f32_16x16x32_bf16 v[68:71], a[88:91], a[220:223], v[68:71]\n"
"ds_read_b64_tr_b16 a[40:41], v208 offset:32928\n"
"v_mfma_f32_16x16x32_bf16 v[72:75], a[92:95], a[216:219], v[72:75]\n"
"ds_read_b64_tr_b16 a[42:43], v208 offset:36512\n"
"ds_read_b64_tr_b16 a[44:45], v208 offset:47392\n"
"v_mfma_f32_16x16x32_bf16 v[76:79], a[92:95], a[220:223], v[76:79]\n"
"ds_read_b64_tr_b16 a[46:47], v208 offset:50976\n"
"v_mfma_f32_16x16x32_bf16 v[80:83], a[96:99], a[216:219], v[80:83]\n"
"ds_read_b64_tr_b16 a[48:49], v208 offset:32960\n"
"v_mfma_f32_16x16x32_bf16 v[84:87], a[96:99], a[220:223], v[84:87]\n"
"ds_read_b64_tr_b16 a[50:51], v208 offset:36544\n"
"v_mfma_f32_16x16x32_bf16 v[88:91], a[100:103], a[216:219], v[88:91]\n"
"ds_read_b64_tr_b16 a[52:53], v208 offset:47424\n"
"v_mfma_f32_16x16x32_bf16 v[92:95], a[100:103], a[220:223], v[92:95]\n"
"ds_read_b64_tr_b16 a[54:55], v208 offset:51008\n"
"v_mfma_f32_16x16x32_bf16 v[96:99], a[104:107], a[216:219], v[96:99]\n"
"v_mfma_f32_16x16x32_bf16 v[100:103], a[104:107], a[220:223], v[100:103]\n"
"v_mfma_f32_16x16x32_bf16 v[104:107], a[108:111], a[216:219], v[104:107]\n"
"v_mfma_f32_16x16x32_bf16 v[108:111], a[108:111], a[220:223], v[108:111]\n"
"s_waitcnt lgkmcnt(0)\n"
"v_mfma_f32_16x16x32_bf16 v[0:3], a[0:3], a[224:227], v[0:3]\n"
"s_add_u32 m0, s44, 0x10000\n"
"ds_read_b64_tr_b16 a[56:57], v208 offset:39936\n"
"ds_read_b64_tr_b16 a[58:59], v208 offset:43520\n"
"v_mfma_f32_16x16x32_bf16 v[4:7], a[0:3], a[228:231], v[4:7]\n"
"buffer_load_dwordx4 v112, s[40:43], s45 offen offset:0 lds\n"
"ds_read_b64_tr_b16 a[60:61], v208 offset:54400\n"
"v_mfma_f32_16x16x32_bf16 v[8:11], a[4:7], a[224:227], v[8:11]\n"
"ds_read_b64_tr_b16 a[62:63], v208 offset:57984\n"
"v_mfma_f32_16x16x32_bf16 v[12:15], a[4:7], a[228:231], v[12:15]\n"
"ds_read_b64_tr_b16 a[64:65], v208 offset:39968\n"
"v_mfma_f32_16x16x32_bf16 v[16:19], a[8:11], a[224:227], v[16:19]\n"
"buffer_load_dwordx4 v113, s[40:43], s45 offen offset:1024 lds\n"
"ds_read_b64_tr_b16 a[66:67], v208 offset:43552\n"
"v_mfma_f32_16x16x32_bf16 v[20:23], a[8:11], a[228:231], v[20:23]\n"
"ds_read_b64_tr_b16 a[68:69], v208 offset:54432\n"
"v_mfma_f32_16x16x32_bf16 v[24:27], a[12:15], a[224:227], v[24:27]\n"
"ds_read_b64_tr_b16 a[70:71], v208 offset:58016\n"
"ds_read_b64_tr_b16 a[72:73], v208 offset:40000\n"
"v_mfma_f32_16x16x32_bf16 v[28:31], a[12:15], a[228:231], v[28:31]\n"
"buffer_load_dwordx4 v114, s[40:43], s45 offen offset:2048 lds\n"
"ds_read_b64_tr_b16 a[74:75], v208 offset:43584\n"
"v_mfma_f32_16x16x32_bf16 v[32:35], a[16:19], a[224:227], v[32:35]\n"
"ds_read_b64_tr_b16 a[76:77], v208 offset:54464\n"
"v_mfma_f32_16x16x32_bf16 v[36:39], a[16:19], a[228:231], v[36:39]\n"
"ds_read_b64_tr_b16 a[78:79], v208 offset:58048\n"
"v_mfma_f32_16x16x32_bf16 v[40:43], a[20:23], a[224:227], v[40:43]\n"
"buffer_load_dwordx4 v115, s[40:43], s45 offen offset:3072 lds\n"
"ds_read_b64_tr_b16 a[80:81], v208 offset:40032\n"
"v_mfma_f32_16x16x32_bf16 v[44:47], a[20:23], a[228:231], v[44:47]\n"
"ds_read_b64_tr_b16 a[82:83], v208 offset:43616\n"
"v_mfma_f32_16x16x32_bf16 v[48:51], a[24:27], a[224:227], v[48:51]\n"
"s_add_u32 m0, s44, 0x11000\n"
"ds_read_b64_tr_b16 a[84:85], v208 offset:54496\n"
"ds_read_b64_tr_b16 a[86:87], v208 offset:58080\n"
"v_mfma_f32_16x16x32_bf16 v[52:55], a[24:27], a[228:231], v[52:55]\n"
"buffer_load_dwordx4 v112, s[40:43], s37 offen offset:0 lds\n"
"ds_read_b64_tr_b16 a[88:89], v208 offset:40064\n"
"v_mfma_f32_16x16x32_bf16 v[56:59], a[28:31], a[224:227], v[56:59]\n"
"ds_read_b64_tr_b16 a[90:91], v208 offset:43648\n"
"v_mfma_f32_16x16x32_bf16 v[60:63], a[28:31], a[228:231], v[60:63]\n"
"ds_read_b64_tr_b16 a[92:93], v208 offset:54528\n"
"v_mfma_f32_16x16x32_bf16 v[64:67], a[32:35], a[224:227], v[64:67]\n"
"buffer_load_dwordx4 v113, s[40:43], s37 offen offset:1024 lds\n"
"ds_read_b64_tr_b16 a[94:95], v208 offset:58112\n"
"v_mfma_f32_16x16x32_bf16 v[68:71], a[32:35], a[228:231], v[68:71]\n"
"ds_read_b64_tr_b16 a[96:97], v208 offset:40096\n"
"v_mfma_f32_16x16x32_bf16 v[72:75], a[36:39], a[224:227], v[72:75]\n"
"ds_read_b64_tr_b16 a[98:99], v208 offset:43680\n"
"ds_read_b64_tr_b16 a[100:101], v208 offset:54560\n"
"v_mfma_f32_16x16x32_bf16 v[76:79], a[36:39], a[228:231], v[76:79]\n"
"buffer_load_dwordx4 v114, s[40:43], s37 offen offset:2048 lds\n"
"ds_read_b64_tr_b16 a[102:103], v208 offset:58144\n"
"v_mfma_f32_16x16x32_bf16 v[80:83], a[40:43], a[224:227], v[80:83]\n"
"ds_read_b64_tr_b16 a[104:105], v208 offset:40128\n"
"v_mfma_f32_16x16x32_bf16 v[84:87], a[40:43], a[228:231], v[84:87]\n"
"ds_read_b64_tr_b16 a[106:107], v208 offset:43712\n"
"v_mfma_f32_16x16x32_bf16 v[88:91], a[44:47], a[224:227], v[88:91]\n"
"buffer_load_dwordx4 v115, s[40:43], s37 offen offset:3072 lds\n"
"ds_read_b64_tr_b16 a[108:109], v208 offset:54592\n"
"v_mfma_f32_16x16x32_bf16 v[92:95], a[44:47], a[228:231], v[92:95]\n"
"ds_read_b64_tr_b16 a[110:111], v208 offset:58176\n"
"v_mfma_f32_16x16x32_bf16 v[96:99], a[48:51], a[224:227], v[96:99]\n"
"v_mfma_f32_16x16x32_bf16 v[100:103], a[48:51], a[228:231], v[100:103]\n"
"s_add_u32 s45, s45, 0x10000\n"
"s_add_u32 s37, s37, 0x10000\n"
"v_mfma_f32_16x16x32_bf16 v[104:107], a[52:55], a[224:227], v[104:107]\n"
"v_mfma_f32_16x16x32_bf16 v[108:111], a[52:55], a[228:231], v[108:111]\n"
"s_waitcnt lgkmcnt(0)\n"
"v_mfma_f32_16x16x32_bf16 v[0:3], a[56:59], a[232:235], v[0:3]\n"
"v_mfma_f32_16x16x32_bf16 v[4:7], a[56:59], a[236:239], v[4:7]\n"
"v_mfma_f32_16x16x32_bf16 v[8:11], a[60:63], a[232:235], v[8:11]\n"
"v_mfma_f32_16x16x32_bf16 v[12:15], a[60:63], a[236:239], v[12:15]\n"
"v_mfma_f32_16x16x32_bf16 v[16:19], a[64:67], a[232:235], v[16:19]\n"
"v_mfma_f32_16x16x32_bf16 v[20:23], a[64:67], a[236:239], v[20:23]\n"
"v_mfma_f32_16x16x32_bf16 v[24:27], a[68:71], a[232:235], v[24:27]\n"
"v_mfma_f32_16x16x32_bf16 v[28:31], a[68:71], a[236:239], v[28:31]\n"
"v_mfma_f32_16x16x32_bf16 v[32:35], a[72:75], a[232:235], v[32:35]\n"
"v_mfma_f32_16x16x32_bf16 v[36:39], a[72:75], a[236:239], v[36:39]\n"
"v_mfma_f32_16x16x32_bf16 v[40:43], a[76:79], a[232:235], v[40:43]\n"
"v_mfma_f32_16x16x32_bf16 v[44:47], a[76:79], a[236:239], v[44:47]\n"
"v_mfma_f32_16x16x32_bf16 v[48:51], a[80:83], a[232:235], v[48:51]\n"
"v_mfma_f32_16x16x32_bf16 v[52:55], a[80:83], a[236:239], v[52:55]\n"
"v_mfma_f32_16x16x32_bf16 v[56:59], a[84:87], a[232:235], v[56:59]\n"
"v_mfma_f32_16x16x32_bf16 v[60:63], a[84:87], a[236:239], v[60:63]\n"
"v_mfma_f32_16x16x32_bf16 v[64:67], a[88:91], a[232:235], v[64:67]\n"
"v_mfma_f32_16x16x32_bf16 v[68:71], a[88:91], a[236:239], v[68:71]\n"
"v_mfma_f32_16x16x32_bf16 v[72:75], a[92:95], a[232:235], v[72:75]\n"
"v_mfma_f32_16x16x32_bf16 v[76:79], a[92:95], a[236:239], v[76:79]\n"
"v_mfma_f32_16x16x32_bf16 v[80:83], a[96:99], a[232:235], v[80:83]\n"
"v_mfma_f32_16x16x32_bf16 v[84:87], a[96:99], a[236:239], v[84:87]\n"
"v_mfma_f32_16x16x32_bf16 v[88:91], a[100:103], a[232:235], v[88:91]\n"
"v_mfma_f32_16x16x32_bf16 v[92:95], a[100:103], a[236:239], v[92:95]\n"
"v_mfma_f32_16x16x32_bf16 v[96:99], a[104:107], a[232:235], v[96:99]\n"
"v_mfma_f32_16x16x32_bf16 v[100:103], a[104:107], a[236:239], v[100:103]\n"
"v_mfma_f32_16x16x32_bf16 v[104:107], a[108:111], a[232:235], v[104:107]\n"
"v_mfma_f32_16x16x32_bf16 v[108:111], a[108:111], a[236:239], v[108:111]\n"
"s_barrier\n"
"s_nop 15\n"
    : "={v[0:15]}"(a0), "={v[16:31]}"(a1), "={v[32:47]}"(a2), "={v[48:63]}"(a3), "={v[64:79]}"(a4), "={v[80:95]}"(a5), "={v[96:111]}"(a6)
    : "{s[4:5]}"(h2), "{s[6:7]}"(wd), "{s[8:9]}"(wg), "{s[10:11]}"(wu), "{s[12:13]}"(c), "{s16}"(bid), "{s17}"(w), "{s18}"(ldsb)
    : "v112","v113","v114","v115","v116","v117","v118","v119","v192","v193","v194","v195","v196","v197","v198","v199","v200","v201","v202","v203","v204","v205","v206","v207",
"v208","v209","v210","v211","v212","v213","v214","v215","a0","a1","a2","a3","a4","a5","a6","a7","a8","a9","a10","a11","a12","a13","a14","a15",
"a16","a17","a18","a19","a20","a21","a22","a23","a24","a25","a26","a27","a28","a29","a30","a31","a32","a33","a34","a35","a36","a37","a38","a39",
"a40","a41","a42","a43","a44","a45","a46","a47","a48","a49","a50","a51","a52","a53","a54","a55","a56","a57","a58","a59","a60","a61","a62","a63",
"a64","a65","a66","a67","a68","a69","a70","a71","a72","a73","a74","a75","a76","a77","a78","a79","a80","a81","a82","a83","a84","a85","a86","a87",
"a88","a89","a90","a91","a92","a93","a94","a95","a96","a97","a98","a99","a100","a101","a102","a103","a104","a105","a106","a107","a108","a109","a110","a111",
"a112","a113","a114","a115","a116","a117","a118","a119","a120","a121","a122","a123","a124","a125","a126","a127","a128","a129","a130","a131","a132","a133","a134","a135",
"a136","a137","a138","a139","a140","a141","a142","a143","a144","a145","a146","a147","a148","a149","a150","a151","a152","a153","a154","a155","a156","a157","a158","a159",
"a160","a161","a162","a163","a164","a165","a166","a167","a168","a169","a170","a171","a172","a173","a174","a175","a176","a177","a178","a179","a180","a181","a182","a183",
"a184","a185","a186","a187","a188","a189","a190","a191","a192","a193","a194","a195","a196","a197","a198","a199","a200","a201","a202","a203","a204","a205","a206","a207",
"a208","a209","a210","a211","a212","a213","a214","a215","a216","a217","a218","a219","a220","a221","a222","a223","a224","a225","a226","a227","a228","a229","a230","a231",
"a232","a233","a234","a235","a236","a237","a238","a239","s19","s20","s21","s22","s23","s24","s25","s26","s27","s28","s29","s30","s31","s34","s35","s36",
"s37","s38","s39","s40","s41","s42","s43","s44","s45","s46","s47","s48","s49","s50","s51","vcc","scc","memory");
    const unsigned lane = lane_now();
    const rsrc_t r = mk_rsrc(ws + WS_ACT, 7340032);
    const unsigned off = (((rt*8+2*w)*56 + ((ct*7)>>1))*64 + lane)*16;
    const unsigned ct5 = (bid>>3)&7;
    const rsrc_t rd = mk_rsrc((const char*)wd + w*(7*65536) + ct5*128, 7*65536 - ct5*128);
    const unsigned v0 = ((lane>>2)&7)*1024 + (lane>>5)*64 + (((lane&3) ^ ((lane>>3)&2))*16), m0b = ldsb + 3*32768 + w*8192;
    down_stage(m0b, v0, rd, 4);
    if(ct&1){
      geglu_single(a0,r,off+8);
      geglu_pair(a1,a2,r,off+1024); geglu_pair(a3,a4,r,off+2048); geglu_pair(a5,a6,r,off+3072);
    } else {
      geglu_pair(a0,a1,r,off);
      geglu_pair(a2,a3,r,off+1024); geglu_pair(a4,a5,r,off+2048); geglu_single(a6,r,off+3072);
    }
    publish(cnt + 32*(64 + rt), w==0 && lane==0);
  }
  {
    const unsigned rt = (bid&7)*4 + (bid>>6), ct = (bid>>3)&7;
    const void* act = ws + WS_ACT; const void* x1p = ws + WS_X1; const unsigned* c = cnt + 32*(64 + (rt>>1));
    const unsigned lane = lane_now(), tid = w*64 + lane;
    const unsigned c8 = (tid&7)*8, n = ct*64 + c8;
    const unsigned xo0 = (unsigned)frag_off(rt*64 + (tid>>3), n, 512)*2, xo1 = (unsigned)frag_off(rt*64 + (tid>>3) + 32, n, 512)*2;
    f32x4 gv[2];
    gv[0] = *(const f32x4*)(tabs + 2560 + n); gv[1] = *(const f32x4*)(tabs + 2560 + n + 4);
    f32x16 a00, a01, a10, a11; u32x4 xr0, xr1;
    asm volatile(
"v_mbcnt_lo_u32_b32 v203, -1, 0\n"
"v_mbcnt_hi_u32_b32 v203, -1, v203\n"
"v_bfe_u32 v200, v203, 4, 1\n"
"v_lshlrev_b32 v200, 1, v200\n"
"v_and_b32 v201, 3, v203\n"
"v_xor_b32 v200, v200, v201\n"
"v_lshlrev_b32 v200, 4, v200\n"
"v_lshrrev_b32 v201, 5, v203\n"
"v_lshl_add_u32 v200, v201, 6, v200\n"
"v_bfe_u32 v201, v203, 2, 3\n"
"v_lshl_add_u32 v192, v201, 10, v200\n"
"v_add_u32 v193, 0x1c00, v192\n"
"v_add_u32 v194, 0x3800, v192\n"
"v_add_u32 v195, 0x5400, v192\n"
"s_mul_i32 s35, s17, 0x70000\n"
"s_bfe_u32 s36, s16, 0x30003\n"
"s_lshl_b32 s36, s36, 7\n"
"s_add_u32 s35, s35, s36\n"
"s_sub_u32 s36, 0x70000, s36\n"
"s_add_u32 s24, s6, s35\n"
"s_addc_u32 s25, s7, 0\n"
"s_and_b32 s25, s25, 0xffff\n"
"s_mov_b32 s26, s36\n"
"s_mov_b32 s27, 0x20000\n"
"s_mov_b32 s30, 0x50000\n"
"s_mov_b32 s31, 0x58000\n"
"s_lshl_b32 s35, s17, 13\n"
"s_add_u32 s29, s18, s35\n"
"v_lshlrev_b32 v198, 4, v203\n"
"v_add_u32 v199, 0xe000, v198\n"
"v_add_u32 v205, 0x1c000, v198\n"
"v_add_u32 v206, 0x2a000, v198\n"
"s_and_b32 s35, s16, 7\n"
"s_lshl_b32 s35, s35, 2\n"
"s_lshr_b32 s36, s16, 6\n"
"s_add_u32 s35, s35, s36\n"
"s_mul_i32 s35, s35, 0x38000\n"
"s_mul_i32 s36, s17, 0x3800\n"
"s_add_u32 s35, s35, s36\n"
"s_add_u32 s20, s4, s35\n"
"s_addc_u32 s21, s5, 0\n"
"s_and_b32 s21, s21, 0xffff\n"
"s_mov_b32 s22, 0x2d800\n"
"s_mov_b32 s23, 0x20000\n"
"s_mov_b32 s34, 0\n"
"v_lshrrev_b32 v200, 5, v203\n"
"v_lshlrev_b32 v200, 10, v200\n"
"v_bfe_u32 v201, v203, 2, 3\n"
"v_lshl_add_u32 v200, v201, 6, v200\n"
"v_and_b32 v201, 3, v203\n"
"v_lshl_add_u32 v200, v201, 3, v200\n"
"v_bfe_u32 v201, v203, 4, 1\n"
"v_lshl_add_u32 v200, v201, 5, v200\n"
"v_add_u32 v196, s29, v200\n"
"v_xor_b32 v197, 32, v196\n"
"v_add_u32 v218, 0x10000, v196\n"
"v_add_u32 v219, 0x10000, v197\n"
"v_add_u32 v220, 0x20000, v196\n"
"v_add_u32 v221, 0x20000, v197\n"
"ds_read_b64_tr_b16 a[0:1], v220 offset:0\n"
"ds_read_b64_tr_b16 a[2:3], v220 offset:2048\n"
"ds_read_b64_tr_b16 a[4:5], v221 offset:0\n"
"ds_read_b64_tr_b16 a[6:7], v221 offset:2048\n"
"ds_read_b64_tr_b16 a[8:9], v220 offset:512\n"
"ds_read_b64_tr_b16 a[10:11], v220 offset:2560\n"
"ds_read_b64_tr_b16 a[12:13], v221 offset:512\n"
"ds_read_b64_tr_b16 a[14:15], v221 offset:2560\n"
"ds_read_b64_tr_b16 a[16:17], v220 offset:4096\n"
"ds_read_b64_tr_b16 a[18:19], v220 offset:6144\n"
"ds_read_b64_tr_b16 a[20:21], v221 offset:4096\n"
"ds_read_b64_tr_b16 a[22:23], v221 offset:6144\n"
"ds_read_b64_tr_b16 a[24:25], v220 offset:4608\n"
"ds_read_b64_tr_b16 a[26:27], v220 offset:6656\n"
"ds_read_b64_tr_b16 a[28:29], v221 offset:4608\n"
"ds_read_b64_tr_b16 a[30:31], v221 offset:6656\n"
"ds_read_b64_tr_b16 a[32:33], v196 offset:0\n"
"ds_read_b64_tr_b16 a[34:35], v196 offset:2048\n"
"ds_read_b64_tr_b16 a[36:37], v197 offset:0\n"
"ds_read_b64_tr_b16 a[38:39], v197 offset:2048\n"
"ds_read_b64_tr_b16 a[40:41], v196 offset:512\n"
"ds_read_b64_tr_b16 a[42:43], v196 offset:2560\n"
"ds_read_b64_tr_b16 a[44:45], v197 offset:512\n"
"ds_read_b64_tr_b16 a[46:47], v197 offset:2560\n"
"ds_read_b64_tr_b16 a[48:49], v196 offset:4096\n"
"ds_read_b64_tr_b16 a[50:51], v196 offset:6144\n"
"ds_read_b64_tr_b16 a[52:53], v197 offset:4096\n"
"ds_read_b64_tr_b16 a[54:55], v197 offset:6144\n"
"ds_read_b64_tr_b16 a[56:57], v196 offset:4608\n"
"ds_read_b64_tr_b16 a[58:59], v196 offset:6656\n"
"ds_read_b64_tr_b16 a[60:61], v197 offset:4608\n"
"ds_read_b64_tr_b16 a[62:63], v197 offset:6656\n"
"s_waitcnt lgkmcnt(15)\n"
"s_add_u32 m0, s29, 0x20000\n"
"s_nop 0\n"
"buffer_load_dwordx4 v192, s[24:27], s30 offen offset:0 lds\n"
"buffer_load_dwordx4 v193, s[24:27], s30 offen offset:1024 lds\n"
"buffer_load_dwordx4 v194, s[24:27], s30 offen offset:2048 lds\n"
"buffer_load_dwordx4 v195, s[24:27], s30 offen offset:3072 lds\n"
"s_add_u32 m0, s29, 0x21000\n"
"s_nop 0\n"
"buffer_load_dwordx4 v192, s[24:27], s31 offen offset:0 lds\n"
"buffer_load_dwordx4 v193, s[24:27], s31 offen offset:1024 lds\n"
"buffer_load_dwordx4 v194, s[24:27], s31 offen offset:2048 lds\n"
"buffer_load_dwordx4 v195, s[24:27], s31 offen offset:3072 lds\n"
"s_add_u32 s30, s30, 0x10000\n"
"s_add_u32 s31, s31, 0x10000\n"
"s_waitcnt lgkmcnt(0)\n"
"s_add_u32 m0, s29, 0\n"
"s_nop 0\n"
"buffer_load_dwordx4 v192, s[24:27], s30 offen offset:0 lds\n"
"buffer_load_dwordx4 v193, s[24:27], s30 offen offset:1024 lds\n"
"buffer_load_dwordx4 v194, s[24:27], s30 offen offset:2048 lds\n"
"buffer_load_dwordx4 v195, s[24:27], s30 offen offset:3072 lds\n"
"s_add_u32 m0, s29, 0x1000\n"
"s_nop 0\n"
"buffer_load_dwordx4 v192, s[24:27], s31 offen offset:0 lds\n"
"buffer_load_dwordx4 v193, s[24:27], s31 offen offset:1024 lds\n"
"buffer_load_dwordx4 v194, s[24:27], s31 offen offset:2048 lds\n"
"buffer_load_dwordx4 v195, s[24:27], s31 offen offset:3072 lds\n"
"s_add_u32 s30, s30, 0x10000\n"
"s_add_u32 s31, s31, 0x10000\n"
"s_cmp_lg_u32 s17, 0\n"
"s_cbranch_scc1 L_dn_done_%=\n"
"v_mov_b32 v207, 0\n"
"s_mov_b32 s36, 0x61a80\n"
"L_dn_poll_%=:\n"
"global_load_dword v204, v207, s[12:13] sc1\n"
"s_waitcnt vmcnt(0)\n"
"v_readfirstlane_b32 s35, v204\n"
"s_cmp_ge_u32 s35, 16\n"
"s_cbranch_scc1 L_dn_done_%=\n"
"s_sub_u32 s36, s36, 1\n"
"s_cmp_eq_u32 s36, 0\n"
"s_cbranch_scc1 L_dn_done_%=\n"
"s_sleep 1\n"
"s_branch L_dn_poll_%=\n"
"L_dn_done_%=:\n"
"s_barrier\n"
"buffer_load_dwordx4 v[64:67], v198, s[20:23], s34 offen offset:0 sc1\n"
"buffer_load_dwordx4 v[80:83], v198, s[20:23], s34 offen offset:1024 sc1\n"
"buffer_load_dwordx4 v[68:71], v199, s[20:23], s34 offen offset:0 sc1\n"
"buffer_load_dwordx4 v[84:87], v199, s[20:23], s34 offen offset:1024 sc1\n"
"buffer_load_dwordx4 v[72:75], v205, s[20:23], s34 offen offset:0 sc1\n"
"buffer_load_dwordx4 v[88:91], v205, s[20:23], s34 offen offset:1024 sc1\n"
"buffer_load_dwordx4 v[76:79], v206, s[20:23], s34 offen offset:0 sc1\n"
"buffer_load_dwordx4 v[92:95], v206, s[20:23], s34 offen offset:1024 sc1\n"
"s_add_u32 s34, s34, 0x800\n"
"buffer_load_dwordx4 v[96:99], v198, s[20:23], s34 offen offset:0 sc1\n"
"buffer_load_dwordx4 v[112:115], v198, s[20:23], s34 offen offset:1024 sc1\n"
"buffer_load_dwordx4 v[100:103], v199, s[20:23], s34 offen offset:0 sc1\n"
"buffer_load_dwordx4 v[116:119], v199, s[20:23], s34 offen offset:1024 sc1\n"
"buffer_load_dwordx4 v[104:107], v205, s[20:23], s34 offen offset:0 sc1\n"
"buffer_load_dwordx4 v[120:123], v205, s[20:23], s34 offen offset:1024 sc1\n"
"buffer_load_dwordx4 v[108:111], v206, s[20:23], s34 offen offset:0 sc1\n"
"buffer_load_dwordx4 v[124:127], v206, s[20:23], s34 offen offset:1024 sc1\n"
"s_add_u32 s34, s34, 0x800\n"
"s_waitcnt vmcnt(12)\n"
"v_mfma_f32_16x16x32_bf16 v[0:3], a[0:3], v[64:67], 0\n"
"buffer_load_dwordx4 v[128:131], v198, s[20:23], s34 offen offset:0 sc1\n"
"v_mfma_f32_16x16x32_bf16 v[0:3], a[16:19], v[80:83], v[0:3]\n"
"v_mfma_f32_16x16x32_bf16 v[4:7], a[4:7], v[64:67], 0\n"
"buffer_load_dwordx4 v[144:147], v198, s[20:23], s34 offen offset:1024 sc1\n"
"v_mfma_f32_16x16x32_bf16 v[4:7], a[20:23], v[80:83], v[4:7]\n"
"v_mfma_f32_16x16x32_bf16 v[8:11], a[8:11], v[64:67], 0\n"
"buffer_load_dwordx4 v[132:135], v199, s[20:23], s34 offen offset:0 sc1\n"
"v_mfma_f32_16x16x32_bf16 v[8:11], a[24:27], v[80:83], v[8:11]\n"
"v_mfma_f32_16x16x32_bf16 v[12:15], a[12:15], v[64:67], 0\n"
"buffer_load_dwordx4 v[148:151], v199, s[20:23], s34 offen offset:1024 sc1\n"
"v_mfma_f32_16x16x32_bf16 v[12:15], a[28:31], v[80:83], v[12:15]\n"
"v_mfma_f32_16x16x32_bf16 v[16:19], a[0:3], v[68:71], 0\n"
"buffer_load_dwordx4 v[136:139], v205, s[20:23], s34 offen offset:0 sc1\n"
"v_mfma_f32_16x16x32_bf16 v[16:19], a[16:19], v[84:87], v[16:19]\n"
"v_mfma_f32_16x16x32_bf16 v[20:23], a[4:7], v[68:71], 0\n"
"buffer_load_dwordx4 v[152:155], v205, s[20:23], s34 offen offset:1024 sc1\n"
"v_mfma_f32_16x16x32_bf16 v[20:23], a[20:23], v[84:87], v[20:23]\n"
"v_mfma_f32_16x16x32_bf16 v[24:27], a[8:11], v[68:71], 0\n"
"buffer_load_dwordx4 v[140:143], v206, s[20:23], s34 offen offset:0 sc1\n"
"v_mfma_f32_16x16x32_bf16 v[24:27], a[24:27], v[84:87], v[24:27]\n"
"v_mfma_f32_16x16x32_bf16 v[28:31], a[12:15], v[68:71], 0\n"
"buffer_load_dwordx4 v[156:159], v206, s[20:23], s34 offen offset:1024 sc1\n"
"v_mfma_f32_16x16x32_bf16 v[28:31], a[28:31], v[84:87], v[28:31]\n"
"s_add_u32 s34, s34, 0x800\n"
"s_waitcnt vmcnt(16)\n"
"v_mfma_f32_16x16x32_bf16 v[32:35], a[0:3], v[72:75], 0\n"
"buffer_load_dwordx4 v[160:163], v198, s[20:23], s34 offen offset:0 sc1\n"
"v_mfma_f32_16x16x32_bf16 v[32:35], a[16:19], v[88:91], v[32:35]\n"
"v_mfma_f32_16x16x32_bf16 v[36:39], a[4:7], v[72:75], 0\n"
"buffer_load_dwordx4 v[176:179], v198, s[20:23], s34 offen offset:1024 sc1\n"
"v_mfma_f32_16x16x32_bf16 v[36:39], a[20:23], v[88:91], v[36:39]\n"
"v_mfma_f32_16x16x32_bf16 v[40:43], a[8:11], v[72:75], 0\n"
"buffer_load_dwordx4 v[164:167], v199, s[20:23], s34 offen offset:0 sc1\n"
"v_mfma_f32_16x16x32_bf16 v[40:43], a[24:27], v[88:91], v[40:43]\n"
"v_mfma_f32_16x16x32_bf16 v[44:47], a[12:15], v[72:75], 0\n"
"buffer_load_dwordx4 v[180:183], v199, s[20:23], s34 offen offset:1024 sc1\n"
"v_mfma_f32_16x16x32_bf16 v[44:47], a[28:31], v[88:91], v[44:47]\n"
"v_mfma_f32_16x16x32_bf16 v[48:51], a[0:3], v[76:79], 0\n"
"buffer_load_dwordx4 v[168:171], v205, s[20:23], s34 offen offset:0 sc1\n"
"v_mfma_f32_16x16x32_bf16 v[48:51], a[16:19], v[92:95], v[48:51]\n"
"v_mfma_f32_16x16x32_bf16 v[52:55], a[4:7], v[76:79], 0\n"
"buffer_load_dwordx4 v[184:187], v205, s[20:23], s34 offen offset:1024 sc1\n"
"v_mfma_f32_16x16x32_bf16 v[52:55], a[20:23], v[92:95], v[52:55]\n"
"v_mfma_f32_16x16x32_bf16 v[56:59], a[8:11], v[76:79], 0\n"
"buffer_load_dwordx4 v[172:175], v206, s[20:23], s34 offen offset:0 sc1\n"
"v_mfma_f32_16x16x32_bf16 v[56:59], a[24:27], v[92:95], v[56:59]\n"
"v_mfma_f32_16x16x32_bf16 v[60:63], a[12:15], v[76:79], 0\n"
"buffer_load_dwordx4 v[188:191], v206, s[20:23], s34 offen offset:1024 sc1\n"
"v_mfma_f32_16x16x32_bf16 v[60:63], a[28:31], v[92:95], v[60:63]\n"
"s_add_u32 s34, s34, 0x800\n"
"s_waitcnt vmcnt(20)\n"
"v_mfma_f32_16x16x32_bf16 v[0:3], a[32:35], v[96:99], v[0:3]\n"
"buffer_load_dwordx4 a[64:67], v198, s[20:23], s34 offen offset:0 sc1\n"
"ds_read_b64_tr_b16 a[0:1], v196 offset:32768\n"
"v_mfma_f32_16x16x32_bf16 v[0:3], a[48:51], v[112:115], v[0:3]\n"
"ds_read_b64_tr_b16 a[2:3], v196 offset:34816\n"
"v_mfma_f32_16x16x32_bf16 v[4:7], a[36:39], v[96:99], v[4:7]\n"
"buffer_load_dwordx4 a[80:83], v198, s[20:23], s34 offen offset:1024 sc1\n"
"v_mfma_f32_16x16x32_bf16 v[4:7], a[52:55], v[112:115], v[4:7]\n"
"ds_read_b64_tr_b16 a[4:5], v197 offset:32768\n"
"v_mfma_f32_16x16x32_bf16 v[8:11], a[40:43], v[96:99], v[8:11]\n"
"buffer_load_dwordx4 a[68:71], v199, s[20:23], s34 offen offset:0 sc1\n"
"ds_read_b64_tr_b16 a[6:7], v197 offset:34816\n"
"v_mfma_f32_16x16x32_bf16 v[8:11], a[56:59], v[112:115], v[8:11]\n"
"v_mfma_f32_16x16x32_bf16 v[12:15], a[44:47], v[96:99], v[12:15]\n"
"buffer_load_dwordx4 a[84:87], v199, s[20:23], s34 offen offset:1024 sc1\n"
"ds_read_b64_tr_b16 a[8:9], v196 offset:33280\n"
"v_mfma_f32_16x16x32_bf16 v[12:15], a[60:63], v[112:115], v[12:15]\n"
"ds_read_b64_tr_b16 a[10:11], v196 offset:35328\n"
"v_mfma_f32_16x16x32_bf16 v[16:19], a[32:35], v[100:103], v[16:19]\n"
"buffer_load_dwordx4 a[72:75], v205, s[20:23], s34 offen offset:0 sc1\n"
"v_mfma_f32_16x16x32_bf16 v[16:19], a[48:51], v[116:119], v[16:19]\n"
"ds_read_b64_tr_b16 a[12:13], v197 offset:33280\n"
"v_mfma_f32_16x16x32_bf16 v[20:23], a[36:39], v[100:103], v[20:23]\n"
"buffer_load_dwordx4 a[88:91], v205, s[20:23], s34 offen offset:1024 sc1\n"
"ds_read_b64_tr_b16 a[14:15], v197 offset:35328\n"
"v_mfma_f32_16x16x32_bf16 v[20:23], a[52:55], v[116:119], v[20:23]\n"
"v_mfma_f32_16x16x32_bf16 v[24:27], a[40:43], v[100:103], v[24:27]\n"
"buffer_load_dwordx4 a[76:79], v206, s[20:23], s34 offen offset:0 sc1\n"
"ds_read_b64_tr_b16 a[16:17], v196 offset:36864\n"
"v_mfma_f32_16x16x32_bf16 v[24:27], a[56:59], v[116:119], v[24:27]\n"
"ds_read_b64_tr_b16 a[18:19], v196 offset:38912\n"
"v_mfma_f32_16x16x32_bf16 v[28:31], a[44:47], v[100:103], v[28:31]\n"
"buffer_load_dwordx4 a[92:95], v206, s[20:23], s34 offen offset:1024 sc1\n"
"v_mfma_f32_16x16x32_bf16 v[28:31], a[60:63], v[116:119], v[28:31]\n"
"s_add_u32 s34, s34, 0x800\n"
"ds_read_b64_tr_b16 a[20:21], v197 offset:36864\n"
"s_waitcnt vmcnt(24)\n"
"v_mfma_f32_16x16x32_bf16 v[32:35], a[32:35], v[104:107], v[32:35]\n"
"buffer_load_dwordx4 a[96:99], v198, s[20:23], s34 offen offset:0 sc1\n"
"ds_read_b64_tr_b16 a[22:23], v197 offset:38912\n"
"v_mfma_f32_16x16x32_bf16 v[32:35], a[48:51], v[120:123], v[32:35]\n"
"v_mfma_f32_16x16x32_bf16 v[36:39], a[36:39], v[104:107], v[36:39]\n"
"buffer_load_dwordx4 a[112:115], v198, s[20:23], s34 offen offset:1024 sc1\n"
"ds_read_b64_tr_b16 a[24:25], v196 offset:37376\n"
"v_mfma_f32_16x16x32_bf16 v[36:39], a[52:55], v[120:123], v[36:39]\n"
"ds_read_b64_tr_b16 a[26:27], v196 offset:39424\n"
"v_mfma_f32_16x16x32_bf16 v[40:43], a[40:43], v[104:107], v[40:43]\n"
"buffer_load_dwordx4 a[100:103], v199, s[20:23], s34 offen offset:0 sc1\n"
"v_mfma_f32_16x16x32_bf16 v[40:43], a[56:59], v[120:123], v[40:43]\n"
"ds_read_b64_tr_b16 a[28:29], v197 offset:37376\n"
"v_mfma_f32_16x16x32_bf16 v[44:47], a[44:47], v[104:107], v[44:47]\n"
"buffer_load_dwordx4 a[116:119], v199, s[20:23], s34 offen offset:1024 sc1\n"
"ds_read_b64_tr_b16 a[30:31], v197 offset:39424\n"
"v_mfma_f32_16x16x32_bf16 v[44:47], a[60:63], v[120:123], v[44:47]\n"
"v_mfma_f32_16x16x32_bf16 v[48:51], a[32:35], v[108:111], v[48:51]\n"
"buffer_load_dwordx4 a[104:107], v205, s[20:23], s34 offen offset:0 sc1\n"
"v_mfma_f32_16x16x32_bf16 v[48:51], a[48:51], v[124:127], v[48:51]\n"
"v_mfma_f32_16x16x32_bf16 v[52:55], a[36:39], v[108:111], v[52:55]\n"
"buffer_load_dwordx4 a[120:123], v205, s[20:23], s34 offen offset:1024 sc1\n"
"v_mfma_f32_16x16x32_bf16 v[52:55], a[52:55], v[124:127], v[52:55]\n"
"v_mfma_f32_16x16x32_bf16 v[56:59], a[40:43], v[108:111], v[56:59]\n"
"buffer_load_dwordx4 a[108:111], v206, s[20:23], s34 offen offset:0 sc1\n"
"v_mfma_f32_16x16x32_bf16 v[56:59], a[56:59], v[124:127], v[56:59]\n"
"v_mfma_f32_16x16x32_bf16 v[60:63], a[44:47], v[108:111], v[60:63]\n"
"buffer_load_dwordx4 a[124:127], v206, s[20:23], s34 offen offset:1024 sc1\n"
"v_mfma_f32_16x16x32_bf16 v[60:63], a[60:63], v[124:127], v[60:63]\n"
"s_add_u32 s34, s34, 0x800\n"
"s_waitcnt vmcnt(28)\n"
"s_waitcnt lgkmcnt(0)\n"
"v_mfma_f32_16x16x32_bf16 v[0:3], a[0:3], v[128:131], v[0:3]\n"
"buffer_load_dwordx4 a[128:131], v198, s[20:23], s34 offen offset:0 sc1\n"
"ds_read_b64_tr_b16 a[32:33], v218 offset:0\n"
"v_mfma_f32_16x16x32_bf16 v[0:3], a[16:19], v[144:147], v[0:3]\n"
"ds_read_b64_tr_b16 a[34:35], v218 offset:2048\n"
"v_mfma_f32_16x16x32_bf16 v[4:7], a[4:7], v[128:131], v[4:7]\n"
"buffer_load_dwordx4 a[144:147], v198, s[20:23], s34 offen offset:1024 sc1\n"
"v_mfma_f32_16x16x32_bf16 v[4:7], a[20:23], v[144:147], v[4:7]\n"
"ds_read_b64_tr_b16 a[36:37], v219 offset:0\n"
"v_mfma_f32_16x16x32_bf16 v[8:11], a[8:11], v[128:131], v[8:11]\n"
"buffer_load_dwordx4 a[132:135], v199, s[20:23], s34 offen offset:0 sc1\n"
"ds_read_b64_tr_b16 a[38:39], v219 offset:2048\n"
"v_mfma_f32_16x16x32_bf16 v[8:11], a[24:27], v[144:147], v[8:11]\n"
"v_mfma_f32_16x16x32_bf16 v[12:15], a[12:15], v[128:131], v[12:15]\n"
"buffer_load_dwordx4 a[148:151], v199, s[20:23], s34 offen offset:1024 sc1\n"
"ds_read_b64_tr_b16 a[40:41], v218 offset:512\n"
"v_mfma_f32_16x16x32_bf16 v[12:15], a[28:31], v[144:147], v[12:15]\n"
"ds_read_b64_tr_b16 a[42:43], v218 offset:2560\n"
"v_mfma_f32_16x16x32_bf16 v[16:19], a[0:3], v[132:135], v[16:19]\n"
"buffer_load_dwordx4 a[136:139], v205, s[20:23], s34 offen offset:0 sc1\n"
"v_mfma_f32_16x16x32_bf16 v[16:19], a[16:19], v[148:151], v[16:19]\n"
"ds_read_b64_tr_b16 a[44:45], v219 offset:512\n"
"v_mfma_f32_16x16x32_bf16 v[20:23], a[4:7], v[132:135], v[20:23]\n"
"buffer_load_dwordx4 a[152:155], v205, s[20:23], s34 offen offset:1024 sc1\n"
"ds_read_b64_tr_b16 a[46:47], v219 offset:2560\n"
"v_mfma_f32_16x16x32_bf16 v[20:23], a[20:23], v[148:151], v[20:23]\n"
"v_mfma_f32_16x16x32_bf16 v[24:27], a[8:11], v[132:135], v[24:27]\n"
"buffer_load_dwordx4 a[140:143], v206, s[20:23], s34 offen offset:0 sc1\n"
"ds_read_b64_tr_b16 a[48:49], v218 offset:4096\n"
"v_mfma_f32_16x16x32_bf16 v[24:27], a[24:27], v[148:151], v[24:27]\n"
"ds_read_b64_tr_b16 a[50:51], v218 offset:6144\n"
"v_mfma_f32_16x16x32_bf16 v[28:31], a[12:15], v[132:135], v[28:31]\n"
"buffer_load_dwordx4 a[156:159], v206, s[20:23], s34 offen offset:1024 sc1\n"
"v_mfma_f32_16x16x32_bf16 v[28:31], a[28:31], v[148:151], v[28:31]\n"
"s_add_u32 s34, s34, 0x800\n"
"ds_read_b64_tr_b16 a[52:53], v219 offset:4096\n"
"s_waitcnt vmcnt(32)\n"
"v_mfma_f32_16x16x32_bf16 v[32:35], a[0:3], v[136:139], v[32:35]\n"
"ds_read_b64_tr_b16 a[54:55], v219 offset:6144\n"
"v_mfma_f32_16x16x32_bf16 v[32:35], a[16:19], v[152:155], v[32:35]\n"
"v_mfma_f32_16x16x32_bf16 v[36:39], a[4:7], v[136:139], v[36:39]\n"
"ds_read_b64_tr_b16 a[56:57], v218 offset:4608\n"
"v_mfma_f32_16x16x32_bf16 v[36:39], a[20:23], v[152:155], v[36:39]\n"
"ds_read_b64_tr_b16 a[58:59], v218 offset:6656\n"
"v_mfma_f32_16x16x32_bf16 v[40:43], a[8:11], v[136:139], v[40:43]\n"
"v_mfma_f32_16x16x32_bf16 v[40:43], a[24:27], v[152:155], v[40:43]\n"
"ds_read_b64_tr_b16 a[60:61], v219 offset:4608\n"
"v_mfma_f32_16x16x32_bf16 v[44:47], a[12:15], v[136:139], v[44:47]\n"
"ds_read_b64_tr_b16 a[62:63], v219 offset:6656\n"
"v_mfma_f32_16x16x32_bf16 v[44:47], a[28:31], v[152:155], v[44:47]\n"
"v_mfma_f32_16x16x32_bf16 v[48:51], a[0:3], v[140:143], v[48:51]\n"
"v_mfma_f32_16x16x32_bf16 v[48:51], a[16:19], v[156:159], v[48:51]\n"
"v_mfma_f32_16x16x32_bf16 v[52:55], a[4:7], v[140:143], v[52:55]\n"
"v_mfma_f32_16x16x32_bf16 v[52:55], a[20:23], v[156:159], v[52:55]\n"
"v_mfma_f32_16x16x32_bf16 v[56:59], a[8:11], v[140:143], v[56:59]\n"
"v_mfma_f32_16x16x32_bf16 v[56:59], a[24:27], v[156:159], v[56:59]\n"
"v_mfma_f32_16x16x32_bf16 v[60:63], a[12:15], v[140:143], v[60:63]\n"
"v_mfma_f32_16x16x32_bf16 v[60:63], a[28:31], v[156:159], v[60:63]\n"
"s_waitcnt vmcnt(28)\n"
"s_waitcnt lgkmcnt(0)\n"
"v_mfma_f32_16x16x32_bf16 v[0:3], a[32:35], v[160:163], v[0:3]\n"
"ds_read_b64_tr_b16 a[0:1], v218 offset:32768\n"
"v_mfma_f32_16x16x32_bf16 v[0:3], a[48:51], v[176:179], v[0:3]\n"
"ds_read_b64_tr_b16 a[2:3], v218 offset:34816\n"
"v_mfma_f32_16x16x32_bf16 v[4:7], a[36:39], v[160:163], v[4:7]\n"
"v_mfma_f32_16x16x32_bf16 v[4:7], a[52:55], v[176:179], v[4:7]\n"
"ds_read_b64_tr_b16 a[4:5], v219 offset:32768\n"
"v_mfma_f32_16x16x32_bf16 v[8:11], a[40:43], v[160:163], v[8:11]\n"
"ds_read_b64_tr_b16 a[6:7], v219 offset:34816\n"
"v_mfma_f32_16x16x32_bf16 v[8:11], a[56:59], v[176:179], v[8:11]\n"
"v_mfma_f32_16x16x32_bf16 v[12:15], a[44:47], v[160:163], v[12:15]\n"
"ds_read_b64_tr_b16 a[8:9], v218 offset:33280\n"
"v_mfma_f32_16x16x32_bf16 v[12:15], a[60:63], v[176:179], v[12:15]\n"
"ds_read_b64_tr_b16 a[10:11], v218 offset:35328\n"
"v_mfma_f32_16x16x32_bf16 v[16:19], a[32:35], v[164:167], v[16:19]\n"
"v_mfma_f32_16x16x32_bf16 v[16:19], a[48:51], v[180:183], v[16:19]\n"
"ds_read_b64_tr_b16 a[12:13], v219 offset:33280\n"
"v_mfma_f32_16x16x32_bf16 v[20:23], a[36:39], v[164:167], v[20:23]\n"
"ds_read_b64_tr_b16 a[14:15], v219 offset:35328\n"
"v_mfma_f32_16x16x32_bf16 v[20:23], a[52:55], v[180:183], v[20:23]\n"
"v_mfma_f32_16x16x32_bf16 v[24:27], a[40:43], v[164:167], v[24:27]\n"
"ds_read_b64_tr_b16 a[16:17], v218 offset:36864\n"
"v_mfma_f32_16x16x32_bf16 v[24:27], a[56:59], v[180:183], v[24:27]\n"
"ds_read_b64_tr_b16 a[18:19], v218 offset:38912\n"
"v_mfma_f32_16x16x32_bf16 v[28:31], a[44:47], v[164:167], v[28:31]\n"
"v_mfma_f32_16x16x32_bf16 v[28:31], a[60:63], v[180:183], v[28:31]\n"
"ds_read_b64_tr_b16 a[20:21], v219 offset:36864\n"
"s_waitcnt vmcnt(24)\n"
"v_mfma_f32_16x16x32_bf16 v[32:35], a[32:35], v[168:171], v[32:35]\n"
"ds_read_b64_tr_b16 a[22:23], v219 offset:38912\n"
"v_mfma_f32_16x16x32_bf16 v[32:35], a[48:51], v[184:187], v[32:35]\n"
"v_mfma_f32_16x16x32_bf16 v[36:39], a[36:39], v[168:171], v[36:39]\n"
"ds_read_b64_tr_b16 a[24:25], v218 offset:37376\n"
"v_mfma_f32_16x16x32_bf16 v[36:39], a[52:55], v[184:187], v[36:39]\n"
"ds_read_b64_tr_b16 a[26:27], v218 offset:39424\n"
"v_mfma_f32_16x16x32_bf16 v[40:43], a[40:43], v[168:171], v[40:43]\n"
"v_mfma_f32_16x16x32_bf16 v[40:43], a[56:59], v[184:187], v[40:43]\n"
"ds_read_b64_tr_b16 a[28:29], v219 offset:37376\n"
"v_mfma_f32_16x16x32_bf16 v[44:47], a[44:47], v[168:171], v[44:47]\n"
"ds_read_b64_tr_b16 a[30:31], v219 offset:39424\n"
"v_mfma_f32_16x16x32_bf16 v[44:47], a[60:63], v[184:187], v[44:47]\n"
"v_mfma_f32_16x16x32_bf16 v[48:51], a[32:35], v[172:175], v[48:51]\n"
"v_mfma_f32_16x16x32_bf16 v[48:51], a[48:51], v[188:191], v[48:51]\n"
"v_mfma_f32_16x16x32_bf16 v[52:55], a[36:39], v[172:175], v[52:55]\n"
"v_mfma_f32_16x16x32_bf16 v[52:55], a[52:55], v[188:191], v[52:55]\n"
"v_mfma_f32_16x16x32_bf16 v[56:59], a[40:43], v[172:175], v[56:59]\n"
"v_mfma_f32_16x16x32_bf16 v[56:59], a[56:59], v[188:191], v[56:59]\n"
"v_mfma_f32_16x16x32_bf16 v[60:63], a[44:47], v[172:175], v[60:63]\n"
"v_mfma_f32_16x16x32_bf16 v[60:63], a[60:63], v[188:191], v[60:63]\n"
"s_waitcnt vmcnt(20)\n"
"s_waitcnt lgkmcnt(0)\n"
"v_mfma_f32_16x16x32_bf16 v[0:3], a[0:3], a[64:67], v[0:3]\n"
"s_add_u32 s40, s8, 0\n"
"s_addc_u32 s41, s9, 0\n"
"s_and_b32 s41, s41, 0xffff\n"
"s_mov_b32 s42, 0x200000\n"
"s_mov_b32 s43, 0x20000\n"
"ds_read_b64_tr_b16 a[32:33], v220 offset:0\n"
"v_mfma_f32_16x16x32_bf16 v[0:3], a[16:19], a[80:83], v[0:3]\n"
"ds_read_b64_tr_b16 a[34:35], v220 offset:2048\n"
"v_mfma_f32_16x16x32_bf16 v[4:7], a[4:7], a[64:67], v[4:7]\n"
"buffer_load_dwordx2 v[210:211], v208, s[40:43], 0 offen offset:0 sc1\n"
"v_mfma_f32_16x16x32_bf16 v[4:7], a[20:23], a[80:83], v[4:7]\n"
"buffer_load_dwordx2 v[212:213], v208, s[40:43], 0 offen offset:256 sc1\n"
"ds_read_b64_tr_b16 a[36:37], v221 offset:0\n"
"v_mfma_f32_16x16x32_bf16 v[8:11], a[8:11], a[64:67], v[8:11]\n"
"buffer_load_dwordx2 v[214:215], v209, s[40:43], 0 offen offset:0 sc1\n"
"ds_read_b64_tr_b16 a[38:39], v221 offset:2048\n"
"v_mfma_f32_16x16x32_bf16 v[8:11], a[24:27], a[80:83], v[8:11]\n"
"buffer_load_dwordx2 v[216:217], v209, s[40:43], 0 offen offset:256 sc1\n"
"v_mfma_f32_16x16x32_bf16 v[12:15], a[12:15], a[64:67], v[12:15]\n"
"ds_read_b64_tr_b16 a[40:41], v220 offset:512\n"
"v_mfma_f32_16x16x32_bf16 v[12:15], a[28:31], a[80:83], v[12:15]\n"
"ds_read_b64_tr_b16 a[42:43], v220 offset:2560\n"
"v_mfma_f32_16x16x32_bf16 v[16:19], a[0:3], a[68:71], v[16:19]\n"
"v_mfma_f32_16x16x32_bf16 v[16:19], a[16:19], a[84:87], v[16:19]\n"
"ds_read_b64_tr_b16 a[44:45], v221 offset:512\n"
"v_mfma_f32_16x16x32_bf16 v[20:23], a[4:7], a[68:71], v[20:23]\n"
"ds_read_b64_tr_b16 a[46:47], v221 offset:2560\n"
"v_mfma_f32_16x16x32_bf16 v[20:23], a[20:23], a[84:87], v[20:23]\n"
"v_mfma_f32_16x16x32_bf16 v[24:27], a[8:11], a[68:71], v[24:27]\n"
"ds_read_b64_tr_b16 a[48:49], v220 offset:4096\n"
"v_mfma_f32_16x16x32_bf16 v[24:27], a[24:27], a[84:87], v[24:27]\n"
"ds_read_b64_tr_b16 a[50:51], v220 offset:6144\n"
"v_mfma_f32_16x16x32_bf16 v[28:31], a[12:15], a[68:71], v[28:31]\n"
"v_mfma_f32_16x16x32_bf16 v[28:31], a[28:31], a[84:87], v[28:31]\n"
"ds_read_b64_tr_b16 a[52:53], v221 offset:4096\n"
"s_waitcnt vmcnt(20)\n"
"v_mfma_f32_16x16x32_bf16 v[32:35], a[0:3], a[72:75], v[32:35]\n"
"ds_read_b64_tr_b16 a[54:55], v221 offset:6144\n"
"v_mfma_f32_16x16x32_bf16 v[32:35], a[16:19], a[88:91], v[32:35]\n"
"v_mfma_f32_16x16x32_bf16 v[36:39], a[4:7], a[72:75], v[36:39]\n"
"ds_read_b64_tr_b16 a[56:57], v220 offset:4608\n"
"v_mfma_f32_16x16x32_bf16 v[36:39], a[20:23], a[88:91], v[36:39]\n"
"ds_read_b64_tr_b16 a[58:59], v220 offset:6656\n"
"v_mfma_f32_16x16x32_bf16 v[40:43], a[8:11], a[72:75], v[40:43]\n"
"v_mfma_f32_16x16x32_bf16 v[40:43], a[24:27], a[88:91], v[40:43]\n"
"ds_read_b64_tr_b16 a[60:61], v221 offset:4608\n"
"v_mfma_f32_16x16x32_bf16 v[44:47], a[12:15], a[72:75], v[44:47]\n"
"ds_read_b64_tr_b16 a[62:63], v221 offset:6656\n"
"v_mfma_f32_16x16x32_bf16 v[44:47], a[28:31], a[88:91], v[44:47]\n"
"v_mfma_f32_16x16x32_bf16 v[48:51], a[0:3], a[76:79], v[48:51]\n"
"v_mfma_f32_16x16x32_bf16 v[48:51], a[16:19], a[92:95], v[48:51]\n"
"v_mfma_f32_16x16x32_bf16 v[52:55], a[4:7], a[76:79], v[52:55]\n"
"v_mfma_f32_16x16x32_bf16 v[52:55], a[20:23], a[92:95], v[52:55]\n"
"v_mfma_f32_16x16x32_bf16 v[56:59], a[8:11], a[76:79], v[56:59]\n"
"v_mfma_f32_16x16x32_bf16 v[56:59], a[24:27], a[92:95], v[56:59]\n"
"v_mfma_f32_16x16x32_bf16 v[60:63], a[12:15], a[76:79], v[60:63]\n"
"v_mfma_f32_16x16x32_bf16 v[60:63], a[28:31], a[92:95], v[60:63]\n"
"s_waitcnt vmcnt(16)\n"
"s_waitcnt lgkmcnt(0)\n"
"v_mfma_f32_16x16x32_bf16 v[0:3], a[32:35], a[96:99], v[0:3]\n"
"ds_read_b64_tr_b16 a[0:1], v196 offset:0\n"
"v_mfma_f32_16x16x32_bf16 v[0:3], a[48:51], a[112:115], v[0:3]\n"
"ds_read_b64_tr_b16 a[2:3], v196 offset:2048\n"
"v_mfma_f32_16x16x32_bf16 v[4:7], a[36:39], a[96:99], v[4:7]\n"
"v_mfma_f32_16x16x32_bf16 v[4:7], a[52:55], a[112:115], v[4:7]\n"
"ds_read_b64_tr_b16 a[4:5], v197 offset:0\n"
"v_mfma_f32_16x16x32_bf16 v[8:11], a[40:43], a[96:99], v[8:11]\n"
"ds_read_b64_tr_b16 a[6:7], v197 offset:2048\n"
"v_mfma_f32_16x16x32_bf16 v[8:11], a[56:59], a[112:115], v[8:11]\n"
"v_mfma_f32_16x16x32_bf16 v[12:15], a[44:47], a[96:99], v[12:15]\n"
"ds_read_b64_tr_b16 a[8:9], v196 offset:512\n"
"v_mfma_f32_16x16x32_bf16 v[12:15], a[60:63], a[112:115], v[12:15]\n"
"ds_read_b64_tr_b16 a[10:11], v196 offset:2560\n"
"v_mfma_f32_16x16x32_bf16 v[16:19], a[32:35], a[100:103], v[16:19]\n"
"v_mfma_f32_16x16x32_bf16 v[16:19], a[48:51], a[116:119], v[16:19]\n"
"ds_read_b64_tr_b16 a[12:13], v197 offset:512\n"
"v_mfma_f32_16x16x32_bf16 v[20:23], a[36:39], a[100:103], v[20:23]\n"
"ds_read_b64_tr_b16 a[14:15], v197 offset:2560\n"
"v_mfma_f32_16x16x32_bf16 v[20:23], a[52:55], a[116:119], v[20:23]\n"
"v_mfma_f32_16x16x32_bf16 v[24:27], a[40:43], a[100:103], v[24:27]\n"
"ds_read_b64_tr_b16 a[16:17], v196 offset:4096\n"
"v_mfma_f32_16x16x32_bf16 v[24:27], a[56:59], a[116:119], v[24:27]\n"
"ds_read_b64_tr_b16 a[18:19], v196 offset:6144\n"
"v_mfma_f32_16x16x32_bf16 v[28:31], a[44:47], a[100:103], v[28:31]\n"
"v_mfma_f32_16x16x32_bf16 v[28:31], a[60:63], a[116:119], v[28:31]\n"
"ds_read_b64_tr_b16 a[20:21], v197 offset:4096\n"
"s_waitcnt vmcnt(12)\n"
"v_mfma_f32_16x16x32_bf16 v[32:35], a[32:35], a[104:107], v[32:35]\n"
"ds_read_b64_tr_b16 a[22:23], v197 offset:6144\n"
"v_mfma_f32_16x16x32_bf16 v[32:35], a[48:51], a[120:123], v[32:35]\n"
"v_mfma_f32_16x16x32_bf16 v[36:39], a[36:39], a[104:107], v[36:39]\n"
"ds_read_b64_tr_b16 a[24:25], v196 offset:4608\n"
"v_mfma_f32_16x16x32_bf16 v[36:39], a[52:55], a[120:123], v[36:39]\n"
"ds_read_b64_tr_b16 a[26:27], v196 offset:6656\n"
"v_mfma_f32_16x16x32_bf16 v[40:43], a[40:43], a[104:107], v[40:43]\n"
"v_mfma_f32_16x16x32_bf16 v[40:43], a[56:59], a[120:123], v[40:43]\n"
"ds_read_b64_tr_b16 a[28:29], v197 offset:4608\n"
"v_mfma_f32_16x16x32_bf16 v[44:47], a[44:47], a[104:107], v[44:47]\n"
"ds_read_b64_tr_b16 a[30:31], v197 offset:6656\n"
"v_mfma_f32_16x16x32_bf16 v[44:47], a[60:63], a[120:123], v[44:47]\n"
"v_mfma_f32_16x16x32_bf16 v[48:51], a[32:35], a[108:111], v[48:51]\n"
"v_mfma_f32_16x16x32_bf16 v[48:51], a[48:51], a[124:127], v[48:51]\n"
"v_mfma_f32_16x16x32_bf16 v[52:55], a[36:39], a[108:111], v[52:55]\n"
"v_mfma_f32_16x16x32_bf16 v[52:55], a[52:55], a[124:127], v[52:55]\n"
"v_mfma_f32_16x16x32_bf16 v[56:59], a[40:43], a[108:111], v[56:59]\n"
"v_mfma_f32_16x16x32_bf16 v[56:59], a[56:59], a[124:127], v[56:59]\n"
"v_mfma_f32_16x16x32_bf16 v[60:63], a[44:47], a[108:111], v[60:63]\n"
"v_mfma_f32_16x16x32_bf16 v[60:63], a[60:63], a[124:127], v[60:63]\n"
"s_waitcnt vmcnt(8)\n"
"s_waitcnt lgkmcnt(0)\n"
"v_mfma_f32_16x16x32_bf16 v[0:3], a[0:3], a[128:131], v[0:3]\n"
"v_mfma_f32_16x16x32_bf16 v[0:3], a[16:19], a[144:147], v[0:3]\n"
"v_mfma_f32_16x16x32_bf16 v[4:7], a[4:7], a[128:131], v[4:7]\n"
"v_mfma_f32_16x16x32_bf16 v[4:7], a[20:23], a[144:147], v[4:7]\n"
"v_mfma_f32_16x16x32_bf16 v[8:11], a[8:11], a[128:131], v[8:11]\n"
"v_mfma_f32_16x16x32_bf16 v[8:11], a[24:27], a[144:147], v[8:11]\n"
"v_mfma_f32_16x16x32_bf16 v[12:15], a[12:15], a[128:131], v[12:15]\n"
"v_mfma_f32_16x16x32_bf16 v[12:15], a[28:31], a[144:147], v[12:15]\n"
"v_mfma_f32_16x16x32_bf16 v[16:19], a[0:3], a[132:135], v[16:19]\n"
"v_mfma_f32_16x16x32_bf16 v[16:19], a[16:19], a[148:151], v[16:19]\n"
"v_mfma_f32_16x16x32_bf16 v[20:23], a[4:7], a[132:135], v[20:23]\n"
"v_mfma_f32_16x16x32_bf16 v[20:23], a[20:23], a[148:151], v[20:23]\n"
"v_mfma_f32_16x16x32_bf16 v[24:27], a[8:11], a[132:135], v[24:27]\n"
"v_mfma_f32_16x16x32_bf16 v[24:27], a[24:27], a[148:151], v[24:27]\n"
"v_mfma_f32_16x16x32_bf16 v[28:31], a[12:15], a[132:135], v[28:31]\n"
"v_mfma_f32_16x16x32_bf16 v[28:31], a[28:31], a[148:151], v[28:31]\n"
"s_waitcnt vmcnt(4)\n"
"v_mfma_f32_16x16x32_bf16 v[32:35], a[0:3], a[136:139], v[32:35]\n"
"v_mfma_f32_16x16x32_bf16 v[32:35], a[16:19], a[152:155], v[32:35]\n"
"v_mfma_f32_16x16x32_bf16 v[36:39], a[4:7], a[136:139], v[36:39]\n"
"v_mfma_f32_16x16x32_bf16 v[36:39], a[20:23], a[152:155], v[36:39]\n"
"v_mfma_f32_16x16x32_bf16 v[40:43], a[8:11], a[136:139], v[40:43]\n"
"v_mfma_f32_16x16x32_bf16 v[40:43], a[24:27], a[152:155], v[40:43]\n"
"v_mfma_f32_16x16x32_bf16 v[44:47], a[12:15], a[136:139], v[44:47]\n"
"v_mfma_f32_16x16x32_bf16 v[44:47], a[28:31], a[152:155], v[44:47]\n"
"v_mfma_f32_16x16x32_bf16 v[48:51], a[0:3], a[140:143], v[48:51]\n"
"v_mfma_f32_16x16x32_bf16 v[48:51], a[16:19], a[156:159], v[48:51]\n"
"v_mfma_f32_16x16x32_bf16 v[52:55], a[4:7], a[140:143], v[52:55]\n"
"v_mfma_f32_16x16x32_bf16 v[52:55], a[20:23], a[156:159], v[52:55]\n"
"v_mfma_f32_16x16x32_bf16 v[56:59], a[8:11], a[140:143], v[56:59]\n"
"v_mfma_f32_16x16x32_bf16 v[56:59], a[24:27], a[156:159], v[56:59]\n"
"v_mfma_f32_16x16x32_bf16 v[60:63], a[12:15], a[140:143], v[60:63]\n"
"v_mfma_f32_16x16x32_bf16 v[60:63], a[28:31], a[156:159], v[60:63]\n"
"s_waitcnt vmcnt(0) lgkmcnt(0)\n"
"s_nop 15\n"
    : "={v[0:15]}"(a00), "={v[16:31]}"(a01), "={v[32:47]}"(a10), "={v[48:63]}"(a11), "={v[210:213]}"(xr0), "={v[214:217]}"(xr1)
    : "{s[4:5]}"(act), "{s[6:7]}"(wd), "{s[8:9]}"(x1p), "{s[12:13]}"(c), "{s16}"(bid), "{s17}"(w), "{s18}"(ldsb), "{v208}"(xo0), "{v209}"(xo1)
    : "v64","v65","v66","v67","v68","v69","v70","v71","v72","v73","v74","v75","v76","v77","v78","v79","v80","v81","v82","v83","v84","v85","v86","v87",
"v88","v89","v90","v91","v92","v93","v94","v95","v96","v97","v98","v99","v100","v101","v102","v103","v104","v105","v106","v107","v108","v109","v110","v111",
"v112","v113","v114","v115","v116","v117","v118","v119","v120","v121","v122","v123","v124","v125","v126","v127","v128","v129","v130","v131","v132","v133","v134","v135",
"v136","v137","v138","v139","v140","v141","v142","v143","v144","v145","v146","v147","v148","v149","v150","v151","v152","v153","v154","v155","v156","v157","v158","v159",
"v160","v161","v162","v163","v164","v165","v166","v167","v168","v169","v170","v171","v172","v173","v174","v175","v176","v177","v178","v179","v180","v181","v182","v183",
"v184","v185","v186","v187","v188","v189","v190","v191","v192","v193","v194","v195","v196","v197","v198","v199","v200","v201","v202","v203","v204","v205","v206","v207",
"v218","v219","v220","v221","a0","a1","a2","a3","a4","a5","a6","a7","a8","a9","a10","a11","a12","a13","a14","a15","a16","a17","a18","a19",
"a20","a21","a22","a23","a24","a25","a26","a27","a28","a29","a30","a31","a32","a33","a34","a35","a36","a37","a38","a39","a40","a41","a42","a43",
"a44","a45","a46","a47","a48","a49","a50","a51","a52","a53","a54","a55","a56","a57","a58","a59","a60","a61","a62","a63","a64","a65","a66","a67",
"a68","a69","a70","a71","a72","a73","a74","a75","a76","a77","a78","a79","a80","a81","a82","a83","a84","a85","a86","a87","a88","a89","a90","a91",
"a92","a93","a94","a95","a96","a97","a98","a99","a100","a101","a102","a103","a104","a105","a106","a107","a108","a109","a110","a111","a112","a113","a114","a115",
"a116","a117","a118","a119","a120","a121","a122","a123","a124","a125","a126","a127","a128","a129","a130","a131","a132","a133","a134","a135","a136","a137","a138","a139",
"a140","a141","a142","a143","a144","a145","a146","a147","a148","a149","a150","a151","a152","a153","a154","a155","a156","a157","a158","a159","s19","s20","s21","s22",
"s23","s24","s25","s26","s27","s28","s29","s30","s31","s34","s35","s36","s37","s38","s39","s40","s41","s42","s43","s44","s45","s46","s47","s48",
"s49","s50","s51","vcc","scc","memory");
    u32x2 xv[2][2] = {{{xr0[0],xr0[1]},{xr0[2],xr0[3]}},{{xr1[0],xr1[1]},{xr1[2],xr1[3]}}};
    asm volatile("" ::: "memory"); __builtin_amdgcn_s_barrier(); asm volatile("" ::: "memory");
    red_write(lds,w,lane,a00,0); red_write(lds,w,lane,a01,1); red_write(lds,w,lane,a10,2); red_write(lds,w,lane,a11,3);
    asm volatile("s_waitcnt lgkmcnt(0)" ::: "memory"); __builtin_amdgcn_s_barrier(); asm volatile("" ::: "memory");
#pragma unroll
    for(int r=0;r<2;++r){
      const int q = (tid>>3) + 32*r;
      u32x4 o;
#pragma unroll
      for(int a=0;a<2;++a){
        const f32x4 y = red_read(lds, q, c8 + 4*a);
        o[2*a]   = pk2(blo(xv[r][a][0]) + gv[a][0]*y[0], bhi(xv[r][a][0]) + gv[a][1]*y[1]);
        o[2*a+1] = pk2(blo(xv[r][a][1]) + gv[a][2]*y[2], bhi(xv[r][a][1]) + gv[a][3]*y[3]);
      }
      *(u32x4*)(out + (size_t)(rt*64+q)*512 + n) = o;
    }
  }
  asm volatile(".Lkm_end:\n" ::: "memory");
}

extern "C" void kernel_launch(void* const* d_in, const int* in_sizes, int n_in, void* d_out, int out_size, void* d_ws, size_t ws_size, hipStream_t stream){
  const u16* x=(const u16*)d_in[0]; const u16* kc=(const u16*)d_in[1]; const u16* vc=(const u16*)d_in[2];
  const u16* wq=(const u16*)d_in[3]; const u16* wo=(const u16*)d_in[4];
  const u16* wg=(const u16*)d_in[6]; const u16* wu=(const u16*)d_in[7]; const u16* wd=(const u16*)d_in[8];
  const u16* c=(const u16*)d_in[9]; const u16* wmod=(const u16*)d_in[10];
  char* ws=(char*)d_ws;
  (void)hipFuncSetAttribute((const void*)k_main, hipFuncAttributeMaxDynamicSharedMemorySize, LDS_MAIN);
  k_prep<<<PREP_GRID,256,0,stream>>>(x,c,wmod,(float*)(ws+WS_TAB),(u16*)(ws+WS_XN),wq,(unsigned*)(ws+WS_CNT),kc,vc);
  k_main<<<256,256,LDS_MAIN,stream>>>(x,kc,vc,wq,wo,wg,wu,wd,ws,(u16*)d_out);
}
```
